```python
import torch

def _softmax(x, axis=-1):
    return torch.softmax(x, dim=axis)
def _gelu(x):
    return torch.nn.functional.gelu(x, approximate="tanh")
def _silu(x):
    return torch.nn.functional.silu(x)
def _sigmoid(x):
    return torch.sigmoid(x)
def _layernorm(x, axis=-1, eps=1e-5):
    m = x.mean(dim=axis, keepdim=True)
    v = x.var(dim=axis, keepdim=True, unbiased=False)
    return (x - m) / torch.sqrt(v + eps)
def _scatter_add(tgt, idx, src, axis=0):
    return tgt.clone().index_add_(axis, idx, src)
def _segment_sum(src, seg, n):
    out = torch.zeros((n,) + src.shape[1:], dtype=src.dtype)
    return out.index_add_(0, seg, src)
def _pad(x, pw):
    flat = tuple(v for pair in reversed(pw) for v in pair)
    return torch.nn.functional.pad(x, flat)

kernel_name = 'concat_heads__mha_block_cached__mha_block_cached__near_ridge'

def setup_inputs():

    _w = (lambda a: (lambda t: t.to(torch.bfloat16) if t.is_floating_point() else t)(torch.as_tensor(a)))
    def _A_setup():
        B, Sq, D = 8, 256, 448
        H, Sk, Dh = 16, 512, 256
        return {
            "x": _w((lambda *s: torch.randn(*s, dtype=torch.bfloat16) * 0.8)(B, Sq, D)),
            "k_cache": _w((lambda *s: torch.randn(*s, dtype=torch.bfloat16) * 0.8)(B, H, Sk, Dh)),
            "v_cache": _w((lambda *s: torch.randn(*s, dtype=torch.bfloat16) * 0.05)(B, H, Sk, Dh)),
            "wq": _w((lambda *s: torch.randn(*s, dtype=torch.bfloat16) * 0.8)(D, H * Dh) * (D ** -0.5)),
            "wo": _w((lambda *s: torch.randn(*s, dtype=torch.bfloat16) * 0.05)(H * Dh, D) * ((H * Dh) ** -0.5)),
        }
    _A_in = _A_setup()

    def _B_setup():
        B, Sq, D = 8, 256, 448
        H, Sk, Dh = 16, 512, 256
        return {
            "x": _w((lambda *s: torch.randn(*s, dtype=torch.bfloat16) * 0.8)(B, Sq, D)),
            "k_cache": _w((lambda *s: torch.randn(*s, dtype=torch.bfloat16) * 0.8)(B, H, Sk, Dh)),
            "v_cache": _w((lambda *s: torch.randn(*s, dtype=torch.bfloat16) * 0.05)(B, H, Sk, Dh)),
            "wq": _w((lambda *s: torch.randn(*s, dtype=torch.bfloat16) * 0.8)(D, H * Dh) * (D ** -0.5)),
            "wo": _w((lambda *s: torch.randn(*s, dtype=torch.bfloat16) * 0.05)(H * Dh, D) * ((H * Dh) ** -0.5)),
        }
    _B_in = _B_setup()

    return {
        "A_x": _A_in["x"],
        "A_k_cache": _A_in["k_cache"],
        "A_v_cache": _A_in["v_cache"],
        "A_wq": _A_in["wq"],
        "A_wo": _A_in["wo"],
        "B_x": _B_in["x"],
        "B_k_cache": _B_in["k_cache"],
        "B_v_cache": _B_in["v_cache"],
        "B_wq": _B_in["wq"],
        "B_wo": _B_in["wo"],
    }

def reference(A_x, A_k_cache, A_v_cache, A_wq, A_wo, B_x, B_k_cache, B_v_cache, B_wq, B_wo):
    def _A_ref(x, k_cache, v_cache, wq, wo):
        B, Sq, D = x.shape
        H, Dh = 16, 256
        q = (lambda x, *d: x.permute(*d))((x @ wq).reshape(B, Sq, H, Dh), 0, 2, 1, 3)
        scores = torch.einsum("bhqd,bhkd->bhqk", q, k_cache) * (Dh ** -0.5)
        p = _softmax(scores, axis=-1)
        o = torch.einsum("bhqk,bhkd->bhqd", p, v_cache)
        return (lambda x, *d: x.permute(*d))(o, 0, 2, 1, 3).reshape(B, Sq, H * Dh) @ wo

    def _B_ref(x, k_cache, v_cache, wq, wo):
        B, Sq, D = x.shape
        H, Dh = 16, 256
        q = (lambda x, *d: x.permute(*d))((x @ wq).reshape(B, Sq, H, Dh), 0, 2, 1, 3)
        scores = torch.einsum("bhqd,bhkd->bhqk", q, k_cache) * (Dh ** -0.5)
        p = _softmax(scores, axis=-1)
        o = torch.einsum("bhqk,bhkd->bhqd", p, v_cache)
        return (lambda x, *d: x.permute(*d))(o, 0, 2, 1, 3).reshape(B, Sq, H * Dh) @ wo

    a = _A_ref(A_x, A_k_cache, A_v_cache, A_wq, A_wo)
    b = _B_ref(B_x, B_k_cache, B_v_cache, B_wq, B_wo)
    return torch.concatenate([a, b], axis=-1)
```

```cpp
#include <hip/hip_runtime.h>
#include <hip/hip_bf16.h>

__global__ __launch_bounds__(256, 1) void mha_block_fused(const void* ax, const void* ak, const void* av, const void* awq, const void* awo,
                                                          const void* bx, const void* bk, const void* bv, const void* bwq, const void* bwo,
                                                          void* out, void* ws) {
  asm volatile(
"s_and_b32 s36, %1, 7\n"
"s_lshr_b32 s37, %1, 3\n"
"s_lshr_b32 s42, s36, 2\n"
"s_mul_i32 s65, s42, 40\n"
"s_load_dwordx8 s[80:87], %0, s65\n"
"s_add_u32 s65, s65, 32\n"
"s_load_dwordx2 s[88:89], %0, s65\n"
"s_load_dwordx4 s[92:95], %0, 80\n"
"s_getpc_b64 s[90:91]\n"
"v_lshlrev_b32 v18, 5, %2\n"
"global_load_dword v200, v18, s[90:91] offset:512\n"
"s_and_b32 s40, s36, 3\n"
"s_lshl_b32 s40, s40, 1\n"
"s_bfe_u32 s65, s37, 0x10001\n"
"s_add_u32 s40, s40, s65\n"
"s_and_b32 s41, s37, 1\n"
"s_lshr_b32 s39, s37, 2\n"
"v_lshrrev_b32 v1, 6, %2\n"
"v_and_b32 v2, 63, %2\n"
"v_readfirstlane_b32 s38, v1\n"
"s_lshr_b32 s36, s38, 1\n"
"s_and_b32 s37, s38, 1\n"
"s_lshl_b32 s43, s42, 3\n"
"s_add_u32 s43, s43, s39\n"
"s_lshl_b32 s43, s43, 1\n"
"s_add_u32 s43, s43, s41\n"
"s_lshl_b32 s96, s40, 1\n"
"s_add_u32 s96, s96, s36\n"
"s_mul_i32 s65, s36, 16896\n"
"s_mul_i32 s44, s37, 8448\n"
"s_add_u32 s44, s44, s65\n"
"s_lshl_b32 s45, s37, 12\n"
"s_lshl_b32 s66, s36, 11\n"
"s_add_u32 s45, s45, s66\n"
"s_add_u32 s45, s45, 135168\n"
"s_lshl_b32 s52, s37, 15\n"
"s_lshl_b32 s66, s96, 9\n"
"s_add_u32 s52, s52, s66\n"
"s_lshl_b32 s53, s39, 8\n"
"s_lshl_b32 s66, s41, 7\n"
"s_add_u32 s53, s53, s66\n"
"s_lshl_b32 s66, s37, 6\n"
"s_add_u32 s53, s53, s66\n"
"s_lshl_b32 s66, s36, 5\n"
"s_add_u32 s53, s53, s66\n"
"s_mul_i32 s53, s53, 896\n"
"s_and_b32 s66, s39, 3\n"
"s_mul_i32 s66, s66, 3\n"
"s_cmp_eq_u32 s39, 7\n"
"s_cselect_b32 s97, 0, s39\n"
"s_lshl_b32 s50, s66, 18\n"
"s_lshl_b32 s51, s66, 6\n"
"s_lshl_b32 s59, s97, 7\n"
"s_lshl_b32 s60, s97, 11\n"
"v_and_b32 v3, 31, v2\n"
"v_lshrrev_b32 v4, 5, v2\n"
"v_lshlrev_b32 v5, 4, v3\n"
"v_lshl_add_u32 v192, v4, 17, v5\n"
"v_and_b32 v6, 15, v2\n"
"v_mul_u32_u24 v7, 896, v6\n"
"v_lshrrev_b32 v8, 4, v2\n"
"v_lshl_add_u32 v193, v8, 4, v7\n"
"s_waitcnt lgkmcnt(0)\n"
"s_lshl_b32 s66, s43, 9\n"
"s_add_u32 s74, s94, s66\n"
"s_addc_u32 s75, s95, 0\n"
"v_and_b32 v11, 7, v2\n"
"s_lshl_b32 s66, s40, 2\n"
"v_lshl_add_u32 v12, v11, 5, s66\n"
"v_mov_b32 v13, s66\n"
"v_cmp_eq_u32 vcc, s40, v11\n"
"v_add_u32 v202, 256, v13\n"
"s_nop 0\n"
"v_cndmask_b32 v12, v12, v202, vcc\n"
"global_load_dword v203, v12, s[74:75] sc1\n"
"s_mov_b32 s8, s80\n"
"s_mov_b32 s9, s81\n"
"s_mov_b32 s10, 1835008\n"
"s_mov_b32 s11, 0x20000\n"
"s_mov_b32 s20, s86\n"
"s_mov_b32 s21, s87\n"
"s_mov_b32 s22, 3670016\n"
"s_mov_b32 s23, 0x20000\n"
"s_add_u32 m0, s45, 0\n"
"s_add_u32 s65, s53, s51\n"
"buffer_load_dwordx4 v193, s[8:11], s65 offen lds\n"
"s_add_u32 m0, s45, 1024\n"
"s_add_u32 s65, s65, 14336\n"
"buffer_load_dwordx4 v193, s[8:11], s65 offen lds\n"
"s_add_u32 s51, s51, 64\n"
"s_cmp_eq_u32 s51, 896\n"
"s_cselect_b32 s51, 0, s51\n"
"s_add_u32 s66, s52, s50\n"
"s_add_u32 m0, s44, 0\n"
"s_add_u32 s65, s66, 0\n"
"buffer_load_dwordx4 v192, s[20:23], s65 offen lds\n"
"s_add_u32 m0, s44, 1032\n"
"s_add_u32 s65, s66, 8192\n"
"buffer_load_dwordx4 v192, s[20:23], s65 offen lds\n"
"s_add_u32 m0, s44, 2112\n"
"s_add_u32 s65, s66, 16384\n"
"buffer_load_dwordx4 v192, s[20:23], s65 offen lds\n"
"s_add_u32 m0, s44, 3144\n"
"s_add_u32 s65, s66, 24576\n"
"buffer_load_dwordx4 v192, s[20:23], s65 offen lds\n"
"s_add_u32 m0, s44, 4224\n"
"s_add_u32 s65, s66, 65536\n"
"buffer_load_dwordx4 v192, s[20:23], s65 offen lds\n"
"s_add_u32 m0, s44, 5256\n"
"s_add_u32 s65, s66, 73728\n"
"buffer_load_dwordx4 v192, s[20:23], s65 offen lds\n"
"s_add_u32 m0, s44, 6336\n"
"s_add_u32 s65, s66, 81920\n"
"buffer_load_dwordx4 v192, s[20:23], s65 offen lds\n"
"s_add_u32 m0, s44, 7368\n"
"s_add_u32 s65, s66, 90112\n"
"buffer_load_dwordx4 v192, s[20:23], s65 offen lds\n"
"s_add_u32 s50, s50, 262144\n"
"s_cmp_eq_u32 s50, 3670016\n"
"s_cselect_b32 s50, 0, s50\n"
"s_mov_b32 s12, s82\n"
"s_mov_b32 s13, s83\n"
"s_mov_b32 s14, 33554432\n"
"s_mov_b32 s15, 0x20000\n"
"s_mov_b32 s16, s84\n"
"s_mov_b32 s17, s85\n"
"s_mov_b32 s18, 33554432\n"
"s_mov_b32 s19, 0x20000\n"
"s_mov_b32 s24, s88\n"
"s_mov_b32 s25, s89\n"
"s_add_u32 s99, s52, s50\n"
"s_add_u32 m0, s44, 33792\n"
"s_add_u32 s98, s99, 0\n"
"buffer_load_dwordx4 v192, s[20:23], s98 offen lds\n"
"s_mov_b32 s26, 3670016\n"
"s_mov_b32 s27, 0x20000\n"
"s_mov_b32 s32, s92\n"
"s_mov_b32 s33, s93\n"
"s_mov_b32 s34, 3670016\n"
"s_mov_b32 s35, 0x20000\n"
"s_mov_b32 s28, s94\n"
"s_mov_b32 s29, s95\n"
"s_mov_b32 s30, 29622272\n"
"s_mov_b32 s31, 0x20000\n"
"s_add_u32 m0, s44, 34824\n"
"s_add_u32 s98, s99, 8192\n"
"buffer_load_dwordx4 v192, s[20:23], s98 offen lds\n"
"s_mov_b32 s67, 3670016\n"
"s_lshl_b32 s54, s39, 4\n"
"s_add_u32 s54, s54, s96\n"
"s_lshl_b32 s54, s54, 18\n"
"s_lshl_b32 s66, s37, 13\n"
"s_add_u32 s55, s54, s66\n"
"s_lshl_b32 s66, s37, 12\n"
"s_add_u32 s54, s54, s66\n"
"s_lshl_b32 s58, s96, 8\n"
"s_lshl_b32 s66, s37, 6\n"
"s_add_u32 m0, s44, 35904\n"
"s_add_u32 s98, s99, 16384\n"
"buffer_load_dwordx4 v192, s[20:23], s98 offen lds\n"
"s_add_u32 s58, s58, s66\n"
"s_mul_i32 s58, s58, 896\n"
"s_lshl_b32 s61, s43, 3\n"
"s_add_u32 s61, s61, s40\n"
"s_mul_i32 s61, s61, 114688\n"
"s_lshl_b32 s66, s37, 1\n"
"s_add_u32 s66, s66, s36\n"
"s_mul_i32 s66, s66, 28672\n"
"s_add_u32 s61, s61, s66\n"
"s_add_u32 s61, s61, 262144\n"
"s_add_u32 m0, s44, 36936\n"
"s_add_u32 s98, s99, 24576\n"
"buffer_load_dwordx4 v192, s[20:23], s98 offen lds\n"
"s_mov_b32 s62, 0x3db8aa3b\n"
"s_mov_b32 s63, 0x3db8aa3b\n"
"s_mov_b32 s68, 32768\n"
"s_mov_b32 s48, 0\n"
"s_mov_b32 s49, 0\n"
"s_add_u32 s46, s44, 101376\n"
"s_add_u32 s47, s45, 16384\n"
"s_mul_i32 s65, s36, 16896\n"
"v_lshl_add_u32 v248, v4, 13, v5\n"
"v_lshl_add_u32 v249, v4, 12, v5\n"
"s_add_u32 m0, s44, 38016\n"
"s_add_u32 s98, s99, 65536\n"
"buffer_load_dwordx4 v192, s[20:23], s98 offen lds\n"
"v_mov_b32 v253, v2\n"
"v_lshrrev_b32 v7, 3, v2\n"
"v_mul_u32_u24 v7, 7168, v7\n"
"v_and_b32 v9, 7, v2\n"
"v_lshl_add_u32 v250, v9, 4, v7\n"
"v_mul_u32_u24 v7, 1056, v6\n"
"v_lshl_add_u32 v7, v8, 4, v7\n"
"v_add_u32 v244, s65, v7\n"
"v_add_u32 v245, 67584, v244\n"
"v_bfe_u32 v7, v2, 2, 3\n"
"s_add_u32 m0, s44, 39048\n"
"s_add_u32 s98, s99, 73728\n"
"buffer_load_dwordx4 v192, s[20:23], s98 offen lds\n"
"v_and_b32 v9, 3, v2\n"
"v_mul_u32_u24 v10, 1056, v7\n"
"v_lshl_add_u32 v10, v9, 3, v10\n"
"v_add_u32 v10, s65, v10\n"
"v_lshl_add_u32 v246, v4, 9, v10\n"
"v_add_u32 v247, 67584, v246\n"
"v_lshl_add_u32 v251, v4, 7, v10\n"
"v_add_u32 v252, 67584, v251\n"
"v_lshlrev_b32 v10, 10, v7\n"
"v_lshl_add_u32 v10, v9, 4, v10\n"
"s_add_u32 m0, s44, 40128\n"
"s_add_u32 s98, s99, 81920\n"
"buffer_load_dwordx4 v192, s[20:23], s98 offen lds\n"
"v_lshl_add_u32 v10, v4, 9, v10\n"
"v_lshrrev_b32 v9, 1, v7\n"
"v_lshl_add_u32 v10, v9, 6, v10\n"
"v_and_b32 v9, 1, v7\n"
"v_lshl_add_u32 v10, v9, 3, v10\n"
"v_add_u32 v194, s65, v10\n"
"v_mov_b32 v195, v194\n"
"v_lshlrev_b32 v21, 4, v2\n"
"v_add_u32 v196, s45, v21\n"
"v_xor_b32 v197, 2048, v196\n"
"s_add_u32 m0, s44, 41160\n"
"s_add_u32 s98, s99, 90112\n"
"buffer_load_dwordx4 v192, s[20:23], s98 offen lds\n"
"s_add_u32 s50, s50, 262144\n"
"s_cmp_eq_u32 s50, 3670016\n"
"s_cselect_b32 s50, 0, s50\n"
"v_mov_b32 v198, v196\n"
"v_mov_b32 v199, v197\n"
"s_lshl_b32 s66, s38, 12\n"
"s_add_u32 s66, s66, 118784\n"
"v_add_u32 v255, s66, v21\n"
"s_sub_u32 s61, s61, s66\n"
"s_xor_b32 s66, s38, 2\n"
"s_lshl_b32 s66, s66, 12\n"
"s_add_u32 s66, s66, 118784\n"
"v_add_u32 v254, s66, v21\n"
"s_add_u32 m0, s45, 8192\n"
"s_add_u32 s98, s53, s51\n"
"buffer_load_dwordx4 v193, s[8:11], s98 offen lds\n"
"s_getpc_b64 s[90:91]\n"
"LPC_%=:\n"
"s_mov_b32 s96, LEND_%=-LPC_%=-4\n"
"v_lshlrev_b32 v18, 7, %2\n"
"v_min_u32 v20, s96, v18\n"
"v_add_u32 v18, 32768, v18\n"
"global_load_dword v200, v20, s[90:91]\n"
"v_min_u32 v20, s96, v18\n"
"v_add_u32 v18, 32768, v18\n"
"global_load_dword v200, v20, s[90:91]\n"
"s_add_u32 m0, s45, 9216\n"
"s_add_u32 s98, s98, 14336\n"
"buffer_load_dwordx4 v193, s[8:11], s98 offen lds\n"
"s_add_u32 s51, s51, 64\n"
"s_cmp_eq_u32 s51, 896\n"
"s_cselect_b32 s51, 0, s51\n"
"v_mul_u32_u24 v16, 3, v11\n"
"v_lshlrev_b32 v201, v16, 56\n"
"v_mov_b32 v204, 0\n"
"v_mov_b32 v205, 0\n"
"v_mov_b32 v206, 0\n"
"v_mov_b32 v207, 0\n"
"s_nop 1\n"
"v_mfma_f32_32x32x16_bf16 a[160:175], v[204:207], v[204:207], 0\n"
"s_add_u32 s99, s52, s50\n"
"s_add_u32 m0, s44, 67584\n"
"s_add_u32 s98, s99, 0\n"
"buffer_load_dwordx4 v192, s[20:23], s98 offen lds\n"
"v_mfma_f32_32x32x16_bf16 a[176:191], v[204:207], v[204:207], 0\n"
"v_mfma_f32_32x32x16_bf16 a[192:207], v[204:207], v[204:207], 0\n"
"s_add_u32 m0, s44, 68616\n"
"s_add_u32 s98, s99, 8192\n"
"buffer_load_dwordx4 v192, s[20:23], s98 offen lds\n"
"v_mfma_f32_32x32x16_bf16 a[208:223], v[204:207], v[204:207], 0\n"
"v_mfma_f32_32x32x16_bf16 a[224:239], v[204:207], v[204:207], 0\n"
"s_add_u32 m0, s44, 69696\n"
"s_add_u32 s98, s99, 16384\n"
"buffer_load_dwordx4 v192, s[20:23], s98 offen lds\n"
"v_mfma_f32_32x32x16_bf16 a[240:255], v[204:207], v[204:207], 0\n"
"v_mfma_f32_32x32x16_bf16 v[160:175], v[204:207], v[204:207], 0\n"
"s_add_u32 m0, s44, 70728\n"
"s_add_u32 s98, s99, 24576\n"
"buffer_load_dwordx4 v192, s[20:23], s98 offen lds\n"
"v_mfma_f32_32x32x16_bf16 v[176:191], v[204:207], v[204:207], 0\n"
"v_mfma_f32_32x32x16_bf16 v[224:239], v[204:207], v[204:207], 0\n"
"s_add_u32 m0, s44, 71808\n"
"s_add_u32 s98, s99, 65536\n"
"buffer_load_dwordx4 v192, s[20:23], s98 offen lds\n"
"v_mfma_f32_32x32x16_bf16 v[112:127], v[204:207], v[204:207], 0\n"
"v_mfma_f32_32x32x16_bf16 v[96:111], v[204:207], v[204:207], 0\n"
"s_add_u32 m0, s44, 72840\n"
"s_add_u32 s98, s99, 73728\n"
"buffer_load_dwordx4 v192, s[20:23], s98 offen lds\n"
"v_mfma_f32_32x32x16_bf16 v[80:95], v[204:207], v[204:207], 0\n"
"v_mfma_f32_32x32x16_bf16 v[64:79], v[204:207], v[204:207], 0\n"
"s_add_u32 m0, s44, 73920\n"
"s_add_u32 s98, s99, 81920\n"
"buffer_load_dwordx4 v192, s[20:23], s98 offen lds\n"
"v_mfma_f32_32x32x16_bf16 v[48:63], v[204:207], v[204:207], 0\n"
"v_mfma_f32_32x32x16_bf16 v[32:47], v[204:207], v[204:207], 0\n"
"s_add_u32 m0, s44, 74952\n"
"s_add_u32 s98, s99, 90112\n"
"buffer_load_dwordx4 v192, s[20:23], s98 offen lds\n"
"s_add_u32 s50, s50, 262144\n"
"s_cmp_eq_u32 s50, 3670016\n"
"s_cselect_b32 s50, 0, s50\n"
"v_mfma_f32_32x32x16_bf16 v[16:31], v[204:207], v[204:207], 0\n"
"v_mfma_f32_32x32x16_bf16 v[0:15], v[204:207], v[204:207], 0\n"
"s_waitcnt vmcnt(20)\n"
"s_barrier\n"
"ds_read_b64_tr_b16 v[208:209], v195 offset:0\n"
"ds_read_b64_tr_b16 v[210:211], v195 offset:8448\n"
"ds_read_b128 v[128:131], v198 offset:0\n"
"ds_read_b64_tr_b16 v[212:213], v195 offset:8\n"
"ds_read_b64_tr_b16 v[214:215], v195 offset:8456\n"
"ds_read_b128 v[132:135], v198 offset:1024\n"
"ds_read_b128 v[136:139], v199 offset:0\n"
"ds_read_b64_tr_b16 v[216:217], v195 offset:64\n"
"ds_read_b64_tr_b16 v[218:219], v195 offset:8512\n"
"ds_read_b128 v[140:143], v199 offset:1024\n"
"ds_read_b64_tr_b16 v[220:221], v195 offset:72\n"
"ds_read_b64_tr_b16 v[222:223], v195 offset:8520\n"
"s_mov_b32 s64, 5\n"
"LQ_%=:\n"
"s_waitcnt lgkmcnt(9)\n"
"v_mfma_f32_16x16x32_bf16 v[0:3], v[208:211], v[128:131], v[0:3]\n"
"s_add_u32 m0, s47, 0\n"
"s_add_u32 s65, s53, s51\n"
"s_waitcnt lgkmcnt(6)\n"
"v_mfma_f32_16x16x32_bf16 v[8:11], v[208:211], v[132:135], v[8:11]\n"
"s_waitcnt lgkmcnt(5)\n"
"v_mfma_f32_16x16x32_bf16 v[16:19], v[208:211], v[136:139], v[16:19]\n"
"buffer_load_dwordx4 v193, s[8:11], s65 offen lds\n"
"s_waitcnt lgkmcnt(2)\n"
"v_mfma_f32_16x16x32_bf16 v[24:27], v[208:211], v[140:143], v[24:27]\n"
"ds_read_b64_tr_b16 v[208:209], v195 offset:128\n"
"v_mfma_f32_16x16x32_bf16 v[4:7], v[212:215], v[128:131], v[4:7]\n"
"s_add_u32 m0, s47, 1024\n"
"s_add_u32 s65, s65, 14336\n"
"v_mfma_f32_16x16x32_bf16 v[12:15], v[212:215], v[132:135], v[12:15]\n"
"ds_read_b64_tr_b16 v[210:211], v195 offset:8576\n"
"v_mfma_f32_16x16x32_bf16 v[20:23], v[212:215], v[136:139], v[20:23]\n"
"buffer_load_dwordx4 v193, s[8:11], s65 offen lds\n"
"v_mfma_f32_16x16x32_bf16 v[28:31], v[212:215], v[140:143], v[28:31]\n"
"ds_read_b64_tr_b16 v[212:213], v195 offset:136\n"
"s_add_u32 s51, s51, 64\n"
"s_cmp_eq_u32 s51, 896\n"
"s_cselect_b32 s51, 0, s51\n"
"s_waitcnt lgkmcnt(3)\n"
"v_mfma_f32_16x16x32_bf16 v[32:35], v[216:219], v[128:131], v[32:35]\n"
"s_add_u32 s66, s52, s50\n"
"s_add_u32 m0, s46, 0\n"
"s_add_u32 s65, s66, 0\n"
"v_mfma_f32_16x16x32_bf16 v[40:43], v[216:219], v[132:135], v[40:43]\n"
"ds_read_b64_tr_b16 v[214:215], v195 offset:8584\n"
"v_mfma_f32_16x16x32_bf16 v[48:51], v[216:219], v[136:139], v[48:51]\n"
"buffer_load_dwordx4 v192, s[20:23], s65 offen lds\n"
"v_mfma_f32_16x16x32_bf16 v[56:59], v[216:219], v[140:143], v[56:59]\n"
"ds_read_b64_tr_b16 v[216:217], v195 offset:192\n"
"v_mfma_f32_16x16x32_bf16 v[36:39], v[220:223], v[128:131], v[36:39]\n"
"s_add_u32 s47, s45, s49\n"
"v_mfma_f32_16x16x32_bf16 v[44:47], v[220:223], v[132:135], v[44:47]\n"
"ds_read_b64_tr_b16 v[218:219], v195 offset:8640\n"
"v_mfma_f32_16x16x32_bf16 v[52:55], v[220:223], v[136:139], v[52:55]\n"
"s_add_u32 m0, s46, 1032\n"
"s_add_u32 s65, s66, 8192\n"
"v_mfma_f32_16x16x32_bf16 v[60:63], v[220:223], v[140:143], v[60:63]\n"
"ds_read_b64_tr_b16 v[220:221], v195 offset:200\n"
"s_waitcnt lgkmcnt(3)\n"
"v_mfma_f32_16x16x32_bf16 v[64:67], v[208:211], v[128:131], v[64:67]\n"
"s_add_u32 s49, s49, 8192\n"
"buffer_load_dwordx4 v192, s[20:23], s65 offen lds\n"
"v_mfma_f32_16x16x32_bf16 v[72:75], v[208:211], v[132:135], v[72:75]\n"
"ds_read_b64_tr_b16 v[222:223], v195 offset:8648\n"
"v_mfma_f32_16x16x32_bf16 v[80:83], v[208:211], v[136:139], v[80:83]\n"
"s_add_u32 m0, s46, 2112\n"
"s_add_u32 s65, s66, 16384\n"
"v_mfma_f32_16x16x32_bf16 v[88:91], v[208:211], v[140:143], v[88:91]\n"
"ds_read_b64_tr_b16 v[208:209], v195 offset:256\n"
"v_mfma_f32_16x16x32_bf16 v[68:71], v[212:215], v[128:131], v[68:71]\n"
"s_cmp_eq_u32 s49, 24576\n"
"s_cselect_b32 s49, 0, s49\n"
"buffer_load_dwordx4 v192, s[20:23], s65 offen lds\n"
"v_mfma_f32_16x16x32_bf16 v[76:79], v[212:215], v[132:135], v[76:79]\n"
"ds_read_b64_tr_b16 v[210:211], v195 offset:8704\n"
"v_mfma_f32_16x16x32_bf16 v[84:87], v[212:215], v[136:139], v[84:87]\n"
"v_mfma_f32_16x16x32_bf16 v[92:95], v[212:215], v[140:143], v[92:95]\n"
"ds_read_b64_tr_b16 v[212:213], v195 offset:264\n"
"s_waitcnt lgkmcnt(3)\n"
"v_mfma_f32_16x16x32_bf16 v[96:99], v[216:219], v[128:131], v[96:99]\n"
"v_add_u32 v198, s49, v196\n"
"s_add_u32 m0, s46, 3144\n"
"s_add_u32 s65, s66, 24576\n"
"v_mfma_f32_16x16x32_bf16 v[104:107], v[216:219], v[132:135], v[104:107]\n"
"ds_read_b64_tr_b16 v[214:215], v195 offset:8712\n"
"v_mfma_f32_16x16x32_bf16 v[112:115], v[216:219], v[136:139], v[112:115]\n"
"buffer_load_dwordx4 v192, s[20:23], s65 offen lds\n"
"v_mfma_f32_16x16x32_bf16 v[120:123], v[216:219], v[140:143], v[120:123]\n"
"ds_read_b64_tr_b16 v[216:217], v195 offset:320\n"
"v_mfma_f32_16x16x32_bf16 v[100:103], v[220:223], v[128:131], v[100:103]\n"
"v_add_u32 v199, s49, v197\n"
"s_add_u32 m0, s46, 4224\n"
"s_add_u32 s65, s66, 65536\n"
"v_mfma_f32_16x16x32_bf16 v[108:111], v[220:223], v[132:135], v[108:111]\n"
"ds_read_b64_tr_b16 v[218:219], v195 offset:8768\n"
"v_mfma_f32_16x16x32_bf16 v[116:119], v[220:223], v[136:139], v[116:119]\n"
"buffer_load_dwordx4 v192, s[20:23], s65 offen lds\n"
"v_mfma_f32_16x16x32_bf16 v[124:127], v[220:223], v[140:143], v[124:127]\n"
"ds_read_b64_tr_b16 v[220:221], v195 offset:328\n"
"s_waitcnt lgkmcnt(3)\n"
"v_mfma_f32_16x16x32_bf16 v[160:163], v[208:211], v[128:131], v[160:163]\n"
"s_add_u32 m0, s46, 5256\n"
"s_add_u32 s65, s66, 73728\n"
"v_mfma_f32_16x16x32_bf16 v[168:171], v[208:211], v[132:135], v[168:171]\n"
"ds_read_b64_tr_b16 v[222:223], v195 offset:8776\n"
"v_mfma_f32_16x16x32_bf16 v[176:179], v[208:211], v[136:139], v[176:179]\n"
"buffer_load_dwordx4 v192, s[20:23], s65 offen lds\n"
"v_mfma_f32_16x16x32_bf16 v[184:187], v[208:211], v[140:143], v[184:187]\n"
"ds_read_b64_tr_b16 v[208:209], v195 offset:384\n"
"v_mfma_f32_16x16x32_bf16 v[164:167], v[212:215], v[128:131], v[164:167]\n"
"v_mfma_f32_16x16x32_bf16 v[172:175], v[212:215], v[132:135], v[172:175]\n"
"ds_read_b64_tr_b16 v[210:211], v195 offset:8832\n"
"v_mfma_f32_16x16x32_bf16 v[180:183], v[212:215], v[136:139], v[180:183]\n"
"s_add_u32 m0, s46, 6336\n"
"s_add_u32 s65, s66, 81920\n"
"v_mfma_f32_16x16x32_bf16 v[188:191], v[212:215], v[140:143], v[188:191]\n"
"ds_read_b64_tr_b16 v[212:213], v195 offset:392\n"
"s_waitcnt lgkmcnt(3)\n"
"v_mfma_f32_16x16x32_bf16 a[160:163], v[216:219], v[128:131], a[160:163]\n"
"buffer_load_dwordx4 v192, s[20:23], s65 offen lds\n"
"v_mfma_f32_16x16x32_bf16 a[164:167], v[216:219], v[132:135], a[164:167]\n"
"ds_read_b64_tr_b16 v[214:215], v195 offset:8840\n"
"v_mfma_f32_16x16x32_bf16 a[168:171], v[216:219], v[136:139], a[168:171]\n"
"s_add_u32 m0, s46, 7368\n"
"s_add_u32 s65, s66, 90112\n"
"v_mfma_f32_16x16x32_bf16 a[172:175], v[216:219], v[140:143], a[172:175]\n"
"ds_read_b64_tr_b16 v[216:217], v195 offset:448\n"
"v_mfma_f32_16x16x32_bf16 a[176:179], v[220:223], v[128:131], a[176:179]\n"
"buffer_load_dwordx4 v192, s[20:23], s65 offen lds\n"
"v_mfma_f32_16x16x32_bf16 a[180:183], v[220:223], v[132:135], a[180:183]\n"
"ds_read_b64_tr_b16 v[218:219], v195 offset:8896\n"
"s_add_u32 s50, s50, 262144\n"
"s_cmp_eq_u32 s50, 3670016\n"
"s_cselect_b32 s50, 0, s50\n"
"v_mfma_f32_16x16x32_bf16 a[184:187], v[220:223], v[136:139], a[184:187]\n"
"v_mfma_f32_16x16x32_bf16 a[188:191], v[220:223], v[140:143], a[188:191]\n"
"ds_read_b64_tr_b16 v[220:221], v195 offset:456\n"
"s_waitcnt lgkmcnt(5)\n"
"v_mfma_f32_16x16x32_bf16 a[192:195], v[208:211], v[128:131], a[192:195]\n"
"v_mfma_f32_16x16x32_bf16 a[196:199], v[208:211], v[132:135], a[196:199]\n"
"ds_read_b64_tr_b16 v[222:223], v195 offset:8904\n"
"v_mfma_f32_16x16x32_bf16 a[200:203], v[208:211], v[136:139], a[200:203]\n"
"s_add_u32 s46, s44, s48\n"
"v_mfma_f32_16x16x32_bf16 a[204:207], v[208:211], v[140:143], a[204:207]\n"
"s_add_u32 s48, s48, 33792\n"
"s_waitcnt lgkmcnt(4)\n"
"v_mfma_f32_16x16x32_bf16 a[208:211], v[212:215], v[128:131], a[208:211]\n"
"s_cmp_eq_u32 s48, 135168\n"
"s_cselect_b32 s48, 0, s48\n"
"v_mfma_f32_16x16x32_bf16 a[212:215], v[212:215], v[132:135], a[212:215]\n"
"v_add_u32 v195, s48, v194\n"
"v_mfma_f32_16x16x32_bf16 a[216:219], v[212:215], v[136:139], a[216:219]\n"
"s_waitcnt vmcnt(18) lgkmcnt(0)\n"
"s_barrier\n"
"ds_read_b64_tr_b16 v[208:209], v195 offset:0\n"
"v_mfma_f32_16x16x32_bf16 a[220:223], v[212:215], v[140:143], a[220:223]\n"
"ds_read_b64_tr_b16 v[210:211], v195 offset:8448\n"
"ds_read_b128 v[144:147], v198 offset:0\n"
"v_mfma_f32_16x16x32_bf16 a[224:227], v[216:219], v[128:131], a[224:227]\n"
"ds_read_b64_tr_b16 v[212:213], v195 offset:8\n"
"v_mfma_f32_16x16x32_bf16 a[228:231], v[216:219], v[132:135], a[228:231]\n"
"ds_read_b64_tr_b16 v[214:215], v195 offset:8456\n"
"ds_read_b128 v[148:151], v198 offset:1024\n"
"v_mfma_f32_16x16x32_bf16 a[232:235], v[216:219], v[136:139], a[232:235]\n"
"ds_read_b128 v[152:155], v199 offset:0\n"
"v_mfma_f32_16x16x32_bf16 a[236:239], v[216:219], v[140:143], a[236:239]\n"
"ds_read_b64_tr_b16 v[216:217], v195 offset:64\n"
"v_mfma_f32_16x16x32_bf16 a[240:243], v[220:223], v[128:131], a[240:243]\n"
"ds_read_b64_tr_b16 v[218:219], v195 offset:8512\n"
"v_mfma_f32_16x16x32_bf16 a[244:247], v[220:223], v[132:135], a[244:247]\n"
"ds_read_b128 v[156:159], v199 offset:1024\n"
"v_mfma_f32_16x16x32_bf16 a[248:251], v[220:223], v[136:139], a[248:251]\n"
"v_mfma_f32_16x16x32_bf16 a[252:255], v[220:223], v[140:143], a[252:255]\n"
"ds_read_b64_tr_b16 v[220:221], v195 offset:72\n"
"ds_read_b64_tr_b16 v[222:223], v195 offset:8520\n"
"s_waitcnt lgkmcnt(9)\n"
"v_mfma_f32_16x16x32_bf16 v[0:3], v[208:211], v[144:147], v[0:3]\n"
"s_add_u32 m0, s47, 0\n"
"s_add_u32 s65, s53, s51\n"
"s_waitcnt lgkmcnt(6)\n"
"v_mfma_f32_16x16x32_bf16 v[8:11], v[208:211], v[148:151], v[8:11]\n"
"s_waitcnt lgkmcnt(5)\n"
"v_mfma_f32_16x16x32_bf16 v[16:19], v[208:211], v[152:155], v[16:19]\n"
"buffer_load_dwordx4 v193, s[8:11], s65 offen lds\n"
"s_waitcnt lgkmcnt(2)\n"
"v_mfma_f32_16x16x32_bf16 v[24:27], v[208:211], v[156:159], v[24:27]\n"
"ds_read_b64_tr_b16 v[208:209], v195 offset:128\n"
"v_mfma_f32_16x16x32_bf16 v[4:7], v[212:215], v[144:147], v[4:7]\n"
"s_add_u32 m0, s47, 1024\n"
"s_add_u32 s65, s65, 14336\n"
"v_mfma_f32_16x16x32_bf16 v[12:15], v[212:215], v[148:151], v[12:15]\n"
"ds_read_b64_tr_b16 v[210:211], v195 offset:8576\n"
"v_mfma_f32_16x16x32_bf16 v[20:23], v[212:215], v[152:155], v[20:23]\n"
"buffer_load_dwordx4 v193, s[8:11], s65 offen lds\n"
"v_mfma_f32_16x16x32_bf16 v[28:31], v[212:215], v[156:159], v[28:31]\n"
"ds_read_b64_tr_b16 v[212:213], v195 offset:136\n"
"s_add_u32 s51, s51, 64\n"
"s_cmp_eq_u32 s51, 896\n"
"s_cselect_b32 s51, 0, s51\n"
"s_waitcnt lgkmcnt(3)\n"
"v_mfma_f32_16x16x32_bf16 v[32:35], v[216:219], v[144:147], v[32:35]\n"
"s_add_u32 s66, s52, s50\n"
"s_add_u32 m0, s46, 0\n"
"s_add_u32 s65, s66, 0\n"
"v_mfma_f32_16x16x32_bf16 v[40:43], v[216:219], v[148:151], v[40:43]\n"
"ds_read_b64_tr_b16 v[214:215], v195 offset:8584\n"
"v_mfma_f32_16x16x32_bf16 v[48:51], v[216:219], v[152:155], v[48:51]\n"
"buffer_load_dwordx4 v192, s[20:23], s65 offen lds\n"
"v_mfma_f32_16x16x32_bf16 v[56:59], v[216:219], v[156:159], v[56:59]\n"
"ds_read_b64_tr_b16 v[216:217], v195 offset:192\n"
"v_mfma_f32_16x16x32_bf16 v[36:39], v[220:223], v[144:147], v[36:39]\n"
"s_add_u32 s47, s45, s49\n"
"v_mfma_f32_16x16x32_bf16 v[44:47], v[220:223], v[148:151], v[44:47]\n"
"ds_read_b64_tr_b16 v[218:219], v195 offset:8640\n"
"v_mfma_f32_16x16x32_bf16 v[52:55], v[220:223], v[152:155], v[52:55]\n"
"s_add_u32 m0, s46, 1032\n"
"s_add_u32 s65, s66, 8192\n"
"v_mfma_f32_16x16x32_bf16 v[60:63], v[220:223], v[156:159], v[60:63]\n"
"ds_read_b64_tr_b16 v[220:221], v195 offset:200\n"
"s_waitcnt lgkmcnt(3)\n"
"v_mfma_f32_16x16x32_bf16 v[64:67], v[208:211], v[144:147], v[64:67]\n"
"s_add_u32 s49, s49, 8192\n"
"buffer_load_dwordx4 v192, s[20:23], s65 offen lds\n"
"v_mfma_f32_16x16x32_bf16 v[72:75], v[208:211], v[148:151], v[72:75]\n"
"ds_read_b64_tr_b16 v[222:223], v195 offset:8648\n"
"v_mfma_f32_16x16x32_bf16 v[80:83], v[208:211], v[152:155], v[80:83]\n"
"s_add_u32 m0, s46, 2112\n"
"s_add_u32 s65, s66, 16384\n"
"v_mfma_f32_16x16x32_bf16 v[88:91], v[208:211], v[156:159], v[88:91]\n"
"ds_read_b64_tr_b16 v[208:209], v195 offset:256\n"
"v_mfma_f32_16x16x32_bf16 v[68:71], v[212:215], v[144:147], v[68:71]\n"
"s_cmp_eq_u32 s49, 24576\n"
"s_cselect_b32 s49, 0, s49\n"
"buffer_load_dwordx4 v192, s[20:23], s65 offen lds\n"
"v_mfma_f32_16x16x32_bf16 v[76:79], v[212:215], v[148:151], v[76:79]\n"
"ds_read_b64_tr_b16 v[210:211], v195 offset:8704\n"
"v_mfma_f32_16x16x32_bf16 v[84:87], v[212:215], v[152:155], v[84:87]\n"
"v_mfma_f32_16x16x32_bf16 v[92:95], v[212:215], v[156:159], v[92:95]\n"
"ds_read_b64_tr_b16 v[212:213], v195 offset:264\n"
"s_waitcnt lgkmcnt(3)\n"
"v_mfma_f32_16x16x32_bf16 v[96:99], v[216:219], v[144:147], v[96:99]\n"
"v_add_u32 v198, s49, v196\n"
"s_add_u32 m0, s46, 3144\n"
"s_add_u32 s65, s66, 24576\n"
"v_mfma_f32_16x16x32_bf16 v[104:107], v[216:219], v[148:151], v[104:107]\n"
"ds_read_b64_tr_b16 v[214:215], v195 offset:8712\n"
"v_mfma_f32_16x16x32_bf16 v[112:115], v[216:219], v[152:155], v[112:115]\n"
"buffer_load_dwordx4 v192, s[20:23], s65 offen lds\n"
"v_mfma_f32_16x16x32_bf16 v[120:123], v[216:219], v[156:159], v[120:123]\n"
"ds_read_b64_tr_b16 v[216:217], v195 offset:320\n"
"v_mfma_f32_16x16x32_bf16 v[100:103], v[220:223], v[144:147], v[100:103]\n"
"v_add_u32 v199, s49, v197\n"
"s_add_u32 m0, s46, 4224\n"
"s_add_u32 s65, s66, 65536\n"
"v_mfma_f32_16x16x32_bf16 v[108:111], v[220:223], v[148:151], v[108:111]\n"
"ds_read_b64_tr_b16 v[218:219], v195 offset:8768\n"
"v_mfma_f32_16x16x32_bf16 v[116:119], v[220:223], v[152:155], v[116:119]\n"
"buffer_load_dwordx4 v192, s[20:23], s65 offen lds\n"
"v_mfma_f32_16x16x32_bf16 v[124:127], v[220:223], v[156:159], v[124:127]\n"
"ds_read_b64_tr_b16 v[220:221], v195 offset:328\n"
"s_waitcnt lgkmcnt(3)\n"
"v_mfma_f32_16x16x32_bf16 v[160:163], v[208:211], v[144:147], v[160:163]\n"
"s_add_u32 m0, s46, 5256\n"
"s_add_u32 s65, s66, 73728\n"
"v_mfma_f32_16x16x32_bf16 v[168:171], v[208:211], v[148:151], v[168:171]\n"
"ds_read_b64_tr_b16 v[222:223], v195 offset:8776\n"
"v_mfma_f32_16x16x32_bf16 v[176:179], v[208:211], v[152:155], v[176:179]\n"
"buffer_load_dwordx4 v192, s[20:23], s65 offen lds\n"
"v_mfma_f32_16x16x32_bf16 v[184:187], v[208:211], v[156:159], v[184:187]\n"
"ds_read_b64_tr_b16 v[208:209], v195 offset:384\n"
"v_mfma_f32_16x16x32_bf16 v[164:167], v[212:215], v[144:147], v[164:167]\n"
"v_mfma_f32_16x16x32_bf16 v[172:175], v[212:215], v[148:151], v[172:175]\n"
"ds_read_b64_tr_b16 v[210:211], v195 offset:8832\n"
"v_mfma_f32_16x16x32_bf16 v[180:183], v[212:215], v[152:155], v[180:183]\n"
"s_add_u32 m0, s46, 6336\n"
"s_add_u32 s65, s66, 81920\n"
"v_mfma_f32_16x16x32_bf16 v[188:191], v[212:215], v[156:159], v[188:191]\n"
"ds_read_b64_tr_b16 v[212:213], v195 offset:392\n"
"s_waitcnt lgkmcnt(3)\n"
"v_mfma_f32_16x16x32_bf16 a[160:163], v[216:219], v[144:147], a[160:163]\n"
"buffer_load_dwordx4 v192, s[20:23], s65 offen lds\n"
"v_mfma_f32_16x16x32_bf16 a[164:167], v[216:219], v[148:151], a[164:167]\n"
"ds_read_b64_tr_b16 v[214:215], v195 offset:8840\n"
"v_mfma_f32_16x16x32_bf16 a[168:171], v[216:219], v[152:155], a[168:171]\n"
"s_add_u32 m0, s46, 7368\n"
"s_add_u32 s65, s66, 90112\n"
"v_mfma_f32_16x16x32_bf16 a[172:175], v[216:219], v[156:159], a[172:175]\n"
"ds_read_b64_tr_b16 v[216:217], v195 offset:448\n"
"v_mfma_f32_16x16x32_bf16 a[176:179], v[220:223], v[144:147], a[176:179]\n"
"buffer_load_dwordx4 v192, s[20:23], s65 offen lds\n"
"v_mfma_f32_16x16x32_bf16 a[180:183], v[220:223], v[148:151], a[180:183]\n"
"ds_read_b64_tr_b16 v[218:219], v195 offset:8896\n"
"s_add_u32 s50, s50, 262144\n"
"s_cmp_eq_u32 s50, 3670016\n"
"s_cselect_b32 s50, 0, s50\n"
"v_mfma_f32_16x16x32_bf16 a[184:187], v[220:223], v[152:155], a[184:187]\n"
"v_mfma_f32_16x16x32_bf16 a[188:191], v[220:223], v[156:159], a[188:191]\n"
"ds_read_b64_tr_b16 v[220:221], v195 offset:456\n"
"s_waitcnt lgkmcnt(5)\n"
"v_mfma_f32_16x16x32_bf16 a[192:195], v[208:211], v[144:147], a[192:195]\n"
"v_mfma_f32_16x16x32_bf16 a[196:199], v[208:211], v[148:151], a[196:199]\n"
"ds_read_b64_tr_b16 v[222:223], v195 offset:8904\n"
"v_mfma_f32_16x16x32_bf16 a[200:203], v[208:211], v[152:155], a[200:203]\n"
"s_add_u32 s46, s44, s48\n"
"v_mfma_f32_16x16x32_bf16 a[204:207], v[208:211], v[156:159], a[204:207]\n"
"s_add_u32 s48, s48, 33792\n"
"s_waitcnt lgkmcnt(4)\n"
"v_mfma_f32_16x16x32_bf16 a[208:211], v[212:215], v[144:147], a[208:211]\n"
"s_cmp_eq_u32 s48, 135168\n"
"s_cselect_b32 s48, 0, s48\n"
"v_mfma_f32_16x16x32_bf16 a[212:215], v[212:215], v[148:151], a[212:215]\n"
"v_add_u32 v195, s48, v194\n"
"v_mfma_f32_16x16x32_bf16 a[216:219], v[212:215], v[152:155], a[216:219]\n"
"s_waitcnt vmcnt(18) lgkmcnt(0)\n"
"s_barrier\n"
"ds_read_b64_tr_b16 v[208:209], v195 offset:0\n"
"v_mfma_f32_16x16x32_bf16 a[220:223], v[212:215], v[156:159], a[220:223]\n"
"ds_read_b64_tr_b16 v[210:211], v195 offset:8448\n"
"ds_read_b128 v[128:131], v198 offset:0\n"
"v_mfma_f32_16x16x32_bf16 a[224:227], v[216:219], v[144:147], a[224:227]\n"
"ds_read_b64_tr_b16 v[212:213], v195 offset:8\n"
"v_mfma_f32_16x16x32_bf16 a[228:231], v[216:219], v[148:151], a[228:231]\n"
"ds_read_b64_tr_b16 v[214:215], v195 offset:8456\n"
"ds_read_b128 v[132:135], v198 offset:1024\n"
"v_mfma_f32_16x16x32_bf16 a[232:235], v[216:219], v[152:155], a[232:235]\n"
"ds_read_b128 v[136:139], v199 offset:0\n"
"v_mfma_f32_16x16x32_bf16 a[236:239], v[216:219], v[156:159], a[236:239]\n"
"ds_read_b64_tr_b16 v[216:217], v195 offset:64\n"
"v_mfma_f32_16x16x32_bf16 a[240:243], v[220:223], v[144:147], a[240:243]\n"
"ds_read_b64_tr_b16 v[218:219], v195 offset:8512\n"
"v_mfma_f32_16x16x32_bf16 a[244:247], v[220:223], v[148:151], a[244:247]\n"
"ds_read_b128 v[140:143], v199 offset:1024\n"
"v_mfma_f32_16x16x32_bf16 a[248:251], v[220:223], v[152:155], a[248:251]\n"
"v_mfma_f32_16x16x32_bf16 a[252:255], v[220:223], v[156:159], a[252:255]\n"
"ds_read_b64_tr_b16 v[220:221], v195 offset:72\n"
"ds_read_b64_tr_b16 v[222:223], v195 offset:8520\n"
"s_sub_u32 s64, s64, 1\n"
"s_cmp_lg_u32 s64, 0\n"
"s_cbranch_scc1 LQ_%=\n"
"s_waitcnt lgkmcnt(9)\n"
"v_mfma_f32_16x16x32_bf16 v[0:3], v[208:211], v[128:131], v[0:3]\n"
"s_add_u32 m0, s47, 0\n"
"s_add_u32 s65, s53, s51\n"
"s_waitcnt lgkmcnt(6)\n"
"v_mfma_f32_16x16x32_bf16 v[8:11], v[208:211], v[132:135], v[8:11]\n"
"s_waitcnt lgkmcnt(5)\n"
"v_mfma_f32_16x16x32_bf16 v[16:19], v[208:211], v[136:139], v[16:19]\n"
"buffer_load_dwordx4 v193, s[8:11], s65 offen lds\n"
"s_waitcnt lgkmcnt(2)\n"
"v_mfma_f32_16x16x32_bf16 v[24:27], v[208:211], v[140:143], v[24:27]\n"
"ds_read_b64_tr_b16 v[208:209], v195 offset:128\n"
"v_mfma_f32_16x16x32_bf16 v[4:7], v[212:215], v[128:131], v[4:7]\n"
"s_add_u32 m0, s47, 1024\n"
"s_add_u32 s65, s65, 14336\n"
"v_mfma_f32_16x16x32_bf16 v[12:15], v[212:215], v[132:135], v[12:15]\n"
"ds_read_b64_tr_b16 v[210:211], v195 offset:8576\n"
"v_mfma_f32_16x16x32_bf16 v[20:23], v[212:215], v[136:139], v[20:23]\n"
"buffer_load_dwordx4 v193, s[8:11], s65 offen lds\n"
"v_mfma_f32_16x16x32_bf16 v[28:31], v[212:215], v[140:143], v[28:31]\n"
"ds_read_b64_tr_b16 v[212:213], v195 offset:136\n"
"s_add_u32 s51, s51, 64\n"
"s_cmp_eq_u32 s51, 896\n"
"s_cselect_b32 s51, 0, s51\n"
"s_waitcnt lgkmcnt(3)\n"
"v_mfma_f32_16x16x32_bf16 v[32:35], v[216:219], v[128:131], v[32:35]\n"
"s_add_u32 s66, s52, s50\n"
"s_add_u32 m0, s46, 0\n"
"s_add_u32 s65, s66, 0\n"
"v_mfma_f32_16x16x32_bf16 v[40:43], v[216:219], v[132:135], v[40:43]\n"
"ds_read_b64_tr_b16 v[214:215], v195 offset:8584\n"
"v_mfma_f32_16x16x32_bf16 v[48:51], v[216:219], v[136:139], v[48:51]\n"
"buffer_load_dwordx4 v192, s[20:23], s65 offen lds\n"
"v_mfma_f32_16x16x32_bf16 v[56:59], v[216:219], v[140:143], v[56:59]\n"
"ds_read_b64_tr_b16 v[216:217], v195 offset:192\n"
"v_mfma_f32_16x16x32_bf16 v[36:39], v[220:223], v[128:131], v[36:39]\n"
"s_add_u32 s47, s45, s49\n"
"v_mfma_f32_16x16x32_bf16 v[44:47], v[220:223], v[132:135], v[44:47]\n"
"ds_read_b64_tr_b16 v[218:219], v195 offset:8640\n"
"v_mfma_f32_16x16x32_bf16 v[52:55], v[220:223], v[136:139], v[52:55]\n"
"s_add_u32 m0, s46, 1032\n"
"s_add_u32 s65, s66, 8192\n"
"v_mfma_f32_16x16x32_bf16 v[60:63], v[220:223], v[140:143], v[60:63]\n"
"ds_read_b64_tr_b16 v[220:221], v195 offset:200\n"
"s_waitcnt lgkmcnt(3)\n"
"v_mfma_f32_16x16x32_bf16 v[64:67], v[208:211], v[128:131], v[64:67]\n"
"s_add_u32 s49, s49, 8192\n"
"buffer_load_dwordx4 v192, s[20:23], s65 offen lds\n"
"v_mfma_f32_16x16x32_bf16 v[72:75], v[208:211], v[132:135], v[72:75]\n"
"ds_read_b64_tr_b16 v[222:223], v195 offset:8648\n"
"v_mfma_f32_16x16x32_bf16 v[80:83], v[208:211], v[136:139], v[80:83]\n"
"s_add_u32 m0, s46, 2112\n"
"s_add_u32 s65, s66, 16384\n"
"v_mfma_f32_16x16x32_bf16 v[88:91], v[208:211], v[140:143], v[88:91]\n"
"ds_read_b64_tr_b16 v[208:209], v195 offset:256\n"
"v_mfma_f32_16x16x32_bf16 v[68:71], v[212:215], v[128:131], v[68:71]\n"
"s_cmp_eq_u32 s49, 24576\n"
"s_cselect_b32 s49, 0, s49\n"
"buffer_load_dwordx4 v192, s[20:23], s65 offen lds\n"
"v_mfma_f32_16x16x32_bf16 v[76:79], v[212:215], v[132:135], v[76:79]\n"
"ds_read_b64_tr_b16 v[210:211], v195 offset:8704\n"
"v_mfma_f32_16x16x32_bf16 v[84:87], v[212:215], v[136:139], v[84:87]\n"
"v_mfma_f32_16x16x32_bf16 v[92:95], v[212:215], v[140:143], v[92:95]\n"
"ds_read_b64_tr_b16 v[212:213], v195 offset:264\n"
"s_waitcnt lgkmcnt(3)\n"
"v_mfma_f32_16x16x32_bf16 v[96:99], v[216:219], v[128:131], v[96:99]\n"
"v_add_u32 v198, s49, v196\n"
"s_add_u32 m0, s46, 3144\n"
"s_add_u32 s65, s66, 24576\n"
"v_mfma_f32_16x16x32_bf16 v[104:107], v[216:219], v[132:135], v[104:107]\n"
"ds_read_b64_tr_b16 v[214:215], v195 offset:8712\n"
"v_mfma_f32_16x16x32_bf16 v[112:115], v[216:219], v[136:139], v[112:115]\n"
"buffer_load_dwordx4 v192, s[20:23], s65 offen lds\n"
"v_mfma_f32_16x16x32_bf16 v[120:123], v[216:219], v[140:143], v[120:123]\n"
"ds_read_b64_tr_b16 v[216:217], v195 offset:320\n"
"v_mfma_f32_16x16x32_bf16 v[100:103], v[220:223], v[128:131], v[100:103]\n"
"v_add_u32 v199, s49, v197\n"
"s_add_u32 m0, s46, 4224\n"
"s_add_u32 s65, s66, 65536\n"
"v_mfma_f32_16x16x32_bf16 v[108:111], v[220:223], v[132:135], v[108:111]\n"
"ds_read_b64_tr_b16 v[218:219], v195 offset:8768\n"
"v_mfma_f32_16x16x32_bf16 v[116:119], v[220:223], v[136:139], v[116:119]\n"
"buffer_load_dwordx4 v192, s[20:23], s65 offen lds\n"
"v_mfma_f32_16x16x32_bf16 v[124:127], v[220:223], v[140:143], v[124:127]\n"
"ds_read_b64_tr_b16 v[220:221], v195 offset:328\n"
"s_waitcnt lgkmcnt(3)\n"
"v_mfma_f32_16x16x32_bf16 v[160:163], v[208:211], v[128:131], v[160:163]\n"
"s_add_u32 m0, s46, 5256\n"
"s_add_u32 s65, s66, 73728\n"
"v_mfma_f32_16x16x32_bf16 v[168:171], v[208:211], v[132:135], v[168:171]\n"
"ds_read_b64_tr_b16 v[222:223], v195 offset:8776\n"
"v_mfma_f32_16x16x32_bf16 v[176:179], v[208:211], v[136:139], v[176:179]\n"
"buffer_load_dwordx4 v192, s[20:23], s65 offen lds\n"
"v_mfma_f32_16x16x32_bf16 v[184:187], v[208:211], v[140:143], v[184:187]\n"
"ds_read_b64_tr_b16 v[208:209], v195 offset:384\n"
"v_mfma_f32_16x16x32_bf16 v[164:167], v[212:215], v[128:131], v[164:167]\n"
"v_mfma_f32_16x16x32_bf16 v[172:175], v[212:215], v[132:135], v[172:175]\n"
"ds_read_b64_tr_b16 v[210:211], v195 offset:8832\n"
"v_mfma_f32_16x16x32_bf16 v[180:183], v[212:215], v[136:139], v[180:183]\n"
"s_add_u32 m0, s46, 6336\n"
"s_add_u32 s65, s66, 81920\n"
"v_mfma_f32_16x16x32_bf16 v[188:191], v[212:215], v[140:143], v[188:191]\n"
"ds_read_b64_tr_b16 v[212:213], v195 offset:392\n"
"s_waitcnt lgkmcnt(3)\n"
"v_mfma_f32_16x16x32_bf16 a[160:163], v[216:219], v[128:131], a[160:163]\n"
"buffer_load_dwordx4 v192, s[20:23], s65 offen lds\n"
"v_mfma_f32_16x16x32_bf16 a[164:167], v[216:219], v[132:135], a[164:167]\n"
"ds_read_b64_tr_b16 v[214:215], v195 offset:8840\n"
"v_mfma_f32_16x16x32_bf16 a[168:171], v[216:219], v[136:139], a[168:171]\n"
"s_add_u32 m0, s46, 7368\n"
"s_add_u32 s65, s66, 90112\n"
"v_mfma_f32_16x16x32_bf16 a[172:175], v[216:219], v[140:143], a[172:175]\n"
"ds_read_b64_tr_b16 v[216:217], v195 offset:448\n"
"v_mfma_f32_16x16x32_bf16 a[176:179], v[220:223], v[128:131], a[176:179]\n"
"buffer_load_dwordx4 v192, s[20:23], s65 offen lds\n"
"v_mfma_f32_16x16x32_bf16 a[180:183], v[220:223], v[132:135], a[180:183]\n"
"ds_read_b64_tr_b16 v[218:219], v195 offset:8896\n"
"s_add_u32 s50, s50, 262144\n"
"s_cmp_eq_u32 s50, 3670016\n"
"s_cselect_b32 s50, 0, s50\n"
"v_mfma_f32_16x16x32_bf16 a[184:187], v[220:223], v[136:139], a[184:187]\n"
"v_mfma_f32_16x16x32_bf16 a[188:191], v[220:223], v[140:143], a[188:191]\n"
"ds_read_b64_tr_b16 v[220:221], v195 offset:456\n"
"s_waitcnt lgkmcnt(5)\n"
"v_mfma_f32_16x16x32_bf16 a[192:195], v[208:211], v[128:131], a[192:195]\n"
"v_mfma_f32_16x16x32_bf16 a[196:199], v[208:211], v[132:135], a[196:199]\n"
"ds_read_b64_tr_b16 v[222:223], v195 offset:8904\n"
"v_mfma_f32_16x16x32_bf16 a[200:203], v[208:211], v[136:139], a[200:203]\n"
"s_add_u32 s46, s44, s48\n"
"v_mfma_f32_16x16x32_bf16 a[204:207], v[208:211], v[140:143], a[204:207]\n"
"s_add_u32 s48, s48, 33792\n"
"s_waitcnt lgkmcnt(4)\n"
"v_mfma_f32_16x16x32_bf16 a[208:211], v[212:215], v[128:131], a[208:211]\n"
"s_cmp_eq_u32 s48, 135168\n"
"s_cselect_b32 s48, 0, s48\n"
"v_mfma_f32_16x16x32_bf16 a[212:215], v[212:215], v[132:135], a[212:215]\n"
"v_add_u32 v195, s48, v194\n"
"v_mfma_f32_16x16x32_bf16 a[216:219], v[212:215], v[136:139], a[216:219]\n"
"s_waitcnt vmcnt(18) lgkmcnt(0)\n"
"s_barrier\n"
"ds_read_b64_tr_b16 v[208:209], v195 offset:0\n"
"v_mfma_f32_16x16x32_bf16 a[220:223], v[212:215], v[140:143], a[220:223]\n"
"ds_read_b64_tr_b16 v[210:211], v195 offset:8448\n"
"ds_read_b128 v[144:147], v198 offset:0\n"
"v_mfma_f32_16x16x32_bf16 a[224:227], v[216:219], v[128:131], a[224:227]\n"
"ds_read_b64_tr_b16 v[212:213], v195 offset:8\n"
"v_mfma_f32_16x16x32_bf16 a[228:231], v[216:219], v[132:135], a[228:231]\n"
"ds_read_b64_tr_b16 v[214:215], v195 offset:8456\n"
"ds_read_b128 v[148:151], v198 offset:1024\n"
"v_mfma_f32_16x16x32_bf16 a[232:235], v[216:219], v[136:139], a[232:235]\n"
"ds_read_b128 v[152:155], v199 offset:0\n"
"v_mfma_f32_16x16x32_bf16 a[236:239], v[216:219], v[140:143], a[236:239]\n"
"ds_read_b64_tr_b16 v[216:217], v195 offset:64\n"
"v_mfma_f32_16x16x32_bf16 a[240:243], v[220:223], v[128:131], a[240:243]\n"
"ds_read_b64_tr_b16 v[218:219], v195 offset:8512\n"
"v_mfma_f32_16x16x32_bf16 a[244:247], v[220:223], v[132:135], a[244:247]\n"
"ds_read_b128 v[156:159], v199 offset:1024\n"
"v_mfma_f32_16x16x32_bf16 a[248:251], v[220:223], v[136:139], a[248:251]\n"
"v_mfma_f32_16x16x32_bf16 a[252:255], v[220:223], v[140:143], a[252:255]\n"
"ds_read_b64_tr_b16 v[220:221], v195 offset:72\n"
"ds_read_b64_tr_b16 v[222:223], v195 offset:8520\n"
"s_waitcnt lgkmcnt(9)\n"
"v_mfma_f32_16x16x32_bf16 v[0:3], v[208:211], v[144:147], v[0:3]\n"
"s_add_u32 m0, s47, 0\n"
"s_add_u32 s65, s53, s51\n"
"s_waitcnt lgkmcnt(6)\n"
"v_mfma_f32_16x16x32_bf16 v[8:11], v[208:211], v[148:151], v[8:11]\n"
"s_waitcnt lgkmcnt(5)\n"
"v_mfma_f32_16x16x32_bf16 v[16:19], v[208:211], v[152:155], v[16:19]\n"
"buffer_load_dwordx4 v193, s[8:11], s65 offen lds\n"
"s_waitcnt lgkmcnt(2)\n"
"v_mfma_f32_16x16x32_bf16 v[24:27], v[208:211], v[156:159], v[24:27]\n"
"ds_read_b64_tr_b16 v[208:209], v195 offset:128\n"
"v_mfma_f32_16x16x32_bf16 v[4:7], v[212:215], v[144:147], v[4:7]\n"
"s_add_u32 m0, s47, 1024\n"
"s_add_u32 s65, s65, 14336\n"
"v_mfma_f32_16x16x32_bf16 v[12:15], v[212:215], v[148:151], v[12:15]\n"
"ds_read_b64_tr_b16 v[210:211], v195 offset:8576\n"
"v_mfma_f32_16x16x32_bf16 v[20:23], v[212:215], v[152:155], v[20:23]\n"
"buffer_load_dwordx4 v193, s[8:11], s65 offen lds\n"
"v_mfma_f32_16x16x32_bf16 v[28:31], v[212:215], v[156:159], v[28:31]\n"
"ds_read_b64_tr_b16 v[212:213], v195 offset:136\n"
"s_add_u32 s51, s51, 64\n"
"s_cmp_eq_u32 s51, 896\n"
"s_cselect_b32 s51, 0, s51\n"
"s_waitcnt lgkmcnt(3)\n"
"v_mfma_f32_16x16x32_bf16 v[32:35], v[216:219], v[144:147], v[32:35]\n"
"s_add_u32 m0, s44, 67584\n"
"v_mfma_f32_16x16x32_bf16 v[40:43], v[216:219], v[148:151], v[40:43]\n"
"ds_read_b64_tr_b16 v[214:215], v195 offset:8584\n"
"v_mfma_f32_16x16x32_bf16 v[48:51], v[216:219], v[152:155], v[48:51]\n"
"buffer_load_dwordx4 v248, s[12:15], s54 offen nt lds\n"
"v_mfma_f32_16x16x32_bf16 v[56:59], v[216:219], v[156:159], v[56:59]\n"
"ds_read_b64_tr_b16 v[216:217], v195 offset:192\n"
"v_mfma_f32_16x16x32_bf16 v[36:39], v[220:223], v[144:147], v[36:39]\n"
"s_add_u32 s47, s45, s49\n"
"v_mfma_f32_16x16x32_bf16 v[44:47], v[220:223], v[148:151], v[44:47]\n"
"ds_read_b64_tr_b16 v[218:219], v195 offset:8640\n"
"v_mfma_f32_16x16x32_bf16 v[52:55], v[220:223], v[152:155], v[52:55]\n"
"s_add_u32 m0, s44, 68128\n"
"v_mfma_f32_16x16x32_bf16 v[60:63], v[220:223], v[156:159], v[60:63]\n"
"ds_read_b64_tr_b16 v[220:221], v195 offset:200\n"
"s_waitcnt lgkmcnt(3)\n"
"v_mfma_f32_16x16x32_bf16 v[64:67], v[208:211], v[144:147], v[64:67]\n"
"s_add_u32 s49, s49, 8192\n"
"buffer_load_dwordx4 v248, s[12:15], s54 offen offset:512 nt lds\n"
"v_mfma_f32_16x16x32_bf16 v[72:75], v[208:211], v[148:151], v[72:75]\n"
"ds_read_b64_tr_b16 v[222:223], v195 offset:8648\n"
"v_mfma_f32_16x16x32_bf16 v[80:83], v[208:211], v[152:155], v[80:83]\n"
"s_add_u32 m0, s44, 68672\n"
"v_mfma_f32_16x16x32_bf16 v[88:91], v[208:211], v[156:159], v[88:91]\n"
"ds_read_b64_tr_b16 v[208:209], v195 offset:256\n"
"v_mfma_f32_16x16x32_bf16 v[68:71], v[212:215], v[144:147], v[68:71]\n"
"s_cmp_eq_u32 s49, 24576\n"
"s_cselect_b32 s49, 0, s49\n"
"buffer_load_dwordx4 v248, s[12:15], s54 offen offset:1024 nt lds\n"
"v_mfma_f32_16x16x32_bf16 v[76:79], v[212:215], v[148:151], v[76:79]\n"
"ds_read_b64_tr_b16 v[210:211], v195 offset:8704\n"
"v_mfma_f32_16x16x32_bf16 v[84:87], v[212:215], v[152:155], v[84:87]\n"
"v_mfma_f32_16x16x32_bf16 v[92:95], v[212:215], v[156:159], v[92:95]\n"
"ds_read_b64_tr_b16 v[212:213], v195 offset:264\n"
"s_waitcnt lgkmcnt(3)\n"
"v_mfma_f32_16x16x32_bf16 v[96:99], v[216:219], v[144:147], v[96:99]\n"
"v_add_u32 v198, s49, v196\n"
"s_add_u32 m0, s44, 69216\n"
"v_mfma_f32_16x16x32_bf16 v[104:107], v[216:219], v[148:151], v[104:107]\n"
"ds_read_b64_tr_b16 v[214:215], v195 offset:8712\n"
"v_mfma_f32_16x16x32_bf16 v[112:115], v[216:219], v[152:155], v[112:115]\n"
"buffer_load_dwordx4 v248, s[12:15], s54 offen offset:1536 nt lds\n"
"v_mfma_f32_16x16x32_bf16 v[120:123], v[216:219], v[156:159], v[120:123]\n"
"ds_read_b64_tr_b16 v[216:217], v195 offset:320\n"
"v_mfma_f32_16x16x32_bf16 v[100:103], v[220:223], v[144:147], v[100:103]\n"
"v_add_u32 v199, s49, v197\n"
"s_add_u32 m0, s44, 69760\n"
"v_mfma_f32_16x16x32_bf16 v[108:111], v[220:223], v[148:151], v[108:111]\n"
"ds_read_b64_tr_b16 v[218:219], v195 offset:8768\n"
"v_mfma_f32_16x16x32_bf16 v[116:119], v[220:223], v[152:155], v[116:119]\n"
"buffer_load_dwordx4 v248, s[12:15], s54 offen offset:2048 nt lds\n"
"v_mfma_f32_16x16x32_bf16 v[124:127], v[220:223], v[156:159], v[124:127]\n"
"ds_read_b64_tr_b16 v[220:221], v195 offset:328\n"
"s_waitcnt lgkmcnt(3)\n"
"v_mfma_f32_16x16x32_bf16 v[160:163], v[208:211], v[144:147], v[160:163]\n"
"s_add_u32 m0, s44, 70304\n"
"v_mfma_f32_16x16x32_bf16 v[168:171], v[208:211], v[148:151], v[168:171]\n"
"ds_read_b64_tr_b16 v[222:223], v195 offset:8776\n"
"v_mfma_f32_16x16x32_bf16 v[176:179], v[208:211], v[152:155], v[176:179]\n"
"buffer_load_dwordx4 v248, s[12:15], s54 offen offset:2560 nt lds\n"
"v_mfma_f32_16x16x32_bf16 v[184:187], v[208:211], v[156:159], v[184:187]\n"
"ds_read_b64_tr_b16 v[208:209], v195 offset:384\n"
"v_mfma_f32_16x16x32_bf16 v[164:167], v[212:215], v[144:147], v[164:167]\n"
"v_mfma_f32_16x16x32_bf16 v[172:175], v[212:215], v[148:151], v[172:175]\n"
"ds_read_b64_tr_b16 v[210:211], v195 offset:8832\n"
"v_mfma_f32_16x16x32_bf16 v[180:183], v[212:215], v[152:155], v[180:183]\n"
"s_add_u32 m0, s44, 70848\n"
"v_mfma_f32_16x16x32_bf16 v[188:191], v[212:215], v[156:159], v[188:191]\n"
"ds_read_b64_tr_b16 v[212:213], v195 offset:392\n"
"s_waitcnt lgkmcnt(3)\n"
"v_mfma_f32_16x16x32_bf16 a[160:163], v[216:219], v[144:147], a[160:163]\n"
"buffer_load_dwordx4 v248, s[12:15], s54 offen offset:3072 nt lds\n"
"v_mfma_f32_16x16x32_bf16 a[164:167], v[216:219], v[148:151], a[164:167]\n"
"ds_read_b64_tr_b16 v[214:215], v195 offset:8840\n"
"v_mfma_f32_16x16x32_bf16 a[168:171], v[216:219], v[152:155], a[168:171]\n"
"s_add_u32 m0, s44, 71392\n"
"v_mfma_f32_16x16x32_bf16 a[172:175], v[216:219], v[156:159], a[172:175]\n"
"ds_read_b64_tr_b16 v[216:217], v195 offset:448\n"
"v_mfma_f32_16x16x32_bf16 a[176:179], v[220:223], v[144:147], a[176:179]\n"
"buffer_load_dwordx4 v248, s[12:15], s54 offen offset:3584 nt lds\n"
"v_mfma_f32_16x16x32_bf16 a[180:183], v[220:223], v[148:151], a[180:183]\n"
"ds_read_b64_tr_b16 v[218:219], v195 offset:8896\n"
"s_add_u32 s54, s54, 16384\n"
"v_mfma_f32_16x16x32_bf16 a[184:187], v[220:223], v[152:155], a[184:187]\n"
"v_mfma_f32_16x16x32_bf16 a[188:191], v[220:223], v[156:159], a[188:191]\n"
"ds_read_b64_tr_b16 v[220:221], v195 offset:456\n"
"s_waitcnt lgkmcnt(5)\n"
"v_mfma_f32_16x16x32_bf16 a[192:195], v[208:211], v[144:147], a[192:195]\n"
"v_mfma_f32_16x16x32_bf16 a[196:199], v[208:211], v[148:151], a[196:199]\n"
"ds_read_b64_tr_b16 v[222:223], v195 offset:8904\n"
"v_mfma_f32_16x16x32_bf16 a[200:203], v[208:211], v[152:155], a[200:203]\n"
"s_add_u32 s46, s44, s48\n"
"v_mfma_f32_16x16x32_bf16 a[204:207], v[208:211], v[156:159], a[204:207]\n"
"s_add_u32 s48, s48, 33792\n"
"s_waitcnt lgkmcnt(4)\n"
"v_mfma_f32_16x16x32_bf16 a[208:211], v[212:215], v[144:147], a[208:211]\n"
"s_cmp_eq_u32 s48, 135168\n"
"s_cselect_b32 s48, 0, s48\n"
"v_mfma_f32_16x16x32_bf16 a[212:215], v[212:215], v[148:151], a[212:215]\n"
"v_add_u32 v195, s48, v194\n"
"v_mfma_f32_16x16x32_bf16 a[216:219], v[212:215], v[152:155], a[216:219]\n"
"s_waitcnt vmcnt(18) lgkmcnt(0)\n"
"s_barrier\n"
"ds_read_b64_tr_b16 v[208:209], v195 offset:0\n"
"v_mfma_f32_16x16x32_bf16 a[220:223], v[212:215], v[156:159], a[220:223]\n"
"ds_read_b64_tr_b16 v[210:211], v195 offset:8448\n"
"ds_read_b128 v[128:131], v198 offset:0\n"
"v_mfma_f32_16x16x32_bf16 a[224:227], v[216:219], v[144:147], a[224:227]\n"
"ds_read_b64_tr_b16 v[212:213], v195 offset:8\n"
"v_mfma_f32_16x16x32_bf16 a[228:231], v[216:219], v[148:151], a[228:231]\n"
"ds_read_b64_tr_b16 v[214:215], v195 offset:8456\n"
"ds_read_b128 v[132:135], v198 offset:1024\n"
"v_mfma_f32_16x16x32_bf16 a[232:235], v[216:219], v[152:155], a[232:235]\n"
"ds_read_b128 v[136:139], v199 offset:0\n"
"v_mfma_f32_16x16x32_bf16 a[236:239], v[216:219], v[156:159], a[236:239]\n"
"ds_read_b64_tr_b16 v[216:217], v195 offset:64\n"
"v_mfma_f32_16x16x32_bf16 a[240:243], v[220:223], v[144:147], a[240:243]\n"
"ds_read_b64_tr_b16 v[218:219], v195 offset:8512\n"
"v_mfma_f32_16x16x32_bf16 a[244:247], v[220:223], v[148:151], a[244:247]\n"
"ds_read_b128 v[140:143], v199 offset:1024\n"
"v_mfma_f32_16x16x32_bf16 a[248:251], v[220:223], v[152:155], a[248:251]\n"
"v_mfma_f32_16x16x32_bf16 a[252:255], v[220:223], v[156:159], a[252:255]\n"
"ds_read_b64_tr_b16 v[220:221], v195 offset:72\n"
"ds_read_b64_tr_b16 v[222:223], v195 offset:8520\n"
"s_waitcnt lgkmcnt(9)\n"
"v_mfma_f32_16x16x32_bf16 v[0:3], v[208:211], v[128:131], v[0:3]\n"
"s_add_u32 m0, s44, 101376\n"
"s_waitcnt lgkmcnt(6)\n"
"v_mfma_f32_16x16x32_bf16 v[8:11], v[208:211], v[132:135], v[8:11]\n"
"s_waitcnt lgkmcnt(5)\n"
"v_mfma_f32_16x16x32_bf16 v[16:19], v[208:211], v[136:139], v[16:19]\n"
"buffer_load_dwordx4 v248, s[12:15], s54 offen nt lds\n"
"s_waitcnt lgkmcnt(2)\n"
"v_mfma_f32_16x16x32_bf16 v[24:27], v[208:211], v[140:143], v[24:27]\n"
"ds_read_b64_tr_b16 v[208:209], v195 offset:128\n"
"v_mfma_f32_16x16x32_bf16 v[4:7], v[212:215], v[128:131], v[4:7]\n"
"v_not_b32 v203, v203\n"
"v_and_b32 v203, v203, v201\n"
"v_mfma_f32_16x16x32_bf16 v[12:15], v[212:215], v[132:135], v[12:15]\n"
"ds_read_b64_tr_b16 v[210:211], v195 offset:8576\n"
"v_readlane_b32 s71, v203, 0\n"
"v_mfma_f32_16x16x32_bf16 v[20:23], v[212:215], v[136:139], v[20:23]\n"
"s_add_u32 m0, s44, 101920\n"
"v_readlane_b32 s96, v203, 1\n"
"v_mfma_f32_16x16x32_bf16 v[28:31], v[212:215], v[140:143], v[28:31]\n"
"ds_read_b64_tr_b16 v[212:213], v195 offset:136\n"
"s_waitcnt lgkmcnt(3)\n"
"v_mfma_f32_16x16x32_bf16 v[32:35], v[216:219], v[128:131], v[32:35]\n"
"buffer_load_dwordx4 v248, s[12:15], s54 offen offset:512 nt lds\n"
"s_or_b32 s71, s71, s96\n"
"v_mfma_f32_16x16x32_bf16 v[40:43], v[216:219], v[132:135], v[40:43]\n"
"ds_read_b64_tr_b16 v[214:215], v195 offset:8584\n"
"v_readlane_b32 s96, v203, 2\n"
"v_mfma_f32_16x16x32_bf16 v[48:51], v[216:219], v[136:139], v[48:51]\n"
"s_or_b32 s71, s71, s96\n"
"v_readlane_b32 s96, v203, 3\n"
"v_mfma_f32_16x16x32_bf16 v[56:59], v[216:219], v[140:143], v[56:59]\n"
"ds_read_b64_tr_b16 v[216:217], v195 offset:192\n"
"v_mfma_f32_16x16x32_bf16 v[36:39], v[220:223], v[128:131], v[36:39]\n"
"s_add_u32 s47, s45, s49\n"
"s_add_u32 m0, s44, 102464\n"
"v_mfma_f32_16x16x32_bf16 v[44:47], v[220:223], v[132:135], v[44:47]\n"
"ds_read_b64_tr_b16 v[218:219], v195 offset:8640\n"
"s_or_b32 s71, s71, s96\n"
"v_mfma_f32_16x16x32_bf16 v[52:55], v[220:223], v[136:139], v[52:55]\n"
"buffer_load_dwordx4 v248, s[12:15], s54 offen offset:1024 nt lds\n"
"v_readlane_b32 s96, v203, 4\n"
"v_mfma_f32_16x16x32_bf16 v[60:63], v[220:223], v[140:143], v[60:63]\n"
"ds_read_b64_tr_b16 v[220:221], v195 offset:200\n"
"s_waitcnt lgkmcnt(3)\n"
"v_mfma_f32_16x16x32_bf16 v[64:67], v[208:211], v[128:131], v[64:67]\n"
"s_add_u32 s49, s49, 8192\n"
"s_or_b32 s71, s71, s96\n"
"v_mfma_f32_16x16x32_bf16 v[72:75], v[208:211], v[132:135], v[72:75]\n"
"ds_read_b64_tr_b16 v[222:223], v195 offset:8648\n"
"v_readlane_b32 s96, v203, 5\n"
"v_mfma_f32_16x16x32_bf16 v[80:83], v[208:211], v[136:139], v[80:83]\n"
"s_add_u32 m0, s44, 103008\n"
"s_or_b32 s71, s71, s96\n"
"v_mfma_f32_16x16x32_bf16 v[88:91], v[208:211], v[140:143], v[88:91]\n"
"ds_read_b64_tr_b16 v[208:209], v195 offset:256\n"
"v_mfma_f32_16x16x32_bf16 v[68:71], v[212:215], v[128:131], v[68:71]\n"
"s_cmp_eq_u32 s49, 24576\n"
"s_cselect_b32 s49, 0, s49\n"
"buffer_load_dwordx4 v248, s[12:15], s54 offen offset:1536 nt lds\n"
"v_mfma_f32_16x16x32_bf16 v[76:79], v[212:215], v[132:135], v[76:79]\n"
"ds_read_b64_tr_b16 v[210:211], v195 offset:8704\n"
"v_readlane_b32 s96, v203, 6\n"
"v_mfma_f32_16x16x32_bf16 v[84:87], v[212:215], v[136:139], v[84:87]\n"
"s_or_b32 s71, s71, s96\n"
"v_readlane_b32 s96, v203, 7\n"
"v_mfma_f32_16x16x32_bf16 v[92:95], v[212:215], v[140:143], v[92:95]\n"
"ds_read_b64_tr_b16 v[212:213], v195 offset:264\n"
"s_waitcnt lgkmcnt(3)\n"
"v_mfma_f32_16x16x32_bf16 v[96:99], v[216:219], v[128:131], v[96:99]\n"
"v_add_u32 v198, s49, v196\n"
"s_add_u32 m0, s44, 103552\n"
"v_mfma_f32_16x16x32_bf16 v[104:107], v[216:219], v[132:135], v[104:107]\n"
"ds_read_b64_tr_b16 v[214:215], v195 offset:8712\n"
"s_or_b32 s71, s71, s96\n"
"v_mfma_f32_16x16x32_bf16 v[112:115], v[216:219], v[136:139], v[112:115]\n"
"buffer_load_dwordx4 v248, s[12:15], s54 offen offset:2048 nt lds\n"
"v_mov_b32 v203, s71\n"
"v_mfma_f32_16x16x32_bf16 v[120:123], v[216:219], v[140:143], v[120:123]\n"
"ds_read_b64_tr_b16 v[216:217], v195 offset:320\n"
"v_mfma_f32_16x16x32_bf16 v[100:103], v[220:223], v[128:131], v[100:103]\n"
"v_add_u32 v199, s49, v197\n"
"v_mfma_f32_16x16x32_bf16 v[108:111], v[220:223], v[132:135], v[108:111]\n"
"ds_read_b64_tr_b16 v[218:219], v195 offset:8768\n"
"v_mfma_f32_16x16x32_bf16 v[116:119], v[220:223], v[136:139], v[116:119]\n"
"s_add_u32 m0, s44, 104096\n"
"v_mfma_f32_16x16x32_bf16 v[124:127], v[220:223], v[140:143], v[124:127]\n"
"ds_read_b64_tr_b16 v[220:221], v195 offset:328\n"
"s_waitcnt lgkmcnt(3)\n"
"v_mfma_f32_16x16x32_bf16 v[160:163], v[208:211], v[128:131], v[160:163]\n"
"buffer_load_dwordx4 v248, s[12:15], s54 offen offset:2560 nt lds\n"
"v_mfma_f32_16x16x32_bf16 v[168:171], v[208:211], v[132:135], v[168:171]\n"
"ds_read_b64_tr_b16 v[222:223], v195 offset:8776\n"
"v_mfma_f32_16x16x32_bf16 v[176:179], v[208:211], v[136:139], v[176:179]\n"
"s_cmp_lg_u32 s38, 0\n"
"s_cbranch_scc1 LHS0_%=\n"
"global_store_dword v202, v203, s[74:75] sc1\n"
"LHS0_%=:\n"
"v_mfma_f32_16x16x32_bf16 v[184:187], v[208:211], v[140:143], v[184:187]\n"
"ds_read_b64_tr_b16 v[208:209], v195 offset:384\n"
"v_mfma_f32_16x16x32_bf16 v[164:167], v[212:215], v[128:131], v[164:167]\n"
"s_add_u32 m0, s44, 104640\n"
"v_mfma_f32_16x16x32_bf16 v[172:175], v[212:215], v[132:135], v[172:175]\n"
"ds_read_b64_tr_b16 v[210:211], v195 offset:8832\n"
"v_mfma_f32_16x16x32_bf16 v[180:183], v[212:215], v[136:139], v[180:183]\n"
"buffer_load_dwordx4 v248, s[12:15], s54 offen offset:3072 nt lds\n"
"v_mfma_f32_16x16x32_bf16 v[188:191], v[212:215], v[140:143], v[188:191]\n"
"ds_read_b64_tr_b16 v[212:213], v195 offset:392\n"
"s_waitcnt lgkmcnt(3)\n"
"v_mfma_f32_16x16x32_bf16 a[160:163], v[216:219], v[128:131], a[160:163]\n"
"v_mfma_f32_16x16x32_bf16 a[164:167], v[216:219], v[132:135], a[164:167]\n"
"ds_read_b64_tr_b16 v[214:215], v195 offset:8840\n"
"v_mfma_f32_16x16x32_bf16 a[168:171], v[216:219], v[136:139], a[168:171]\n"
"s_add_u32 m0, s44, 105184\n"
"v_mfma_f32_16x16x32_bf16 a[172:175], v[216:219], v[140:143], a[172:175]\n"
"ds_read_b64_tr_b16 v[216:217], v195 offset:448\n"
"v_mfma_f32_16x16x32_bf16 a[176:179], v[220:223], v[128:131], a[176:179]\n"
"buffer_load_dwordx4 v248, s[12:15], s54 offen offset:3584 nt lds\n"
"v_mfma_f32_16x16x32_bf16 a[180:183], v[220:223], v[132:135], a[180:183]\n"
"ds_read_b64_tr_b16 v[218:219], v195 offset:8896\n"
"s_add_u32 s54, s54, 16384\n"
"v_mfma_f32_16x16x32_bf16 a[184:187], v[220:223], v[136:139], a[184:187]\n"
"v_mfma_f32_16x16x32_bf16 a[188:191], v[220:223], v[140:143], a[188:191]\n"
"ds_read_b64_tr_b16 v[220:221], v195 offset:456\n"
"s_waitcnt lgkmcnt(5)\n"
"v_mfma_f32_16x16x32_bf16 a[192:195], v[208:211], v[128:131], a[192:195]\n"
"v_mfma_f32_16x16x32_bf16 a[196:199], v[208:211], v[132:135], a[196:199]\n"
"ds_read_b64_tr_b16 v[222:223], v195 offset:8904\n"
"v_mfma_f32_16x16x32_bf16 a[200:203], v[208:211], v[136:139], a[200:203]\n"
"s_add_u32 s46, s44, s48\n"
"v_mfma_f32_16x16x32_bf16 a[204:207], v[208:211], v[140:143], a[204:207]\n"
"s_add_u32 s48, s48, 33792\n"
"s_waitcnt lgkmcnt(4)\n"
"v_mfma_f32_16x16x32_bf16 a[208:211], v[212:215], v[128:131], a[208:211]\n"
"s_cmp_eq_u32 s48, 135168\n"
"s_cselect_b32 s48, 0, s48\n"
"v_mfma_f32_16x16x32_bf16 a[212:215], v[212:215], v[132:135], a[212:215]\n"
"v_add_u32 v195, s48, v194\n"
"v_mfma_f32_16x16x32_bf16 a[216:219], v[212:215], v[136:139], a[216:219]\n"
"s_waitcnt vmcnt(16) lgkmcnt(0)\n"
"s_barrier\n"
"ds_read_b64_tr_b16 v[208:209], v195 offset:0\n"
"v_mfma_f32_16x16x32_bf16 a[220:223], v[212:215], v[140:143], a[220:223]\n"
"ds_read_b64_tr_b16 v[210:211], v195 offset:8448\n"
"ds_read_b128 v[144:147], v198 offset:0\n"
"v_mfma_f32_16x16x32_bf16 a[224:227], v[216:219], v[128:131], a[224:227]\n"
"ds_read_b64_tr_b16 v[212:213], v195 offset:8\n"
"v_mfma_f32_16x16x32_bf16 a[228:231], v[216:219], v[132:135], a[228:231]\n"
"ds_read_b64_tr_b16 v[214:215], v195 offset:8456\n"
"ds_read_b128 v[148:151], v198 offset:1024\n"
"v_mfma_f32_16x16x32_bf16 a[232:235], v[216:219], v[136:139], a[232:235]\n"
"ds_read_b128 v[152:155], v199 offset:0\n"
"v_mfma_f32_16x16x32_bf16 a[236:239], v[216:219], v[140:143], a[236:239]\n"
"ds_read_b64_tr_b16 v[216:217], v195 offset:64\n"
"v_mfma_f32_16x16x32_bf16 a[240:243], v[220:223], v[128:131], a[240:243]\n"
"ds_read_b64_tr_b16 v[218:219], v195 offset:8512\n"
"v_mfma_f32_16x16x32_bf16 a[244:247], v[220:223], v[132:135], a[244:247]\n"
"ds_read_b128 v[156:159], v199 offset:1024\n"
"v_mfma_f32_16x16x32_bf16 a[248:251], v[220:223], v[136:139], a[248:251]\n"
"v_mfma_f32_16x16x32_bf16 a[252:255], v[220:223], v[140:143], a[252:255]\n"
"ds_read_b64_tr_b16 v[220:221], v195 offset:72\n"
"ds_read_b64_tr_b16 v[222:223], v195 offset:8520\n"
"s_waitcnt lgkmcnt(9)\n"
"v_mfma_f32_16x16x32_bf16 v[0:3], v[208:211], v[144:147], v[0:3]\n"
"s_add_u32 m0, s44, 0\n"
"s_waitcnt lgkmcnt(6)\n"
"v_mfma_f32_16x16x32_bf16 v[8:11], v[208:211], v[148:151], v[8:11]\n"
"s_waitcnt lgkmcnt(5)\n"
"v_mfma_f32_16x16x32_bf16 v[16:19], v[208:211], v[152:155], v[16:19]\n"
"buffer_load_dwordx4 v249, s[16:19], s55 offen nt lds\n"
"s_waitcnt lgkmcnt(2)\n"
"v_mfma_f32_16x16x32_bf16 v[24:27], v[208:211], v[156:159], v[24:27]\n"
"ds_read_b64_tr_b16 v[208:209], v195 offset:128\n"
"v_mfma_f32_16x16x32_bf16 v[4:7], v[212:215], v[144:147], v[4:7]\n"
"v_mfma_f32_16x16x32_bf16 v[12:15], v[212:215], v[148:151], v[12:15]\n"
"ds_read_b64_tr_b16 v[210:211], v195 offset:8576\n"
"v_mfma_f32_16x16x32_bf16 v[20:23], v[212:215], v[152:155], v[20:23]\n"
"s_add_u32 m0, s44, 544\n"
"v_mfma_f32_16x16x32_bf16 v[28:31], v[212:215], v[156:159], v[28:31]\n"
"ds_read_b64_tr_b16 v[212:213], v195 offset:136\n"
"s_waitcnt lgkmcnt(3)\n"
"v_mfma_f32_16x16x32_bf16 v[32:35], v[216:219], v[144:147], v[32:35]\n"
"buffer_load_dwordx4 v249, s[16:19], s55 offen offset:512 nt lds\n"
"v_mfma_f32_16x16x32_bf16 v[40:43], v[216:219], v[148:151], v[40:43]\n"
"ds_read_b64_tr_b16 v[214:215], v195 offset:8584\n"
"v_mfma_f32_16x16x32_bf16 v[48:51], v[216:219], v[152:155], v[48:51]\n"
"v_mul_f32 v0, s62, v0\n"
"v_mul_f32 v1, s62, v1\n"
"v_mul_f32 v2, s62, v2\n"
"v_mul_f32 v3, s62, v3\n"
"v_mul_f32 v4, s62, v4\n"
"v_mul_f32 v5, s62, v5\n"
"v_mfma_f32_16x16x32_bf16 v[56:59], v[216:219], v[156:159], v[56:59]\n"
"ds_read_b64_tr_b16 v[216:217], v195 offset:192\n"
"v_mul_f32 v6, s62, v6\n"
"v_mul_f32 v7, s62, v7\n"
"v_cvt_pk_bf16_f32 v0, v0, v1\n"
"v_cvt_pk_bf16_f32 v1, v2, v3\n"
"v_mfma_f32_16x16x32_bf16 v[36:39], v[220:223], v[144:147], v[36:39]\n"
"s_add_u32 m0, s44, 1088\n"
"v_cvt_pk_bf16_f32 v2, v4, v5\n"
"v_cvt_pk_bf16_f32 v3, v6, v7\n"
"v_mul_f32 v8, s62, v8\n"
"v_mul_f32 v9, s62, v9\n"
"v_mul_f32 v10, s62, v10\n"
"v_mfma_f32_16x16x32_bf16 v[44:47], v[220:223], v[148:151], v[44:47]\n"
"ds_read_b64_tr_b16 v[218:219], v195 offset:8640\n"
"v_mul_f32 v11, s62, v11\n"
"v_mul_f32 v12, s62, v12\n"
"v_mul_f32 v13, s62, v13\n"
"v_mul_f32 v14, s62, v14\n"
"v_mul_f32 v15, s62, v15\n"
"v_mfma_f32_16x16x32_bf16 v[52:55], v[220:223], v[152:155], v[52:55]\n"
"buffer_load_dwordx4 v249, s[16:19], s55 offen offset:1024 nt lds\n"
"v_cvt_pk_bf16_f32 v4, v8, v9\n"
"v_cvt_pk_bf16_f32 v5, v10, v11\n"
"v_cvt_pk_bf16_f32 v6, v12, v13\n"
"v_cvt_pk_bf16_f32 v7, v14, v15\n"
"v_mul_f32 v16, s62, v16\n"
"v_mfma_f32_16x16x32_bf16 v[60:63], v[220:223], v[156:159], v[60:63]\n"
"ds_read_b64_tr_b16 v[220:221], v195 offset:200\n"
"v_mul_f32 v17, s62, v17\n"
"v_mul_f32 v18, s62, v18\n"
"v_mul_f32 v19, s62, v19\n"
"v_mul_f32 v20, s62, v20\n"
"s_waitcnt lgkmcnt(3)\n"
"v_mfma_f32_16x16x32_bf16 v[64:67], v[208:211], v[144:147], v[64:67]\n"
"v_mul_f32 v21, s62, v21\n"
"v_mul_f32 v22, s62, v22\n"
"v_mul_f32 v23, s62, v23\n"
"v_cvt_pk_bf16_f32 v8, v16, v17\n"
"v_cvt_pk_bf16_f32 v9, v18, v19\n"
"v_cvt_pk_bf16_f32 v10, v20, v21\n"
"v_mfma_f32_16x16x32_bf16 v[72:75], v[208:211], v[148:151], v[72:75]\n"
"ds_read_b64_tr_b16 v[222:223], v195 offset:8648\n"
"v_cvt_pk_bf16_f32 v11, v22, v23\n"
"v_mul_f32 v24, s62, v24\n"
"v_mul_f32 v25, s62, v25\n"
"v_mul_f32 v26, s62, v26\n"
"v_mul_f32 v27, s62, v27\n"
"v_mfma_f32_16x16x32_bf16 v[80:83], v[208:211], v[152:155], v[80:83]\n"
"s_add_u32 m0, s44, 1632\n"
"v_mul_f32 v28, s62, v28\n"
"v_mul_f32 v29, s62, v29\n"
"v_mul_f32 v30, s62, v30\n"
"v_mul_f32 v31, s62, v31\n"
"v_cvt_pk_bf16_f32 v12, v24, v25\n"
"v_mfma_f32_16x16x32_bf16 v[88:91], v[208:211], v[156:159], v[88:91]\n"
"ds_read_b64_tr_b16 v[208:209], v195 offset:256\n"
"v_cvt_pk_bf16_f32 v13, v26, v27\n"
"v_cvt_pk_bf16_f32 v14, v28, v29\n"
"v_cvt_pk_bf16_f32 v15, v30, v31\n"
"v_mul_f32 v32, s62, v32\n"
"v_mfma_f32_16x16x32_bf16 v[68:71], v[212:215], v[144:147], v[68:71]\n"
"buffer_load_dwordx4 v249, s[16:19], s55 offen offset:1536 nt lds\n"
"v_mul_f32 v33, s62, v33\n"
"v_mul_f32 v34, s62, v34\n"
"v_mul_f32 v35, s62, v35\n"
"v_mul_f32 v36, s62, v36\n"
"v_mul_f32 v37, s62, v37\n"
"v_mfma_f32_16x16x32_bf16 v[76:79], v[212:215], v[148:151], v[76:79]\n"
"ds_read_b64_tr_b16 v[210:211], v195 offset:8704\n"
"v_mul_f32 v38, s62, v38\n"
"v_mul_f32 v39, s62, v39\n"
"v_cvt_pk_bf16_f32 v16, v32, v33\n"
"v_cvt_pk_bf16_f32 v17, v34, v35\n"
"v_cvt_pk_bf16_f32 v18, v36, v37\n"
"v_mfma_f32_16x16x32_bf16 v[84:87], v[212:215], v[152:155], v[84:87]\n"
"v_cvt_pk_bf16_f32 v19, v38, v39\n"
"v_mul_f32 v40, s62, v40\n"
"v_mul_f32 v41, s62, v41\n"
"v_mul_f32 v42, s62, v42\n"
"v_mul_f32 v43, s62, v43\n"
"v_mul_f32 v44, s62, v44\n"
"v_mfma_f32_16x16x32_bf16 v[92:95], v[212:215], v[156:159], v[92:95]\n"
"ds_read_b64_tr_b16 v[212:213], v195 offset:264\n"
"v_mul_f32 v45, s62, v45\n"
"v_mul_f32 v46, s62, v46\n"
"v_mul_f32 v47, s62, v47\n"
"v_cvt_pk_bf16_f32 v20, v40, v41\n"
"s_waitcnt lgkmcnt(3)\n"
"v_mfma_f32_16x16x32_bf16 v[96:99], v[216:219], v[144:147], v[96:99]\n"
"s_add_u32 m0, s44, 2176\n"
"v_cvt_pk_bf16_f32 v21, v42, v43\n"
"v_cvt_pk_bf16_f32 v22, v44, v45\n"
"v_cvt_pk_bf16_f32 v23, v46, v47\n"
"v_mul_f32 v48, s62, v48\n"
"v_mul_f32 v49, s62, v49\n"
"v_mfma_f32_16x16x32_bf16 v[104:107], v[216:219], v[148:151], v[104:107]\n"
"ds_read_b64_tr_b16 v[214:215], v195 offset:8712\n"
"v_mul_f32 v50, s62, v50\n"
"v_mul_f32 v51, s62, v51\n"
"v_mul_f32 v52, s62, v52\n"
"v_mul_f32 v53, s62, v53\n"
"v_mul_f32 v54, s62, v54\n"
"v_mfma_f32_16x16x32_bf16 v[112:115], v[216:219], v[152:155], v[112:115]\n"
"buffer_load_dwordx4 v249, s[16:19], s55 offen offset:2048 nt lds\n"
"v_mul_f32 v55, s62, v55\n"
"v_cvt_pk_bf16_f32 v24, v48, v49\n"
"v_cvt_pk_bf16_f32 v25, v50, v51\n"
"v_cvt_pk_bf16_f32 v26, v52, v53\n"
"v_cvt_pk_bf16_f32 v27, v54, v55\n"
"v_mfma_f32_16x16x32_bf16 v[120:123], v[216:219], v[156:159], v[120:123]\n"
"ds_read_b64_tr_b16 v[216:217], v195 offset:320\n"
"v_mul_f32 v56, s62, v56\n"
"v_mul_f32 v57, s62, v57\n"
"v_mul_f32 v58, s62, v58\n"
"v_mul_f32 v59, s62, v59\n"
"v_mfma_f32_16x16x32_bf16 v[100:103], v[220:223], v[144:147], v[100:103]\n"
"v_mul_f32 v60, s62, v60\n"
"v_mul_f32 v61, s62, v61\n"
"v_mul_f32 v62, s62, v62\n"
"v_mul_f32 v63, s62, v63\n"
"v_cvt_pk_bf16_f32 v28, v56, v57\n"
"v_cvt_pk_bf16_f32 v29, v58, v59\n"
"v_mfma_f32_16x16x32_bf16 v[108:111], v[220:223], v[148:151], v[108:111]\n"
"ds_read_b64_tr_b16 v[218:219], v195 offset:8768\n"
"v_cvt_pk_bf16_f32 v30, v60, v61\n"
"v_cvt_pk_bf16_f32 v31, v62, v63\n"
"v_mul_f32 v64, s62, v64\n"
"v_mul_f32 v65, s62, v65\n"
"v_mul_f32 v66, s62, v66\n"
"v_mfma_f32_16x16x32_bf16 v[116:119], v[220:223], v[152:155], v[116:119]\n"
"s_add_u32 m0, s44, 2720\n"
"v_mul_f32 v67, s62, v67\n"
"v_mul_f32 v68, s62, v68\n"
"v_mul_f32 v69, s62, v69\n"
"v_mul_f32 v70, s62, v70\n"
"v_mul_f32 v71, s62, v71\n"
"v_mfma_f32_16x16x32_bf16 v[124:127], v[220:223], v[156:159], v[124:127]\n"
"ds_read_b64_tr_b16 v[220:221], v195 offset:328\n"
"v_cvt_pk_bf16_f32 v32, v64, v65\n"
"v_cvt_pk_bf16_f32 v33, v66, v67\n"
"v_cvt_pk_bf16_f32 v34, v68, v69\n"
"v_cvt_pk_bf16_f32 v35, v70, v71\n"
"s_waitcnt lgkmcnt(3)\n"
"v_mfma_f32_16x16x32_bf16 v[160:163], v[208:211], v[144:147], v[160:163]\n"
"buffer_load_dwordx4 v249, s[16:19], s55 offen offset:2560 nt lds\n"
"v_mul_f32 v72, s62, v72\n"
"v_mul_f32 v73, s62, v73\n"
"v_mul_f32 v74, s62, v74\n"
"v_mul_f32 v75, s62, v75\n"
"v_mul_f32 v76, s62, v76\n"
"v_mfma_f32_16x16x32_bf16 v[168:171], v[208:211], v[148:151], v[168:171]\n"
"ds_read_b64_tr_b16 v[222:223], v195 offset:8776\n"
"v_mul_f32 v77, s62, v77\n"
"v_mul_f32 v78, s62, v78\n"
"v_mul_f32 v79, s62, v79\n"
"v_cvt_pk_bf16_f32 v36, v72, v73\n"
"v_cvt_pk_bf16_f32 v37, v74, v75\n"
"v_mfma_f32_16x16x32_bf16 v[176:179], v[208:211], v[152:155], v[176:179]\n"
"v_cvt_pk_bf16_f32 v38, v76, v77\n"
"v_cvt_pk_bf16_f32 v39, v78, v79\n"
"v_mul_f32 v80, s62, v80\n"
"v_mul_f32 v81, s62, v81\n"
"v_mul_f32 v82, s62, v82\n"
"v_mul_f32 v83, s62, v83\n"
"v_mfma_f32_16x16x32_bf16 v[184:187], v[208:211], v[156:159], v[184:187]\n"
"ds_read_b64_tr_b16 v[208:209], v195 offset:384\n"
"v_mul_f32 v84, s62, v84\n"
"v_mul_f32 v85, s62, v85\n"
"v_mul_f32 v86, s62, v86\n"
"v_mul_f32 v87, s62, v87\n"
"v_mfma_f32_16x16x32_bf16 v[164:167], v[212:215], v[144:147], v[164:167]\n"
"s_add_u32 m0, s44, 3264\n"
"v_cvt_pk_bf16_f32 v40, v80, v81\n"
"v_cvt_pk_bf16_f32 v41, v82, v83\n"
"v_cvt_pk_bf16_f32 v42, v84, v85\n"
"v_cvt_pk_bf16_f32 v43, v86, v87\n"
"v_mul_f32 v88, s62, v88\n"
"v_mfma_f32_16x16x32_bf16 v[172:175], v[212:215], v[148:151], v[172:175]\n"
"ds_read_b64_tr_b16 v[210:211], v195 offset:8832\n"
"v_mul_f32 v89, s62, v89\n"
"v_mul_f32 v90, s62, v90\n"
"v_mul_f32 v91, s62, v91\n"
"v_mul_f32 v92, s62, v92\n"
"v_mul_f32 v93, s62, v93\n"
"v_mfma_f32_16x16x32_bf16 v[180:183], v[212:215], v[152:155], v[180:183]\n"
"buffer_load_dwordx4 v249, s[16:19], s55 offen offset:3072 nt lds\n"
"v_mul_f32 v94, s62, v94\n"
"v_mul_f32 v95, s62, v95\n"
"v_cvt_pk_bf16_f32 v44, v88, v89\n"
"v_cvt_pk_bf16_f32 v45, v90, v91\n"
"v_cvt_pk_bf16_f32 v46, v92, v93\n"
"v_mfma_f32_16x16x32_bf16 v[188:191], v[212:215], v[156:159], v[188:191]\n"
"ds_read_b64_tr_b16 v[212:213], v195 offset:392\n"
"v_cvt_pk_bf16_f32 v47, v94, v95\n"
"v_mul_f32 v96, s62, v96\n"
"v_mul_f32 v97, s62, v97\n"
"v_mul_f32 v98, s62, v98\n"
"s_waitcnt lgkmcnt(3)\n"
"v_mfma_f32_16x16x32_bf16 a[160:163], v[216:219], v[144:147], a[160:163]\n"
"v_mul_f32 v99, s62, v99\n"
"v_mul_f32 v100, s62, v100\n"
"v_mul_f32 v101, s62, v101\n"
"v_mul_f32 v102, s62, v102\n"
"v_mul_f32 v103, s62, v103\n"
"v_cvt_pk_bf16_f32 v48, v96, v97\n"
"v_mfma_f32_16x16x32_bf16 a[164:167], v[216:219], v[148:151], a[164:167]\n"
"ds_read_b64_tr_b16 v[214:215], v195 offset:8840\n"
"v_cvt_pk_bf16_f32 v49, v98, v99\n"
"v_cvt_pk_bf16_f32 v50, v100, v101\n"
"v_cvt_pk_bf16_f32 v51, v102, v103\n"
"v_mul_f32 v104, s62, v104\n"
"v_mul_f32 v105, s62, v105\n"
"v_mfma_f32_16x16x32_bf16 a[168:171], v[216:219], v[152:155], a[168:171]\n"
"s_add_u32 m0, s44, 3808\n"
"v_mul_f32 v106, s62, v106\n"
"v_mul_f32 v107, s62, v107\n"
"v_mul_f32 v108, s62, v108\n"
"v_mul_f32 v109, s62, v109\n"
"v_mul_f32 v110, s62, v110\n"
"v_mfma_f32_16x16x32_bf16 a[172:175], v[216:219], v[156:159], a[172:175]\n"
"ds_read_b64_tr_b16 v[216:217], v195 offset:448\n"
"v_mul_f32 v111, s62, v111\n"
"v_cvt_pk_bf16_f32 v52, v104, v105\n"
"v_cvt_pk_bf16_f32 v53, v106, v107\n"
"v_cvt_pk_bf16_f32 v54, v108, v109\n"
"v_mfma_f32_16x16x32_bf16 a[176:179], v[220:223], v[144:147], a[176:179]\n"
"buffer_load_dwordx4 v249, s[16:19], s55 offen offset:3584 nt lds\n"
"v_cvt_pk_bf16_f32 v55, v110, v111\n"
"v_mul_f32 v112, s62, v112\n"
"v_mul_f32 v113, s62, v113\n"
"v_mul_f32 v114, s62, v114\n"
"v_mul_f32 v115, s62, v115\n"
"v_mfma_f32_16x16x32_bf16 a[180:183], v[220:223], v[148:151], a[180:183]\n"
"ds_read_b64_tr_b16 v[218:219], v195 offset:8896\n"
"s_add_u32 s55, s55, 16384\n"
"v_mul_f32 v116, s62, v116\n"
"v_mul_f32 v117, s62, v117\n"
"v_mul_f32 v118, s62, v118\n"
"v_mul_f32 v119, s62, v119\n"
"v_mfma_f32_16x16x32_bf16 a[184:187], v[220:223], v[152:155], a[184:187]\n"
"v_cvt_pk_bf16_f32 v56, v112, v113\n"
"v_cvt_pk_bf16_f32 v57, v114, v115\n"
"v_cvt_pk_bf16_f32 v58, v116, v117\n"
"v_cvt_pk_bf16_f32 v59, v118, v119\n"
"v_mul_f32 v120, s62, v120\n"
"v_mul_f32 v121, s62, v121\n"
"v_mfma_f32_16x16x32_bf16 a[188:191], v[220:223], v[156:159], a[188:191]\n"
"ds_read_b64_tr_b16 v[220:221], v195 offset:456\n"
"v_mul_f32 v122, s62, v122\n"
"v_mul_f32 v123, s62, v123\n"
"v_mul_f32 v124, s62, v124\n"
"v_mul_f32 v125, s62, v125\n"
"s_waitcnt lgkmcnt(5)\n"
"v_mfma_f32_16x16x32_bf16 a[192:195], v[208:211], v[144:147], a[192:195]\n"
"v_mul_f32 v126, s62, v126\n"
"v_mul_f32 v127, s62, v127\n"
"v_cvt_pk_bf16_f32 v60, v120, v121\n"
"v_cvt_pk_bf16_f32 v61, v122, v123\n"
"v_cvt_pk_bf16_f32 v62, v124, v125\n"
"v_cvt_pk_bf16_f32 v63, v126, v127\n"
"v_mfma_f32_16x16x32_bf16 a[196:199], v[208:211], v[148:151], a[196:199]\n"
"ds_read_b64_tr_b16 v[222:223], v195 offset:8904\n"
"v_mul_f32 v160, s62, v160\n"
"v_mul_f32 v161, s62, v161\n"
"v_mul_f32 v162, s62, v162\n"
"v_mul_f32 v163, s62, v163\n"
"v_mul_f32 v164, s62, v164\n"
"v_mfma_f32_16x16x32_bf16 a[200:203], v[208:211], v[152:155], a[200:203]\n"
"v_mul_f32 v165, s62, v165\n"
"v_mul_f32 v166, s62, v166\n"
"v_mul_f32 v167, s62, v167\n"
"v_cvt_pk_bf16_f32 v64, v160, v161\n"
"v_cvt_pk_bf16_f32 v65, v162, v163\n"
"v_cvt_pk_bf16_f32 v66, v164, v165\n"
"v_mfma_f32_16x16x32_bf16 a[204:207], v[208:211], v[156:159], a[204:207]\n"
"v_cvt_pk_bf16_f32 v67, v166, v167\n"
"v_mul_f32 v168, s62, v168\n"
"v_mul_f32 v169, s62, v169\n"
"v_mul_f32 v170, s62, v170\n"
"v_mul_f32 v171, s62, v171\n"
"s_waitcnt lgkmcnt(4)\n"
"v_mfma_f32_16x16x32_bf16 a[208:211], v[212:215], v[144:147], a[208:211]\n"
"v_mul_f32 v172, s62, v172\n"
"v_mul_f32 v173, s62, v173\n"
"v_mul_f32 v174, s62, v174\n"
"v_mul_f32 v175, s62, v175\n"
"v_cvt_pk_bf16_f32 v68, v168, v169\n"
"v_cvt_pk_bf16_f32 v69, v170, v171\n"
"v_mfma_f32_16x16x32_bf16 a[212:215], v[212:215], v[148:151], a[212:215]\n"
"v_cvt_pk_bf16_f32 v70, v172, v173\n"
"v_cvt_pk_bf16_f32 v71, v174, v175\n"
"v_mul_f32 v176, s62, v176\n"
"v_mul_f32 v177, s62, v177\n"
"v_mul_f32 v178, s62, v178\n"
"v_mul_f32 v179, s62, v179\n"
"v_mfma_f32_16x16x32_bf16 a[216:219], v[212:215], v[152:155], a[216:219]\n"
"s_waitcnt vmcnt(16) lgkmcnt(0)\n"
"s_barrier\n"
"ds_read_b128 v[208:211], v245 offset:0\n"
"v_mul_f32 v180, s62, v180\n"
"v_mul_f32 v181, s62, v181\n"
"v_mul_f32 v182, s62, v182\n"
"v_mul_f32 v183, s62, v183\n"
"v_mfma_f32_16x16x32_bf16 a[220:223], v[212:215], v[156:159], a[220:223]\n"
"v_cvt_pk_bf16_f32 v72, v176, v177\n"
"v_cvt_pk_bf16_f32 v73, v178, v179\n"
"v_cvt_pk_bf16_f32 v74, v180, v181\n"
"v_cvt_pk_bf16_f32 v75, v182, v183\n"
"v_mul_f32 v184, s62, v184\n"
"v_mfma_f32_16x16x32_bf16 a[224:227], v[216:219], v[144:147], a[224:227]\n"
"ds_read_b128 v[212:215], v245 offset:512\n"
"v_mul_f32 v185, s62, v185\n"
"v_mul_f32 v186, s62, v186\n"
"v_mul_f32 v187, s62, v187\n"
"v_mul_f32 v188, s62, v188\n"
"v_mul_f32 v189, s62, v189\n"
"v_mfma_f32_16x16x32_bf16 a[228:231], v[216:219], v[148:151], a[228:231]\n"
"v_mul_f32 v190, s62, v190\n"
"v_mul_f32 v191, s62, v191\n"
"v_cvt_pk_bf16_f32 v76, v184, v185\n"
"v_cvt_pk_bf16_f32 v77, v186, v187\n"
"v_cvt_pk_bf16_f32 v78, v188, v189\n"
"v_cvt_pk_bf16_f32 v79, v190, v191\n"
"v_mfma_f32_16x16x32_bf16 a[232:235], v[216:219], v[152:155], a[232:235]\n"
"v_accvgpr_read_b32 v160, a160\n"
"v_accvgpr_read_b32 v161, a161\n"
"v_accvgpr_read_b32 v162, a162\n"
"v_accvgpr_read_b32 v163, a163\n"
"v_accvgpr_read_b32 v164, a176\n"
"v_accvgpr_read_b32 v165, a177\n"
"v_mfma_f32_16x16x32_bf16 a[236:239], v[216:219], v[156:159], a[236:239]\n"
"ds_read_b128 v[216:219], v245 offset:64\n"
"v_accvgpr_read_b32 v166, a178\n"
"v_accvgpr_read_b32 v167, a179\n"
"v_mul_f32 v160, s62, v160\n"
"v_mul_f32 v161, s62, v161\n"
"v_mfma_f32_16x16x32_bf16 a[240:243], v[220:223], v[144:147], a[240:243]\n"
"v_mul_f32 v162, s62, v162\n"
"v_mul_f32 v163, s62, v163\n"
"v_mul_f32 v164, s62, v164\n"
"v_mul_f32 v165, s62, v165\n"
"v_mul_f32 v166, s62, v166\n"
"v_mul_f32 v167, s62, v167\n"
"v_mfma_f32_16x16x32_bf16 a[244:247], v[220:223], v[148:151], a[244:247]\n"
"v_cvt_pk_bf16_f32 v80, v160, v161\n"
"v_cvt_pk_bf16_f32 v81, v162, v163\n"
"v_cvt_pk_bf16_f32 v82, v164, v165\n"
"v_cvt_pk_bf16_f32 v83, v166, v167\n"
"v_accvgpr_read_b32 v168, a164\n"
"v_accvgpr_read_b32 v169, a165\n"
"v_mfma_f32_16x16x32_bf16 a[248:251], v[220:223], v[152:155], a[248:251]\n"
"v_accvgpr_read_b32 v170, a166\n"
"v_accvgpr_read_b32 v171, a167\n"
"v_accvgpr_read_b32 v172, a180\n"
"v_accvgpr_read_b32 v173, a181\n"
"v_accvgpr_read_b32 v174, a182\n"
"v_accvgpr_read_b32 v175, a183\n"
"v_mfma_f32_16x16x32_bf16 a[252:255], v[220:223], v[156:159], a[252:255]\n"
"ds_read_b128 v[220:223], v245 offset:576\n"
"v_mul_f32 v168, s62, v168\n"
"v_mul_f32 v169, s62, v169\n"
"v_mul_f32 v170, s62, v170\n"
"v_mul_f32 v171, s62, v171\n"
"s_waitcnt lgkmcnt(3)\n"
"v_mfma_f32_16x16x32_bf16 v[128:131], v[208:211], v[0:3], 0\n"
"s_add_u32 m0, s44, 33792\n"
"v_mul_f32 v172, s62, v172\n"
"v_mul_f32 v173, s62, v173\n"
"v_mul_f32 v174, s62, v174\n"
"v_mul_f32 v175, s62, v175\n"
"v_cvt_pk_bf16_f32 v84, v168, v169\n"
"v_mfma_f32_16x16x32_bf16 v[136:139], v[208:211], v[4:7], 0\n"
"v_cvt_pk_bf16_f32 v85, v170, v171\n"
"v_cvt_pk_bf16_f32 v86, v172, v173\n"
"v_cvt_pk_bf16_f32 v87, v174, v175\n"
"v_accvgpr_read_b32 v176, a168\n"
"v_accvgpr_read_b32 v177, a169\n"
"v_accvgpr_read_b32 v178, a170\n"
"v_mfma_f32_16x16x32_bf16 v[144:147], v[208:211], v[8:11], 0\n"
"buffer_load_dwordx4 v248, s[12:15], s54 offen nt lds\n"
"v_accvgpr_read_b32 v179, a171\n"
"v_accvgpr_read_b32 v180, a184\n"
"v_accvgpr_read_b32 v181, a185\n"
"v_accvgpr_read_b32 v182, a186\n"
"v_accvgpr_read_b32 v183, a187\n"
"v_mfma_f32_16x16x32_bf16 v[152:155], v[208:211], v[12:15], 0\n"
"ds_read_b128 v[208:211], v245 offset:128\n"
"v_mul_f32 v176, s62, v176\n"
"v_mul_f32 v177, s62, v177\n"
"v_mul_f32 v178, s62, v178\n"
"v_mul_f32 v179, s62, v179\n"
"s_waitcnt lgkmcnt(3)\n"
"v_mfma_f32_16x16x32_bf16 v[132:135], v[212:215], v[0:3], 0\n"
"v_mul_f32 v180, s62, v180\n"
"v_mul_f32 v181, s62, v181\n"
"v_mul_f32 v182, s62, v182\n"
"v_mul_f32 v183, s62, v183\n"
"v_cvt_pk_bf16_f32 v88, v176, v177\n"
"v_cvt_pk_bf16_f32 v89, v178, v179\n"
"v_mfma_f32_16x16x32_bf16 v[140:143], v[212:215], v[4:7], 0\n"
"v_cvt_pk_bf16_f32 v90, v180, v181\n"
"v_cvt_pk_bf16_f32 v91, v182, v183\n"
"v_accvgpr_read_b32 v184, a172\n"
"v_accvgpr_read_b32 v185, a173\n"
"v_accvgpr_read_b32 v186, a174\n"
"v_accvgpr_read_b32 v187, a175\n"
"v_mfma_f32_16x16x32_bf16 v[148:151], v[212:215], v[8:11], 0\n"
"s_add_u32 m0, s44, 34336\n"
"v_accvgpr_read_b32 v188, a188\n"
"v_accvgpr_read_b32 v189, a189\n"
"v_accvgpr_read_b32 v190, a190\n"
"v_accvgpr_read_b32 v191, a191\n"
"v_mul_f32 v184, s62, v184\n"
"v_mfma_f32_16x16x32_bf16 v[156:159], v[212:215], v[12:15], 0\n"
"ds_read_b128 v[212:215], v245 offset:640\n"
"v_mul_f32 v185, s62, v185\n"
"v_mul_f32 v186, s62, v186\n"
"v_mul_f32 v187, s62, v187\n"
"v_mul_f32 v188, s62, v188\n"
"s_waitcnt lgkmcnt(2)\n"
"v_mfma_f32_16x16x32_bf16 v[128:131], v[216:219], v[16:19], v[128:131]\n"
"buffer_load_dwordx4 v248, s[12:15], s54 offen offset:512 nt lds\n"
"v_mul_f32 v189, s62, v189\n"
"v_mul_f32 v190, s62, v190\n"
"v_mul_f32 v191, s62, v191\n"
"v_cvt_pk_bf16_f32 v92, v184, v185\n"
"v_cvt_pk_bf16_f32 v93, v186, v187\n"
"v_mfma_f32_16x16x32_bf16 v[136:139], v[216:219], v[20:23], v[136:139]\n"
"v_cvt_pk_bf16_f32 v94, v188, v189\n"
"v_cvt_pk_bf16_f32 v95, v190, v191\n"
"v_accvgpr_read_b32 v160, a192\n"
"v_accvgpr_read_b32 v161, a193\n"
"v_accvgpr_read_b32 v162, a194\n"
"v_accvgpr_read_b32 v163, a195\n"
"v_mfma_f32_16x16x32_bf16 v[144:147], v[216:219], v[24:27], v[144:147]\n"
"v_accvgpr_read_b32 v164, a208\n"
"v_accvgpr_read_b32 v165, a209\n"
"v_accvgpr_read_b32 v166, a210\n"
"v_accvgpr_read_b32 v167, a211\n"
"v_mul_f32 v160, s62, v160\n"
"v_mul_f32 v161, s62, v161\n"
"v_mfma_f32_16x16x32_bf16 v[152:155], v[216:219], v[28:31], v[152:155]\n"
"ds_read_b128 v[216:219], v245 offset:192\n"
"v_mul_f32 v162, s62, v162\n"
"v_mul_f32 v163, s62, v163\n"
"v_mul_f32 v164, s62, v164\n"
"v_mul_f32 v165, s62, v165\n"
"v_mfma_f32_16x16x32_bf16 v[132:135], v[220:223], v[16:19], v[132:135]\n"
"s_add_u32 m0, s44, 34880\n"
"v_mul_f32 v166, s62, v166\n"
"v_mul_f32 v167, s62, v167\n"
"v_cvt_pk_bf16_f32 v96, v160, v161\n"
"v_cvt_pk_bf16_f32 v97, v162, v163\n"
"v_cvt_pk_bf16_f32 v98, v164, v165\n"
"v_mfma_f32_16x16x32_bf16 v[140:143], v[220:223], v[20:23], v[140:143]\n"
"v_cvt_pk_bf16_f32 v99, v166, v167\n"
"v_accvgpr_read_b32 v168, a196\n"
"v_accvgpr_read_b32 v169, a197\n"
"v_accvgpr_read_b32 v170, a198\n"
"v_accvgpr_read_b32 v171, a199\n"
"v_accvgpr_read_b32 v172, a212\n"
"v_mfma_f32_16x16x32_bf16 v[148:151], v[220:223], v[24:27], v[148:151]\n"
"buffer_load_dwordx4 v248, s[12:15], s54 offen offset:1024 nt lds\n"
"v_accvgpr_read_b32 v173, a213\n"
"v_accvgpr_read_b32 v174, a214\n"
"v_accvgpr_read_b32 v175, a215\n"
"v_mul_f32 v168, s62, v168\n"
"v_mul_f32 v169, s62, v169\n"
"v_mfma_f32_16x16x32_bf16 v[156:159], v[220:223], v[28:31], v[156:159]\n"
"ds_read_b128 v[220:223], v245 offset:704\n"
"v_mul_f32 v170, s62, v170\n"
"v_mul_f32 v171, s62, v171\n"
"v_mul_f32 v172, s62, v172\n"
"v_mul_f32 v173, s62, v173\n"
"s_waitcnt lgkmcnt(2)\n"
"v_mfma_f32_16x16x32_bf16 v[128:131], v[208:211], v[32:35], v[128:131]\n"
"v_mul_f32 v174, s62, v174\n"
"v_mul_f32 v175, s62, v175\n"
"v_cvt_pk_bf16_f32 v100, v168, v169\n"
"v_cvt_pk_bf16_f32 v101, v170, v171\n"
"v_cvt_pk_bf16_f32 v102, v172, v173\n"
"v_cvt_pk_bf16_f32 v103, v174, v175\n"
"v_mfma_f32_16x16x32_bf16 v[136:139], v[208:211], v[36:39], v[136:139]\n"
"v_accvgpr_read_b32 v176, a200\n"
"v_accvgpr_read_b32 v177, a201\n"
"v_accvgpr_read_b32 v178, a202\n"
"v_accvgpr_read_b32 v179, a203\n"
"v_accvgpr_read_b32 v180, a216\n"
"v_accvgpr_read_b32 v181, a217\n"
"v_mfma_f32_16x16x32_bf16 v[144:147], v[208:211], v[40:43], v[144:147]\n"
"s_add_u32 m0, s44, 35424\n"
"v_accvgpr_read_b32 v182, a218\n"
"v_accvgpr_read_b32 v183, a219\n"
"v_mul_f32 v176, s62, v176\n"
"v_mul_f32 v177, s62, v177\n"
"v_mul_f32 v178, s62, v178\n"
"v_mfma_f32_16x16x32_bf16 v[152:155], v[208:211], v[44:47], v[152:155]\n"
"ds_read_b128 v[208:211], v245 offset:256\n"
"v_mul_f32 v179, s62, v179\n"
"v_mul_f32 v180, s62, v180\n"
"v_mul_f32 v181, s62, v181\n"
"v_mul_f32 v182, s62, v182\n"
"v_mfma_f32_16x16x32_bf16 v[132:135], v[212:215], v[32:35], v[132:135]\n"
"buffer_load_dwordx4 v248, s[12:15], s54 offen offset:1536 nt lds\n"
"v_mul_f32 v183, s62, v183\n"
"v_cvt_pk_bf16_f32 v104, v176, v177\n"
"v_cvt_pk_bf16_f32 v105, v178, v179\n"
"v_cvt_pk_bf16_f32 v106, v180, v181\n"
"v_cvt_pk_bf16_f32 v107, v182, v183\n"
"v_mfma_f32_16x16x32_bf16 v[140:143], v[212:215], v[36:39], v[140:143]\n"
"v_accvgpr_read_b32 v184, a204\n"
"v_accvgpr_read_b32 v185, a205\n"
"v_accvgpr_read_b32 v186, a206\n"
"v_accvgpr_read_b32 v187, a207\n"
"v_accvgpr_read_b32 v188, a220\n"
"v_accvgpr_read_b32 v189, a221\n"
"v_mfma_f32_16x16x32_bf16 v[148:151], v[212:215], v[40:43], v[148:151]\n"
"v_accvgpr_read_b32 v190, a222\n"
"v_accvgpr_read_b32 v191, a223\n"
"v_mul_f32 v184, s62, v184\n"
"v_mul_f32 v185, s62, v185\n"
"v_mul_f32 v186, s62, v186\n"
"v_mul_f32 v187, s62, v187\n"
"v_mfma_f32_16x16x32_bf16 v[156:159], v[212:215], v[44:47], v[156:159]\n"
"ds_read_b128 v[212:215], v245 offset:768\n"
"v_mul_f32 v188, s62, v188\n"
"v_mul_f32 v189, s62, v189\n"
"v_mul_f32 v190, s62, v190\n"
"v_mul_f32 v191, s62, v191\n"
"s_waitcnt lgkmcnt(2)\n"
"v_mfma_f32_16x16x32_bf16 v[128:131], v[216:219], v[48:51], v[128:131]\n"
"s_add_u32 m0, s44, 35968\n"
"v_cvt_pk_bf16_f32 v108, v184, v185\n"
"v_cvt_pk_bf16_f32 v109, v186, v187\n"
"v_cvt_pk_bf16_f32 v110, v188, v189\n"
"v_cvt_pk_bf16_f32 v111, v190, v191\n"
"v_accvgpr_read_b32 v160, a224\n"
"v_mfma_f32_16x16x32_bf16 v[136:139], v[216:219], v[52:55], v[136:139]\n"
"v_accvgpr_read_b32 v161, a225\n"
"v_accvgpr_read_b32 v162, a226\n"
"v_accvgpr_read_b32 v163, a227\n"
"v_accvgpr_read_b32 v164, a240\n"
"v_accvgpr_read_b32 v165, a241\n"
"v_accvgpr_read_b32 v166, a242\n"
"v_mfma_f32_16x16x32_bf16 v[144:147], v[216:219], v[56:59], v[144:147]\n"
"buffer_load_dwordx4 v248, s[12:15], s54 offen offset:2048 nt lds\n"
"v_accvgpr_read_b32 v167, a243\n"
"v_mul_f32 v160, s62, v160\n"
"v_mul_f32 v161, s62, v161\n"
"v_mul_f32 v162, s62, v162\n"
"v_mul_f32 v163, s62, v163\n"
"v_mfma_f32_16x16x32_bf16 v[152:155], v[216:219], v[60:63], v[152:155]\n"
"ds_read_b128 v[216:219], v245 offset:320\n"
"v_mul_f32 v164, s62, v164\n"
"v_mul_f32 v165, s62, v165\n"
"v_mul_f32 v166, s62, v166\n"
"v_mul_f32 v167, s62, v167\n"
"v_mfma_f32_16x16x32_bf16 v[132:135], v[220:223], v[48:51], v[132:135]\n"
"v_cvt_pk_bf16_f32 v112, v160, v161\n"
"v_cvt_pk_bf16_f32 v113, v162, v163\n"
"v_cvt_pk_bf16_f32 v114, v164, v165\n"
"v_cvt_pk_bf16_f32 v115, v166, v167\n"
"v_accvgpr_read_b32 v168, a228\n"
"v_accvgpr_read_b32 v169, a229\n"
"v_mfma_f32_16x16x32_bf16 v[140:143], v[220:223], v[52:55], v[140:143]\n"
"v_accvgpr_read_b32 v170, a230\n"
"v_accvgpr_read_b32 v171, a231\n"
"v_accvgpr_read_b32 v172, a244\n"
"v_accvgpr_read_b32 v173, a245\n"
"v_accvgpr_read_b32 v174, a246\n"
"v_accvgpr_read_b32 v175, a247\n"
"v_mfma_f32_16x16x32_bf16 v[148:151], v[220:223], v[56:59], v[148:151]\n"
"s_add_u32 m0, s44, 36512\n"
"v_mul_f32 v168, s62, v168\n"
"v_mul_f32 v169, s62, v169\n"
"v_mul_f32 v170, s62, v170\n"
"v_mul_f32 v171, s62, v171\n"
"v_mul_f32 v172, s62, v172\n"
"v_mfma_f32_16x16x32_bf16 v[156:159], v[220:223], v[60:63], v[156:159]\n"
"ds_read_b128 v[220:223], v245 offset:832\n"
"v_mul_f32 v173, s62, v173\n"
"v_mul_f32 v174, s62, v174\n"
"v_mul_f32 v175, s62, v175\n"
"v_cvt_pk_bf16_f32 v116, v168, v169\n"
"s_waitcnt lgkmcnt(2)\n"
"v_mfma_f32_16x16x32_bf16 v[128:131], v[208:211], v[64:67], v[128:131]\n"
"buffer_load_dwordx4 v248, s[12:15], s54 offen offset:2560 nt lds\n"
"v_cvt_pk_bf16_f32 v117, v170, v171\n"
"v_cvt_pk_bf16_f32 v118, v172, v173\n"
"v_cvt_pk_bf16_f32 v119, v174, v175\n"
"v_accvgpr_read_b32 v176, a232\n"
"v_accvgpr_read_b32 v177, a233\n"
"v_mfma_f32_16x16x32_bf16 v[136:139], v[208:211], v[68:71], v[136:139]\n"
"v_accvgpr_read_b32 v178, a234\n"
"v_accvgpr_read_b32 v179, a235\n"
"v_accvgpr_read_b32 v180, a248\n"
"v_accvgpr_read_b32 v181, a249\n"
"v_accvgpr_read_b32 v182, a250\n"
"v_accvgpr_read_b32 v183, a251\n"
"v_mfma_f32_16x16x32_bf16 v[144:147], v[208:211], v[72:75], v[144:147]\n"
"v_mul_f32 v176, s62, v176\n"
"v_mul_f32 v177, s62, v177\n"
"v_mul_f32 v178, s62, v178\n"
"v_mul_f32 v179, s62, v179\n"
"v_mul_f32 v180, s62, v180\n"
"v_mul_f32 v181, s62, v181\n"
"v_mfma_f32_16x16x32_bf16 v[152:155], v[208:211], v[76:79], v[152:155]\n"
"ds_read_b128 v[208:211], v245 offset:384\n"
"v_mul_f32 v182, s62, v182\n"
"v_mul_f32 v183, s62, v183\n"
"v_cvt_pk_bf16_f32 v120, v176, v177\n"
"v_cvt_pk_bf16_f32 v121, v178, v179\n"
"v_mfma_f32_16x16x32_bf16 v[132:135], v[212:215], v[64:67], v[132:135]\n"
"s_add_u32 m0, s44, 37056\n"
"v_cvt_pk_bf16_f32 v122, v180, v181\n"
"v_cvt_pk_bf16_f32 v123, v182, v183\n"
"v_accvgpr_read_b32 v184, a236\n"
"v_accvgpr_read_b32 v185, a237\n"
"v_accvgpr_read_b32 v186, a238\n"
"v_mfma_f32_16x16x32_bf16 v[140:143], v[212:215], v[68:71], v[140:143]\n"
"v_accvgpr_read_b32 v187, a239\n"
"v_accvgpr_read_b32 v188, a252\n"
"v_accvgpr_read_b32 v189, a253\n"
"v_accvgpr_read_b32 v190, a254\n"
"v_accvgpr_read_b32 v191, a255\n"
"v_mul_f32 v184, s62, v184\n"
"v_mfma_f32_16x16x32_bf16 v[148:151], v[212:215], v[72:75], v[148:151]\n"
"buffer_load_dwordx4 v248, s[12:15], s54 offen offset:3072 nt lds\n"
"v_mul_f32 v185, s62, v185\n"
"v_mul_f32 v186, s62, v186\n"
"v_mul_f32 v187, s62, v187\n"
"v_mul_f32 v188, s62, v188\n"
"v_mul_f32 v189, s62, v189\n"
"v_mfma_f32_16x16x32_bf16 v[156:159], v[212:215], v[76:79], v[156:159]\n"
"ds_read_b128 v[212:215], v245 offset:896\n"
"v_mul_f32 v190, s62, v190\n"
"v_mul_f32 v191, s62, v191\n"
"v_cvt_pk_bf16_f32 v124, v184, v185\n"
"v_cvt_pk_bf16_f32 v125, v186, v187\n"
"s_waitcnt lgkmcnt(2)\n"
"v_mfma_f32_16x16x32_bf16 v[128:131], v[216:219], v[80:83], v[128:131]\n"
"v_cvt_pk_bf16_f32 v126, v188, v189\n"
"v_cvt_pk_bf16_f32 v127, v190, v191\n"
"v_mfma_f32_16x16x32_bf16 v[136:139], v[216:219], v[84:87], v[136:139]\n"
"v_mfma_f32_16x16x32_bf16 v[144:147], v[216:219], v[88:91], v[144:147]\n"
"s_add_u32 m0, s44, 37600\n"
"v_mfma_f32_16x16x32_bf16 v[152:155], v[216:219], v[92:95], v[152:155]\n"
"ds_read_b128 v[216:219], v245 offset:448\n"
"v_mfma_f32_16x16x32_bf16 v[132:135], v[220:223], v[80:83], v[132:135]\n"
"buffer_load_dwordx4 v248, s[12:15], s54 offen offset:3584 nt lds\n"
"v_mfma_f32_16x16x32_bf16 v[140:143], v[220:223], v[84:87], v[140:143]\n"
"s_add_u32 s54, s54, 16384\n"
"v_mfma_f32_16x16x32_bf16 v[148:151], v[220:223], v[88:91], v[148:151]\n"
"v_mfma_f32_16x16x32_bf16 v[156:159], v[220:223], v[92:95], v[156:159]\n"
"ds_read_b128 v[220:223], v245 offset:960\n"
"s_waitcnt lgkmcnt(3)\n"
"v_mfma_f32_16x16x32_bf16 v[128:131], v[208:211], v[96:99], v[128:131]\n"
"v_mfma_f32_16x16x32_bf16 v[136:139], v[208:211], v[100:103], v[136:139]\n"
"v_mfma_f32_16x16x32_bf16 v[144:147], v[208:211], v[104:107], v[144:147]\n"
"v_mfma_f32_16x16x32_bf16 v[152:155], v[208:211], v[108:111], v[152:155]\n"
"s_waitcnt lgkmcnt(2)\n"
"v_mfma_f32_16x16x32_bf16 v[132:135], v[212:215], v[96:99], v[132:135]\n"
"v_mfma_f32_16x16x32_bf16 v[140:143], v[212:215], v[100:103], v[140:143]\n"
"v_mfma_f32_16x16x32_bf16 v[148:151], v[212:215], v[104:107], v[148:151]\n"
"s_waitcnt vmcnt(16) lgkmcnt(0)\n"
"s_barrier\n"
"ds_read_b128 v[208:211], v245 offset:33792\n"
"v_mfma_f32_16x16x32_bf16 v[156:159], v[212:215], v[108:111], v[156:159]\n"
"v_mfma_f32_16x16x32_bf16 v[128:131], v[216:219], v[112:115], v[128:131]\n"
"ds_read_b128 v[212:215], v245 offset:34304\n"
"v_mfma_f32_16x16x32_bf16 v[136:139], v[216:219], v[116:119], v[136:139]\n"
"v_mfma_f32_16x16x32_bf16 v[144:147], v[216:219], v[120:123], v[144:147]\n"
"v_mfma_f32_16x16x32_bf16 v[152:155], v[216:219], v[124:127], v[152:155]\n"
"ds_read_b128 v[216:219], v245 offset:33856\n"
"v_mfma_f32_16x16x32_bf16 v[132:135], v[220:223], v[112:115], v[132:135]\n"
"v_mfma_f32_16x16x32_bf16 v[140:143], v[220:223], v[116:119], v[140:143]\n"
"v_mfma_f32_16x16x32_bf16 v[148:151], v[220:223], v[120:123], v[148:151]\n"
"v_mfma_f32_16x16x32_bf16 v[156:159], v[220:223], v[124:127], v[156:159]\n"
"ds_read_b128 v[220:223], v245 offset:34368\n"
"s_waitcnt lgkmcnt(3)\n"
"v_mfma_f32_16x16x32_bf16 v[160:163], v[208:211], v[0:3], 0\n"
"s_add_u32 m0, s44, 67584\n"
"v_mfma_f32_16x16x32_bf16 v[168:171], v[208:211], v[4:7], 0\n"
"v_mfma_f32_16x16x32_bf16 v[176:179], v[208:211], v[8:11], 0\n"
"buffer_load_dwordx4 v249, s[16:19], s55 offen nt lds\n"
"v_mfma_f32_16x16x32_bf16 v[184:187], v[208:211], v[12:15], 0\n"
"ds_read_b128 v[208:211], v245 offset:33920\n"
"v_exp_f32 v128, v128\n"
"s_waitcnt lgkmcnt(3)\n"
"v_mfma_f32_16x16x32_bf16 v[164:167], v[212:215], v[0:3], 0\n"
"v_exp_f32 v129, v129\n"
"v_exp_f32 v130, v130\n"
"v_mfma_f32_16x16x32_bf16 v[172:175], v[212:215], v[4:7], 0\n"
"v_exp_f32 v131, v131\n"
"v_exp_f32 v132, v132\n"
"v_mfma_f32_16x16x32_bf16 v[180:183], v[212:215], v[8:11], 0\n"
"s_add_u32 m0, s44, 68128\n"
"v_exp_f32 v133, v133\n"
"v_mfma_f32_16x16x32_bf16 v[188:191], v[212:215], v[12:15], 0\n"
"ds_read_b128 v[212:215], v245 offset:34432\n"
"v_exp_f32 v134, v134\n"
"s_waitcnt lgkmcnt(2)\n"
"v_mfma_f32_16x16x32_bf16 v[160:163], v[216:219], v[16:19], v[160:163]\n"
"buffer_load_dwordx4 v249, s[16:19], s55 offen offset:512 nt lds\n"
"v_exp_f32 v135, v135\n"
"v_mfma_f32_16x16x32_bf16 v[168:171], v[216:219], v[20:23], v[168:171]\n"
"v_exp_f32 v136, v136\n"
"v_cvt_pk_bf16_f32 v192, v128, v129\n"
"v_add_f32 v128, v128, v129\n"
"v_mfma_f32_16x16x32_bf16 v[176:179], v[216:219], v[24:27], v[176:179]\n"
"v_exp_f32 v137, v137\n"
"v_cvt_pk_bf16_f32 v193, v130, v131\n"
"v_mfma_f32_16x16x32_bf16 v[184:187], v[216:219], v[28:31], v[184:187]\n"
"ds_read_b128 v[216:219], v245 offset:33984\n"
"v_exp_f32 v138, v138\n"
"v_mfma_f32_16x16x32_bf16 v[164:167], v[220:223], v[16:19], v[164:167]\n"
"s_add_u32 m0, s44, 68672\n"
"v_add_f32 v130, v130, v131\n"
"v_cvt_pk_bf16_f32 v194, v132, v133\n"
"v_mfma_f32_16x16x32_bf16 v[172:175], v[220:223], v[20:23], v[172:175]\n"
"v_exp_f32 v139, v139\n"
"v_add_f32 v132, v132, v133\n"
"v_mfma_f32_16x16x32_bf16 v[180:183], v[220:223], v[24:27], v[180:183]\n"
"buffer_load_dwordx4 v249, s[16:19], s55 offen offset:1024 nt lds\n"
"v_exp_f32 v140, v140\n"
"v_add_f32 v128, v128, v130\n"
"v_mfma_f32_16x16x32_bf16 v[188:191], v[220:223], v[28:31], v[188:191]\n"
"ds_read_b128 v[220:223], v245 offset:34496\n"
"v_exp_f32 v141, v141\n"
"s_waitcnt lgkmcnt(2)\n"
"v_mfma_f32_16x16x32_bf16 v[160:163], v[208:211], v[32:35], v[160:163]\n"
"v_cvt_pk_bf16_f32 v195, v134, v135\n"
"v_add_f32 v134, v134, v135\n"
"v_exp_f32 v142, v142\n"
"v_mfma_f32_16x16x32_bf16 v[168:171], v[208:211], v[36:39], v[168:171]\n"
"v_add_f32 v128, v128, v132\n"
"v_exp_f32 v143, v143\n"
"v_add_f32 v128, v128, v134\n"
"v_mfma_f32_16x16x32_bf16 v[176:179], v[208:211], v[40:43], v[176:179]\n"
"s_add_u32 m0, s44, 69216\n"
"v_exp_f32 v144, v144\n"
"v_cvt_pk_bf16_f32 v196, v136, v137\n"
"v_mfma_f32_16x16x32_bf16 v[184:187], v[208:211], v[44:47], v[184:187]\n"
"ds_read_b128 v[208:211], v245 offset:34048\n"
"v_add_f32 v136, v136, v137\n"
"v_mfma_f32_16x16x32_bf16 v[164:167], v[212:215], v[32:35], v[164:167]\n"
"buffer_load_dwordx4 v249, s[16:19], s55 offen offset:1536 nt lds\n"
"v_exp_f32 v145, v145\n"
"v_cvt_pk_bf16_f32 v197, v138, v139\n"
"v_mfma_f32_16x16x32_bf16 v[172:175], v[212:215], v[36:39], v[172:175]\n"
"v_exp_f32 v146, v146\n"
"v_add_f32 v138, v138, v139\n"
"v_cvt_pk_bf16_f32 v198, v140, v141\n"
"v_mfma_f32_16x16x32_bf16 v[180:183], v[212:215], v[40:43], v[180:183]\n"
"v_exp_f32 v147, v147\n"
"v_add_f32 v140, v140, v141\n"
"v_mfma_f32_16x16x32_bf16 v[188:191], v[212:215], v[44:47], v[188:191]\n"
"ds_read_b128 v[212:215], v245 offset:34560\n"
"v_exp_f32 v148, v148\n"
"s_waitcnt lgkmcnt(2)\n"
"v_mfma_f32_16x16x32_bf16 v[160:163], v[216:219], v[48:51], v[160:163]\n"
"s_add_u32 m0, s44, 69760\n"
"v_add_f32 v136, v136, v138\n"
"v_exp_f32 v149, v149\n"
"v_mfma_f32_16x16x32_bf16 v[168:171], v[216:219], v[52:55], v[168:171]\n"
"v_cvt_pk_bf16_f32 v199, v142, v143\n"
"v_add_f32 v142, v142, v143\n"
"v_exp_f32 v150, v150\n"
"v_mfma_f32_16x16x32_bf16 v[176:179], v[216:219], v[56:59], v[176:179]\n"
"buffer_load_dwordx4 v249, s[16:19], s55 offen offset:2048 nt lds\n"
"v_add_f32 v136, v136, v140\n"
"v_exp_f32 v151, v151\n"
"v_mfma_f32_16x16x32_bf16 v[184:187], v[216:219], v[60:63], v[184:187]\n"
"ds_read_b128 v[216:219], v245 offset:34112\n"
"v_add_f32 v136, v136, v142\n"
"v_mfma_f32_16x16x32_bf16 v[164:167], v[220:223], v[48:51], v[164:167]\n"
"v_exp_f32 v152, v152\n"
"v_cvt_pk_bf16_f32 v200, v144, v145\n"
"v_add_f32 v144, v144, v145\n"
"v_mfma_f32_16x16x32_bf16 v[172:175], v[220:223], v[52:55], v[172:175]\n"
"v_exp_f32 v153, v153\n"
"v_cvt_pk_bf16_f32 v201, v146, v147\n"
"v_mfma_f32_16x16x32_bf16 v[180:183], v[220:223], v[56:59], v[180:183]\n"
"s_add_u32 m0, s44, 70304\n"
"v_exp_f32 v154, v154\n"
"v_add_f32 v146, v146, v147\n"
"v_mfma_f32_16x16x32_bf16 v[188:191], v[220:223], v[60:63], v[188:191]\n"
"ds_read_b128 v[220:223], v245 offset:34624\n"
"v_cvt_pk_bf16_f32 v202, v148, v149\n"
"s_waitcnt lgkmcnt(2)\n"
"v_mfma_f32_16x16x32_bf16 v[160:163], v[208:211], v[64:67], v[160:163]\n"
"buffer_load_dwordx4 v249, s[16:19], s55 offen offset:2560 nt lds\n"
"v_exp_f32 v155, v155\n"
"v_add_f32 v148, v148, v149\n"
"v_mfma_f32_16x16x32_bf16 v[168:171], v[208:211], v[68:71], v[168:171]\n"
"v_exp_f32 v156, v156\n"
"v_add_f32 v144, v144, v146\n"
"v_mfma_f32_16x16x32_bf16 v[176:179], v[208:211], v[72:75], v[176:179]\n"
"v_exp_f32 v157, v157\n"
"v_cvt_pk_bf16_f32 v203, v150, v151\n"
"v_add_f32 v150, v150, v151\n"
"v_mfma_f32_16x16x32_bf16 v[184:187], v[208:211], v[76:79], v[184:187]\n"
"ds_read_b128 v[208:211], v245 offset:34176\n"
"v_exp_f32 v158, v158\n"
"v_mfma_f32_16x16x32_bf16 v[164:167], v[212:215], v[64:67], v[164:167]\n"
"s_add_u32 m0, s44, 70848\n"
"v_add_f32 v144, v144, v148\n"
"v_exp_f32 v159, v159\n"
"v_mfma_f32_16x16x32_bf16 v[172:175], v[212:215], v[68:71], v[172:175]\n"
"v_add_f32 v144, v144, v150\n"
"v_cvt_pk_bf16_f32 v204, v152, v153\n"
"v_add_f32 v152, v152, v153\n"
"v_cvt_pk_bf16_f32 v205, v154, v155\n"
"v_mfma_f32_16x16x32_bf16 v[180:183], v[212:215], v[72:75], v[180:183]\n"
"buffer_load_dwordx4 v249, s[16:19], s55 offen offset:3072 nt lds\n"
"v_add_f32 v154, v154, v155\n"
"v_cvt_pk_bf16_f32 v206, v156, v157\n"
"v_add_f32 v156, v156, v157\n"
"v_mfma_f32_16x16x32_bf16 v[188:191], v[212:215], v[76:79], v[188:191]\n"
"ds_read_b128 v[212:215], v245 offset:34688\n"
"v_add_f32 v152, v152, v154\n"
"v_cvt_pk_bf16_f32 v207, v158, v159\n"
"s_waitcnt lgkmcnt(2)\n"
"v_mfma_f32_16x16x32_bf16 v[160:163], v[216:219], v[80:83], v[160:163]\n"
"v_add_f32 v158, v158, v159\n"
"v_add_f32 v152, v152, v156\n"
"v_add_f32 v152, v152, v158\n"
"v_max3_f32 v129, v128, v136, v144\n"
"v_mfma_f32_16x16x32_bf16 v[168:171], v[216:219], v[84:87], v[168:171]\n"
"v_max_f32 v129, v129, v152\n"
"v_cmp_nge_f32 vcc, 0x43800000, v129\n"
"v_min3_f32 v137, v128, v136, v144\n"
"v_min_f32 v137, v137, v152\n"
"v_mfma_f32_16x16x32_bf16 v[176:179], v[216:219], v[88:91], v[176:179]\n"
"s_add_u32 m0, s44, 71392\n"
"s_mov_b64 s[90:91], vcc\n"
"v_cmp_nle_f32 vcc, 0x2b800000, v137\n"
"s_nop 4\n"
"v_mfma_f32_16x16x32_bf16 v[184:187], v[216:219], v[92:95], v[184:187]\n"
"ds_read_b128 v[216:219], v245 offset:34240\n"
"s_or_b64 vcc, vcc, s[90:91]\n"
"v_mfma_f32_16x16x32_bf16 v[164:167], v[220:223], v[80:83], v[164:167]\n"
"buffer_load_dwordx4 v249, s[16:19], s55 offen offset:3584 nt lds\n"
"v_mfma_f32_16x16x32_bf16 v[172:175], v[220:223], v[84:87], v[172:175]\n"
"s_add_u32 s55, s55, 16384\n"
"v_mfma_f32_16x16x32_bf16 v[180:183], v[220:223], v[88:91], v[180:183]\n"
"v_mfma_f32_16x16x32_bf16 v[188:191], v[220:223], v[92:95], v[188:191]\n"
"ds_read_b128 v[220:223], v245 offset:34752\n"
"s_waitcnt lgkmcnt(3)\n"
"v_mfma_f32_16x16x32_bf16 v[160:163], v[208:211], v[96:99], v[160:163]\n"
"v_mfma_f32_16x16x32_bf16 v[168:171], v[208:211], v[100:103], v[168:171]\n"
"v_mfma_f32_16x16x32_bf16 v[176:179], v[208:211], v[104:107], v[176:179]\n"
"v_mfma_f32_16x16x32_bf16 v[184:187], v[208:211], v[108:111], v[184:187]\n"
"s_waitcnt lgkmcnt(2)\n"
"v_mfma_f32_16x16x32_bf16 v[164:167], v[212:215], v[96:99], v[164:167]\n"
"v_mfma_f32_16x16x32_bf16 v[172:175], v[212:215], v[100:103], v[172:175]\n"
"v_mfma_f32_16x16x32_bf16 v[180:183], v[212:215], v[104:107], v[180:183]\n"
"s_waitcnt vmcnt(16) lgkmcnt(0)\n"
"s_barrier\n"
"ds_read_b64_tr_b16 v[208:209], v246 offset:0\n"
"v_mfma_f32_16x16x32_bf16 v[188:191], v[212:215], v[108:111], v[188:191]\n"
"ds_read_b64_tr_b16 v[210:211], v246 offset:8448\n"
"v_mfma_f32_16x16x32_bf16 v[160:163], v[216:219], v[112:115], v[160:163]\n"
"ds_read_b64_tr_b16 v[212:213], v246 offset:32\n"
"v_mfma_f32_16x16x32_bf16 v[168:171], v[216:219], v[116:119], v[168:171]\n"
"ds_read_b64_tr_b16 v[214:215], v246 offset:8480\n"
"v_mfma_f32_16x16x32_bf16 v[176:179], v[216:219], v[120:123], v[176:179]\n"
"v_mfma_f32_16x16x32_bf16 v[184:187], v[216:219], v[124:127], v[184:187]\n"
"ds_read_b64_tr_b16 v[216:217], v246 offset:64\n"
"v_mfma_f32_16x16x32_bf16 v[164:167], v[220:223], v[112:115], v[164:167]\n"
"ds_read_b64_tr_b16 v[218:219], v246 offset:8512\n"
"v_mfma_f32_16x16x32_bf16 v[172:175], v[220:223], v[116:119], v[172:175]\n"
"v_mfma_f32_16x16x32_bf16 v[180:183], v[220:223], v[120:123], v[180:183]\n"
"v_mfma_f32_16x16x32_bf16 v[188:191], v[220:223], v[124:127], v[188:191]\n"
"ds_read_b64_tr_b16 v[220:221], v246 offset:96\n"
"ds_read_b64_tr_b16 v[222:223], v246 offset:8544\n"
"s_cbranch_vccz LR_1_%=\n"
"s_getpc_b64 s[72:73]\n"
"s_branch LP13_%=\n"
"LR_1_%=:\n"
"s_waitcnt lgkmcnt(6)\n"
"v_mfma_f32_16x16x32_bf16 a[0:3], v[208:211], v[192:195], 0\n"
"s_add_u32 m0, s44, 101376\n"
"v_mfma_f32_16x16x32_bf16 a[4:7], v[208:211], v[196:199], 0\n"
"v_mov_b32 v240, v128\n"
"v_mov_b32 v241, v136\n"
"v_mfma_f32_16x16x32_bf16 a[8:11], v[208:211], v[200:203], 0\n"
"v_mov_b32 v242, v144\n"
"v_mov_b32 v243, v152\n"
"v_mfma_f32_16x16x32_bf16 a[12:15], v[208:211], v[204:207], 0\n"
"ds_read_b64_tr_b16 v[208:209], v246 offset:128\n"
"s_waitcnt lgkmcnt(5)\n"
"v_mfma_f32_16x16x32_bf16 a[16:19], v[212:215], v[192:195], 0\n"
"ds_read_b64_tr_b16 v[210:211], v246 offset:8576\n"
"buffer_load_dwordx4 v248, s[12:15], s54 offen nt lds\n"
"v_mfma_f32_16x16x32_bf16 a[20:23], v[212:215], v[196:199], 0\n"
"v_exp_f32 v160, v160\n"
"v_mfma_f32_16x16x32_bf16 a[24:27], v[212:215], v[200:203], 0\n"
"v_exp_f32 v161, v161\n"
"v_mfma_f32_16x16x32_bf16 a[28:31], v[212:215], v[204:207], 0\n"
"ds_read_b64_tr_b16 v[212:213], v246 offset:160\n"
"s_waitcnt lgkmcnt(5)\n"
"v_mfma_f32_16x16x32_bf16 a[32:35], v[216:219], v[192:195], 0\n"
"ds_read_b64_tr_b16 v[214:215], v246 offset:8608\n"
"s_add_u32 m0, s44, 101920\n"
"v_mfma_f32_16x16x32_bf16 a[36:39], v[216:219], v[196:199], 0\n"
"v_exp_f32 v162, v162\n"
"v_mfma_f32_16x16x32_bf16 a[40:43], v[216:219], v[200:203], 0\n"
"v_exp_f32 v163, v163\n"
"v_mfma_f32_16x16x32_bf16 a[44:47], v[216:219], v[204:207], 0\n"
"ds_read_b64_tr_b16 v[216:217], v246 offset:192\n"
"s_waitcnt lgkmcnt(5)\n"
"v_mfma_f32_16x16x32_bf16 a[48:51], v[220:223], v[192:195], 0\n"
"ds_read_b64_tr_b16 v[218:219], v246 offset:8640\n"
"buffer_load_dwordx4 v248, s[12:15], s54 offen offset:512 nt lds\n"
"v_mfma_f32_16x16x32_bf16 a[52:55], v[220:223], v[196:199], 0\n"
"v_exp_f32 v164, v164\n"
"v_mfma_f32_16x16x32_bf16 a[56:59], v[220:223], v[200:203], 0\n"
"v_exp_f32 v165, v165\n"
"v_mfma_f32_16x16x32_bf16 a[60:63], v[220:223], v[204:207], 0\n"
"ds_read_b64_tr_b16 v[220:221], v246 offset:224\n"
"s_waitcnt lgkmcnt(5)\n"
"v_mfma_f32_16x16x32_bf16 a[64:67], v[208:211], v[192:195], 0\n"
"ds_read_b64_tr_b16 v[222:223], v246 offset:8672\n"
"s_add_u32 m0, s44, 102464\n"
"v_mfma_f32_16x16x32_bf16 a[68:71], v[208:211], v[196:199], 0\n"
"v_exp_f32 v166, v166\n"
"v_mfma_f32_16x16x32_bf16 a[72:75], v[208:211], v[200:203], 0\n"
"v_exp_f32 v167, v167\n"
"v_mfma_f32_16x16x32_bf16 a[76:79], v[208:211], v[204:207], 0\n"
"ds_read_b64_tr_b16 v[208:209], v246 offset:256\n"
"s_waitcnt lgkmcnt(5)\n"
"v_mfma_f32_16x16x32_bf16 a[80:83], v[212:215], v[192:195], 0\n"
"ds_read_b64_tr_b16 v[210:211], v246 offset:8704\n"
"buffer_load_dwordx4 v248, s[12:15], s54 offen offset:1024 nt lds\n"
"v_mfma_f32_16x16x32_bf16 a[84:87], v[212:215], v[196:199], 0\n"
"v_exp_f32 v168, v168\n"
"v_mfma_f32_16x16x32_bf16 a[88:91], v[212:215], v[200:203], 0\n"
"v_exp_f32 v169, v169\n"
"v_mfma_f32_16x16x32_bf16 a[92:95], v[212:215], v[204:207], 0\n"
"ds_read_b64_tr_b16 v[212:213], v246 offset:288\n"
"s_waitcnt lgkmcnt(5)\n"
"v_mfma_f32_16x16x32_bf16 a[96:99], v[216:219], v[192:195], 0\n"
"ds_read_b64_tr_b16 v[214:215], v246 offset:8736\n"
"s_add_u32 m0, s44, 103008\n"
"v_mfma_f32_16x16x32_bf16 a[100:103], v[216:219], v[196:199], 0\n"
"v_exp_f32 v170, v170\n"
"v_mfma_f32_16x16x32_bf16 a[104:107], v[216:219], v[200:203], 0\n"
"v_exp_f32 v171, v171\n"
"v_mfma_f32_16x16x32_bf16 a[108:111], v[216:219], v[204:207], 0\n"
"ds_read_b64_tr_b16 v[216:217], v246 offset:320\n"
"s_waitcnt lgkmcnt(5)\n"
"v_mfma_f32_16x16x32_bf16 a[112:115], v[220:223], v[192:195], 0\n"
"ds_read_b64_tr_b16 v[218:219], v246 offset:8768\n"
"buffer_load_dwordx4 v248, s[12:15], s54 offen offset:1536 nt lds\n"
"v_mfma_f32_16x16x32_bf16 a[116:119], v[220:223], v[196:199], 0\n"
"v_exp_f32 v172, v172\n"
"v_mfma_f32_16x16x32_bf16 a[120:123], v[220:223], v[200:203], 0\n"
"v_exp_f32 v173, v173\n"
"v_mfma_f32_16x16x32_bf16 a[124:127], v[220:223], v[204:207], 0\n"
"ds_read_b64_tr_b16 v[220:221], v246 offset:352\n"
"s_waitcnt lgkmcnt(5)\n"
"v_mfma_f32_16x16x32_bf16 a[128:131], v[208:211], v[192:195], 0\n"
"ds_read_b64_tr_b16 v[222:223], v246 offset:8800\n"
"s_add_u32 m0, s44, 103552\n"
"v_mfma_f32_16x16x32_bf16 a[132:135], v[208:211], v[196:199], 0\n"
"v_exp_f32 v174, v174\n"
"v_mfma_f32_16x16x32_bf16 a[136:139], v[208:211], v[200:203], 0\n"
"v_exp_f32 v175, v175\n"
"v_mfma_f32_16x16x32_bf16 a[140:143], v[208:211], v[204:207], 0\n"
"ds_read_b64_tr_b16 v[208:209], v246 offset:384\n"
"s_waitcnt lgkmcnt(5)\n"
"v_mfma_f32_16x16x32_bf16 a[144:147], v[212:215], v[192:195], 0\n"
"ds_read_b64_tr_b16 v[210:211], v246 offset:8832\n"
"buffer_load_dwordx4 v248, s[12:15], s54 offen offset:2048 nt lds\n"
"v_mfma_f32_16x16x32_bf16 a[148:151], v[212:215], v[196:199], 0\n"
"v_exp_f32 v176, v176\n"
"v_mfma_f32_16x16x32_bf16 a[152:155], v[212:215], v[200:203], 0\n"
"v_exp_f32 v177, v177\n"
"v_mfma_f32_16x16x32_bf16 a[156:159], v[212:215], v[204:207], 0\n"
"ds_read_b64_tr_b16 v[212:213], v246 offset:416\n"
"s_waitcnt lgkmcnt(5)\n"
"v_mfma_f32_16x16x32_bf16 a[160:163], v[216:219], v[192:195], 0\n"
"ds_read_b64_tr_b16 v[214:215], v246 offset:8864\n"
"s_add_u32 m0, s44, 104096\n"
"v_mfma_f32_16x16x32_bf16 a[164:167], v[216:219], v[196:199], 0\n"
"v_exp_f32 v178, v178\n"
"v_mfma_f32_16x16x32_bf16 a[168:171], v[216:219], v[200:203], 0\n"
"v_exp_f32 v179, v179\n"
"v_mfma_f32_16x16x32_bf16 a[172:175], v[216:219], v[204:207], 0\n"
"ds_read_b64_tr_b16 v[216:217], v246 offset:448\n"
"s_waitcnt lgkmcnt(5)\n"
"v_mfma_f32_16x16x32_bf16 a[176:179], v[220:223], v[192:195], 0\n"
"ds_read_b64_tr_b16 v[218:219], v246 offset:8896\n"
"buffer_load_dwordx4 v248, s[12:15], s54 offen offset:2560 nt lds\n"
"v_mfma_f32_16x16x32_bf16 a[180:183], v[220:223], v[196:199], 0\n"
"v_exp_f32 v180, v180\n"
"v_mfma_f32_16x16x32_bf16 a[184:187], v[220:223], v[200:203], 0\n"
"v_exp_f32 v181, v181\n"
"v_mfma_f32_16x16x32_bf16 a[188:191], v[220:223], v[204:207], 0\n"
"ds_read_b64_tr_b16 v[220:221], v246 offset:480\n"
"s_waitcnt lgkmcnt(5)\n"
"v_mfma_f32_16x16x32_bf16 a[192:195], v[208:211], v[192:195], 0\n"
"ds_read_b64_tr_b16 v[222:223], v246 offset:8928\n"
"s_add_u32 m0, s44, 104640\n"
"v_mfma_f32_16x16x32_bf16 a[196:199], v[208:211], v[196:199], 0\n"
"v_exp_f32 v182, v182\n"
"v_mfma_f32_16x16x32_bf16 a[200:203], v[208:211], v[200:203], 0\n"
"v_exp_f32 v183, v183\n"
"v_mfma_f32_16x16x32_bf16 a[204:207], v[208:211], v[204:207], 0\n"
"s_waitcnt lgkmcnt(4)\n"
"v_mfma_f32_16x16x32_bf16 a[208:211], v[212:215], v[192:195], 0\n"
"buffer_load_dwordx4 v248, s[12:15], s54 offen offset:3072 nt lds\n"
"v_mfma_f32_16x16x32_bf16 a[212:215], v[212:215], v[196:199], 0\n"
"v_exp_f32 v184, v184\n"
"v_mfma_f32_16x16x32_bf16 a[216:219], v[212:215], v[200:203], 0\n"
"s_waitcnt vmcnt(15) lgkmcnt(0)\n"
"s_barrier\n"
"ds_read_b128 v[208:211], v244 offset:33792\n"
"v_mfma_f32_16x16x32_bf16 a[220:223], v[212:215], v[204:207], 0\n"
"v_mfma_f32_16x16x32_bf16 a[224:227], v[216:219], v[192:195], 0\n"
"ds_read_b128 v[212:215], v244 offset:34304\n"
"s_add_u32 m0, s44, 105184\n"
"v_mfma_f32_16x16x32_bf16 a[228:231], v[216:219], v[196:199], 0\n"
"v_exp_f32 v185, v185\n"
"v_mfma_f32_16x16x32_bf16 a[232:235], v[216:219], v[200:203], 0\n"
"v_mfma_f32_16x16x32_bf16 a[236:239], v[216:219], v[204:207], 0\n"
"ds_read_b128 v[216:219], v244 offset:33856\n"
"v_mfma_f32_16x16x32_bf16 a[240:243], v[220:223], v[192:195], 0\n"
"buffer_load_dwordx4 v248, s[12:15], s54 offen offset:3584 nt lds\n"
"s_add_u32 s54, s54, 16384\n"
"v_mfma_f32_16x16x32_bf16 a[244:247], v[220:223], v[196:199], 0\n"
"v_mfma_f32_16x16x32_bf16 a[248:251], v[220:223], v[200:203], 0\n"
"v_mfma_f32_16x16x32_bf16 a[252:255], v[220:223], v[204:207], 0\n"
"ds_read_b128 v[220:223], v244 offset:34368\n"
"s_mov_b32 s64, 6\n"
"LA_%=:\n"
"s_waitcnt lgkmcnt(3)\n"
"v_mfma_f32_16x16x32_bf16 v[128:131], v[208:211], v[0:3], v[224:227]\n"
"v_cvt_pk_bf16_f32 v192, v160, v161\n"
"v_add_f32 v160, v160, v161\n"
"v_mfma_f32_16x16x32_bf16 v[136:139], v[208:211], v[4:7], v[228:231]\n"
"s_add_u32 m0, s44, 0\n"
"v_cvt_pk_bf16_f32 v193, v162, v163\n"
"v_mfma_f32_16x16x32_bf16 v[144:147], v[208:211], v[8:11], v[232:235]\n"
"v_add_f32 v162, v162, v163\n"
"v_cvt_pk_bf16_f32 v194, v164, v165\n"
"v_mfma_f32_16x16x32_bf16 v[152:155], v[208:211], v[12:15], v[236:239]\n"
"ds_read_b128 v[208:211], v244 offset:33920\n"
"s_waitcnt lgkmcnt(3)\n"
"v_mfma_f32_16x16x32_bf16 v[132:135], v[212:215], v[0:3], v[224:227]\n"
"v_add_f32 v164, v164, v165\n"
"v_add_f32 v160, v160, v162\n"
"v_mfma_f32_16x16x32_bf16 v[140:143], v[212:215], v[4:7], v[228:231]\n"
"buffer_load_dwordx4 v249, s[16:19], s55 offen nt lds\n"
"v_cvt_pk_bf16_f32 v195, v166, v167\n"
"v_mfma_f32_16x16x32_bf16 v[148:151], v[212:215], v[8:11], v[232:235]\n"
"v_add_f32 v166, v166, v167\n"
"v_add_f32 v160, v160, v164\n"
"v_mfma_f32_16x16x32_bf16 v[156:159], v[212:215], v[12:15], v[236:239]\n"
"ds_read_b128 v[212:215], v244 offset:34432\n"
"s_waitcnt lgkmcnt(2)\n"
"v_mfma_f32_16x16x32_bf16 v[128:131], v[216:219], v[16:19], v[128:131]\n"
"v_add_f32 v160, v160, v166\n"
"v_cvt_pk_bf16_f32 v196, v168, v169\n"
"v_mfma_f32_16x16x32_bf16 v[136:139], v[216:219], v[20:23], v[136:139]\n"
"s_add_u32 m0, s44, 544\n"
"v_add_f32 v168, v168, v169\n"
"v_mfma_f32_16x16x32_bf16 v[144:147], v[216:219], v[24:27], v[144:147]\n"
"v_cvt_pk_bf16_f32 v197, v170, v171\n"
"v_add_f32 v170, v170, v171\n"
"v_mfma_f32_16x16x32_bf16 v[152:155], v[216:219], v[28:31], v[152:155]\n"
"ds_read_b128 v[216:219], v244 offset:33984\n"
"v_mfma_f32_16x16x32_bf16 v[132:135], v[220:223], v[16:19], v[132:135]\n"
"v_cvt_pk_bf16_f32 v198, v172, v173\n"
"v_add_f32 v172, v172, v173\n"
"v_mfma_f32_16x16x32_bf16 v[140:143], v[220:223], v[20:23], v[140:143]\n"
"buffer_load_dwordx4 v249, s[16:19], s55 offen offset:512 nt lds\n"
"v_add_f32 v168, v168, v170\n"
"v_mfma_f32_16x16x32_bf16 v[148:151], v[220:223], v[24:27], v[148:151]\n"
"v_cvt_pk_bf16_f32 v199, v174, v175\n"
"v_add_f32 v174, v174, v175\n"
"v_mfma_f32_16x16x32_bf16 v[156:159], v[220:223], v[28:31], v[156:159]\n"
"ds_read_b128 v[220:223], v244 offset:34496\n"
"s_waitcnt lgkmcnt(2)\n"
"v_mfma_f32_16x16x32_bf16 v[128:131], v[208:211], v[32:35], v[128:131]\n"
"v_add_f32 v168, v168, v172\n"
"v_add_f32 v168, v168, v174\n"
"v_mfma_f32_16x16x32_bf16 v[136:139], v[208:211], v[36:39], v[136:139]\n"
"s_add_u32 m0, s44, 1088\n"
"v_mfma_f32_16x16x32_bf16 v[144:147], v[208:211], v[40:43], v[144:147]\n"
"v_exp_f32 v186, v186\n"
"v_mfma_f32_16x16x32_bf16 v[152:155], v[208:211], v[44:47], v[152:155]\n"
"ds_read_b128 v[208:211], v244 offset:34048\n"
"v_mfma_f32_16x16x32_bf16 v[132:135], v[212:215], v[32:35], v[132:135]\n"
"v_cvt_pk_bf16_f32 v200, v176, v177\n"
"v_add_f32 v176, v176, v177\n"
"v_mfma_f32_16x16x32_bf16 v[140:143], v[212:215], v[36:39], v[140:143]\n"
"buffer_load_dwordx4 v249, s[16:19], s55 offen offset:1024 nt lds\n"
"v_mfma_f32_16x16x32_bf16 v[148:151], v[212:215], v[40:43], v[148:151]\n"
"v_exp_f32 v187, v187\n"
"v_mfma_f32_16x16x32_bf16 v[156:159], v[212:215], v[44:47], v[156:159]\n"
"ds_read_b128 v[212:215], v244 offset:34560\n"
"s_waitcnt lgkmcnt(2)\n"
"v_mfma_f32_16x16x32_bf16 v[128:131], v[216:219], v[48:51], v[128:131]\n"
"v_cvt_pk_bf16_f32 v201, v178, v179\n"
"v_add_f32 v178, v178, v179\n"
"v_mfma_f32_16x16x32_bf16 v[136:139], v[216:219], v[52:55], v[136:139]\n"
"s_add_u32 m0, s44, 1632\n"
"v_mfma_f32_16x16x32_bf16 v[144:147], v[216:219], v[56:59], v[144:147]\n"
"v_exp_f32 v188, v188\n"
"v_mfma_f32_16x16x32_bf16 v[152:155], v[216:219], v[60:63], v[152:155]\n"
"ds_read_b128 v[216:219], v244 offset:34112\n"
"v_mfma_f32_16x16x32_bf16 v[132:135], v[220:223], v[48:51], v[132:135]\n"
"v_cvt_pk_bf16_f32 v202, v180, v181\n"
"v_add_f32 v180, v180, v181\n"
"v_mfma_f32_16x16x32_bf16 v[140:143], v[220:223], v[52:55], v[140:143]\n"
"buffer_load_dwordx4 v249, s[16:19], s55 offen offset:1536 nt lds\n"
"v_mfma_f32_16x16x32_bf16 v[148:151], v[220:223], v[56:59], v[148:151]\n"
"v_exp_f32 v189, v189\n"
"v_mfma_f32_16x16x32_bf16 v[156:159], v[220:223], v[60:63], v[156:159]\n"
"ds_read_b128 v[220:223], v244 offset:34624\n"
"s_waitcnt lgkmcnt(2)\n"
"v_mfma_f32_16x16x32_bf16 v[128:131], v[208:211], v[64:67], v[128:131]\n"
"v_add_f32 v176, v176, v178\n"
"v_cvt_pk_bf16_f32 v203, v182, v183\n"
"v_mfma_f32_16x16x32_bf16 v[136:139], v[208:211], v[68:71], v[136:139]\n"
"s_add_u32 m0, s44, 2176\n"
"v_mfma_f32_16x16x32_bf16 v[144:147], v[208:211], v[72:75], v[144:147]\n"
"v_exp_f32 v190, v190\n"
"v_mfma_f32_16x16x32_bf16 v[152:155], v[208:211], v[76:79], v[152:155]\n"
"ds_read_b128 v[208:211], v244 offset:34176\n"
"v_mfma_f32_16x16x32_bf16 v[132:135], v[212:215], v[64:67], v[132:135]\n"
"v_add_f32 v182, v182, v183\n"
"v_add_f32 v176, v176, v180\n"
"v_mfma_f32_16x16x32_bf16 v[140:143], v[212:215], v[68:71], v[140:143]\n"
"buffer_load_dwordx4 v249, s[16:19], s55 offen offset:2048 nt lds\n"
"v_mfma_f32_16x16x32_bf16 v[148:151], v[212:215], v[72:75], v[148:151]\n"
"v_exp_f32 v191, v191\n"
"v_mfma_f32_16x16x32_bf16 v[156:159], v[212:215], v[76:79], v[156:159]\n"
"ds_read_b128 v[212:215], v244 offset:34688\n"
"s_waitcnt lgkmcnt(2)\n"
"v_mfma_f32_16x16x32_bf16 v[128:131], v[216:219], v[80:83], v[128:131]\n"
"v_add_f32 v176, v176, v182\n"
"v_cvt_pk_bf16_f32 v204, v184, v185\n"
"v_mfma_f32_16x16x32_bf16 v[136:139], v[216:219], v[84:87], v[136:139]\n"
"s_add_u32 m0, s44, 2720\n"
"v_add_f32 v184, v184, v185\n"
"v_mfma_f32_16x16x32_bf16 v[144:147], v[216:219], v[88:91], v[144:147]\n"
"v_cvt_pk_bf16_f32 v205, v186, v187\n"
"v_add_f32 v186, v186, v187\n"
"v_mfma_f32_16x16x32_bf16 v[152:155], v[216:219], v[92:95], v[152:155]\n"
"ds_read_b128 v[216:219], v244 offset:34240\n"
"v_mfma_f32_16x16x32_bf16 v[132:135], v[220:223], v[80:83], v[132:135]\n"
"v_cvt_pk_bf16_f32 v206, v188, v189\n"
"v_add_f32 v188, v188, v189\n"
"v_mfma_f32_16x16x32_bf16 v[140:143], v[220:223], v[84:87], v[140:143]\n"
"buffer_load_dwordx4 v249, s[16:19], s55 offen offset:2560 nt lds\n"
"v_add_f32 v184, v184, v186\n"
"v_mfma_f32_16x16x32_bf16 v[148:151], v[220:223], v[88:91], v[148:151]\n"
"v_cvt_pk_bf16_f32 v207, v190, v191\n"
"v_add_f32 v190, v190, v191\n"
"v_mfma_f32_16x16x32_bf16 v[156:159], v[220:223], v[92:95], v[156:159]\n"
"ds_read_b128 v[220:223], v244 offset:34752\n"
"s_waitcnt lgkmcnt(3)\n"
"v_mfma_f32_16x16x32_bf16 v[128:131], v[208:211], v[96:99], v[128:131]\n"
"v_add_f32 v184, v184, v188\n"
"v_add_f32 v184, v184, v190\n"
"v_mfma_f32_16x16x32_bf16 v[136:139], v[208:211], v[100:103], v[136:139]\n"
"s_add_u32 m0, s44, 3264\n"
"v_max3_f32 v161, v160, v168, v176\n"
"v_mfma_f32_16x16x32_bf16 v[144:147], v[208:211], v[104:107], v[144:147]\n"
"v_max_f32 v161, v161, v184\n"
"v_cmp_nge_f32 vcc, 0x43800000, v161\n"
"v_mfma_f32_16x16x32_bf16 v[152:155], v[208:211], v[108:111], v[152:155]\n"
"s_waitcnt lgkmcnt(2)\n"
"v_mfma_f32_16x16x32_bf16 v[132:135], v[212:215], v[96:99], v[132:135]\n"
"v_mfma_f32_16x16x32_bf16 v[140:143], v[212:215], v[100:103], v[140:143]\n"
"buffer_load_dwordx4 v249, s[16:19], s55 offen offset:3072 nt lds\n"
"v_mfma_f32_16x16x32_bf16 v[148:151], v[212:215], v[104:107], v[148:151]\n"
"s_waitcnt vmcnt(15) lgkmcnt(0)\n"
"s_barrier\n"
"ds_read_b64_tr_b16 v[208:209], v247 offset:0\n"
"v_mfma_f32_16x16x32_bf16 v[156:159], v[212:215], v[108:111], v[156:159]\n"
"ds_read_b64_tr_b16 v[210:211], v247 offset:8448\n"
"v_mfma_f32_16x16x32_bf16 v[128:131], v[216:219], v[112:115], v[128:131]\n"
"ds_read_b64_tr_b16 v[212:213], v247 offset:32\n"
"v_mfma_f32_16x16x32_bf16 v[136:139], v[216:219], v[116:119], v[136:139]\n"
"ds_read_b64_tr_b16 v[214:215], v247 offset:8480\n"
"s_add_u32 m0, s44, 3808\n"
"v_mfma_f32_16x16x32_bf16 v[144:147], v[216:219], v[120:123], v[144:147]\n"
"v_mfma_f32_16x16x32_bf16 v[152:155], v[216:219], v[124:127], v[152:155]\n"
"ds_read_b64_tr_b16 v[216:217], v247 offset:64\n"
"v_mfma_f32_16x16x32_bf16 v[132:135], v[220:223], v[112:115], v[132:135]\n"
"ds_read_b64_tr_b16 v[218:219], v247 offset:8512\n"
"v_mfma_f32_16x16x32_bf16 v[140:143], v[220:223], v[116:119], v[140:143]\n"
"buffer_load_dwordx4 v249, s[16:19], s55 offen offset:3584 nt lds\n"
"s_add_u32 s55, s55, 16384\n"
"v_mfma_f32_16x16x32_bf16 v[148:151], v[220:223], v[120:123], v[148:151]\n"
"v_mfma_f32_16x16x32_bf16 v[156:159], v[220:223], v[124:127], v[156:159]\n"
"ds_read_b64_tr_b16 v[220:221], v247 offset:96\n"
"ds_read_b64_tr_b16 v[222:223], v247 offset:8544\n"
"s_cbranch_vccz LR_2_%=\n"
"s_getpc_b64 s[72:73]\n"
"s_branch LP11_%=\n"
"LR_2_%=:\n"
"s_waitcnt lgkmcnt(6)\n"
"v_mfma_f32_16x16x32_bf16 a[0:3], v[208:211], v[192:195], a[0:3]\n"
"s_add_u32 m0, s44, 33792\n"
"v_mfma_f32_16x16x32_bf16 a[4:7], v[208:211], v[196:199], a[4:7]\n"
"v_add_f32 v240, v240, v160\n"
"v_add_f32 v241, v241, v168\n"
"v_mfma_f32_16x16x32_bf16 a[8:11], v[208:211], v[200:203], a[8:11]\n"
"v_add_f32 v242, v242, v176\n"
"v_add_f32 v243, v243, v184\n"
"v_mfma_f32_16x16x32_bf16 a[12:15], v[208:211], v[204:207], a[12:15]\n"
"ds_read_b64_tr_b16 v[208:209], v247 offset:128\n"
"s_waitcnt lgkmcnt(5)\n"
"v_mfma_f32_16x16x32_bf16 a[16:19], v[212:215], v[192:195], a[16:19]\n"
"ds_read_b64_tr_b16 v[210:211], v247 offset:8576\n"
"buffer_load_dwordx4 v248, s[12:15], s54 offen nt lds\n"
"v_mfma_f32_16x16x32_bf16 a[20:23], v[212:215], v[196:199], a[20:23]\n"
"v_exp_f32 v128, v128\n"
"v_mfma_f32_16x16x32_bf16 a[24:27], v[212:215], v[200:203], a[24:27]\n"
"v_exp_f32 v129, v129\n"
"v_mfma_f32_16x16x32_bf16 a[28:31], v[212:215], v[204:207], a[28:31]\n"
"ds_read_b64_tr_b16 v[212:213], v247 offset:160\n"
"s_waitcnt lgkmcnt(5)\n"
"v_mfma_f32_16x16x32_bf16 a[32:35], v[216:219], v[192:195], a[32:35]\n"
"ds_read_b64_tr_b16 v[214:215], v247 offset:8608\n"
"s_add_u32 m0, s44, 34336\n"
"v_mfma_f32_16x16x32_bf16 a[36:39], v[216:219], v[196:199], a[36:39]\n"
"v_exp_f32 v130, v130\n"
"v_mfma_f32_16x16x32_bf16 a[40:43], v[216:219], v[200:203], a[40:43]\n"
"v_exp_f32 v131, v131\n"
"v_mfma_f32_16x16x32_bf16 a[44:47], v[216:219], v[204:207], a[44:47]\n"
"ds_read_b64_tr_b16 v[216:217], v247 offset:192\n"
"s_waitcnt lgkmcnt(5)\n"
"v_mfma_f32_16x16x32_bf16 a[48:51], v[220:223], v[192:195], a[48:51]\n"
"ds_read_b64_tr_b16 v[218:219], v247 offset:8640\n"
"buffer_load_dwordx4 v248, s[12:15], s54 offen offset:512 nt lds\n"
"v_mfma_f32_16x16x32_bf16 a[52:55], v[220:223], v[196:199], a[52:55]\n"
"v_exp_f32 v132, v132\n"
"v_mfma_f32_16x16x32_bf16 a[56:59], v[220:223], v[200:203], a[56:59]\n"
"v_exp_f32 v133, v133\n"
"v_mfma_f32_16x16x32_bf16 a[60:63], v[220:223], v[204:207], a[60:63]\n"
"ds_read_b64_tr_b16 v[220:221], v247 offset:224\n"
"s_waitcnt lgkmcnt(5)\n"
"v_mfma_f32_16x16x32_bf16 a[64:67], v[208:211], v[192:195], a[64:67]\n"
"ds_read_b64_tr_b16 v[222:223], v247 offset:8672\n"
"s_add_u32 m0, s44, 34880\n"
"v_mfma_f32_16x16x32_bf16 a[68:71], v[208:211], v[196:199], a[68:71]\n"
"v_exp_f32 v134, v134\n"
"v_mfma_f32_16x16x32_bf16 a[72:75], v[208:211], v[200:203], a[72:75]\n"
"v_exp_f32 v135, v135\n"
"v_mfma_f32_16x16x32_bf16 a[76:79], v[208:211], v[204:207], a[76:79]\n"
"ds_read_b64_tr_b16 v[208:209], v247 offset:256\n"
"s_waitcnt lgkmcnt(5)\n"
"v_mfma_f32_16x16x32_bf16 a[80:83], v[212:215], v[192:195], a[80:83]\n"
"ds_read_b64_tr_b16 v[210:211], v247 offset:8704\n"
"buffer_load_dwordx4 v248, s[12:15], s54 offen offset:1024 nt lds\n"
"v_mfma_f32_16x16x32_bf16 a[84:87], v[212:215], v[196:199], a[84:87]\n"
"v_exp_f32 v136, v136\n"
"v_mfma_f32_16x16x32_bf16 a[88:91], v[212:215], v[200:203], a[88:91]\n"
"v_exp_f32 v137, v137\n"
"v_mfma_f32_16x16x32_bf16 a[92:95], v[212:215], v[204:207], a[92:95]\n"
"ds_read_b64_tr_b16 v[212:213], v247 offset:288\n"
"s_waitcnt lgkmcnt(5)\n"
"v_mfma_f32_16x16x32_bf16 a[96:99], v[216:219], v[192:195], a[96:99]\n"
"ds_read_b64_tr_b16 v[214:215], v247 offset:8736\n"
"s_add_u32 m0, s44, 35424\n"
"v_mfma_f32_16x16x32_bf16 a[100:103], v[216:219], v[196:199], a[100:103]\n"
"v_exp_f32 v138, v138\n"
"v_mfma_f32_16x16x32_bf16 a[104:107], v[216:219], v[200:203], a[104:107]\n"
"v_exp_f32 v139, v139\n"
"v_mfma_f32_16x16x32_bf16 a[108:111], v[216:219], v[204:207], a[108:111]\n"
"ds_read_b64_tr_b16 v[216:217], v247 offset:320\n"
"s_waitcnt lgkmcnt(5)\n"
"v_mfma_f32_16x16x32_bf16 a[112:115], v[220:223], v[192:195], a[112:115]\n"
"ds_read_b64_tr_b16 v[218:219], v247 offset:8768\n"
"buffer_load_dwordx4 v248, s[12:15], s54 offen offset:1536 nt lds\n"
"v_mfma_f32_16x16x32_bf16 a[116:119], v[220:223], v[196:199], a[116:119]\n"
"v_exp_f32 v140, v140\n"
"v_mfma_f32_16x16x32_bf16 a[120:123], v[220:223], v[200:203], a[120:123]\n"
"v_exp_f32 v141, v141\n"
"v_mfma_f32_16x16x32_bf16 a[124:127], v[220:223], v[204:207], a[124:127]\n"
"ds_read_b64_tr_b16 v[220:221], v247 offset:352\n"
"s_waitcnt lgkmcnt(5)\n"
"v_mfma_f32_16x16x32_bf16 a[128:131], v[208:211], v[192:195], a[128:131]\n"
"ds_read_b64_tr_b16 v[222:223], v247 offset:8800\n"
"s_add_u32 m0, s44, 35968\n"
"v_mfma_f32_16x16x32_bf16 a[132:135], v[208:211], v[196:199], a[132:135]\n"
"v_exp_f32 v142, v142\n"
"v_mfma_f32_16x16x32_bf16 a[136:139], v[208:211], v[200:203], a[136:139]\n"
"v_exp_f32 v143, v143\n"
"v_mfma_f32_16x16x32_bf16 a[140:143], v[208:211], v[204:207], a[140:143]\n"
"ds_read_b64_tr_b16 v[208:209], v247 offset:384\n"
"s_waitcnt lgkmcnt(5)\n"
"v_mfma_f32_16x16x32_bf16 a[144:147], v[212:215], v[192:195], a[144:147]\n"
"ds_read_b64_tr_b16 v[210:211], v247 offset:8832\n"
"buffer_load_dwordx4 v248, s[12:15], s54 offen offset:2048 nt lds\n"
"v_mfma_f32_16x16x32_bf16 a[148:151], v[212:215], v[196:199], a[148:151]\n"
"v_exp_f32 v144, v144\n"
"v_mfma_f32_16x16x32_bf16 a[152:155], v[212:215], v[200:203], a[152:155]\n"
"v_exp_f32 v145, v145\n"
"v_mfma_f32_16x16x32_bf16 a[156:159], v[212:215], v[204:207], a[156:159]\n"
"ds_read_b64_tr_b16 v[212:213], v247 offset:416\n"
"s_waitcnt lgkmcnt(5)\n"
"v_mfma_f32_16x16x32_bf16 a[160:163], v[216:219], v[192:195], a[160:163]\n"
"ds_read_b64_tr_b16 v[214:215], v247 offset:8864\n"
"s_add_u32 m0, s44, 36512\n"
"v_mfma_f32_16x16x32_bf16 a[164:167], v[216:219], v[196:199], a[164:167]\n"
"v_exp_f32 v146, v146\n"
"v_mfma_f32_16x16x32_bf16 a[168:171], v[216:219], v[200:203], a[168:171]\n"
"v_exp_f32 v147, v147\n"
"v_mfma_f32_16x16x32_bf16 a[172:175], v[216:219], v[204:207], a[172:175]\n"
"ds_read_b64_tr_b16 v[216:217], v247 offset:448\n"
"s_waitcnt lgkmcnt(5)\n"
"v_mfma_f32_16x16x32_bf16 a[176:179], v[220:223], v[192:195], a[176:179]\n"
"ds_read_b64_tr_b16 v[218:219], v247 offset:8896\n"
"buffer_load_dwordx4 v248, s[12:15], s54 offen offset:2560 nt lds\n"
"v_mfma_f32_16x16x32_bf16 a[180:183], v[220:223], v[196:199], a[180:183]\n"
"v_exp_f32 v148, v148\n"
"v_mfma_f32_16x16x32_bf16 a[184:187], v[220:223], v[200:203], a[184:187]\n"
"v_exp_f32 v149, v149\n"
"v_mfma_f32_16x16x32_bf16 a[188:191], v[220:223], v[204:207], a[188:191]\n"
"ds_read_b64_tr_b16 v[220:221], v247 offset:480\n"
"s_waitcnt lgkmcnt(5)\n"
"v_mfma_f32_16x16x32_bf16 a[192:195], v[208:211], v[192:195], a[192:195]\n"
"ds_read_b64_tr_b16 v[222:223], v247 offset:8928\n"
"s_add_u32 m0, s44, 37056\n"
"v_mfma_f32_16x16x32_bf16 a[196:199], v[208:211], v[196:199], a[196:199]\n"
"v_exp_f32 v150, v150\n"
"v_mfma_f32_16x16x32_bf16 a[200:203], v[208:211], v[200:203], a[200:203]\n"
"v_exp_f32 v151, v151\n"
"v_mfma_f32_16x16x32_bf16 a[204:207], v[208:211], v[204:207], a[204:207]\n"
"s_waitcnt lgkmcnt(4)\n"
"v_mfma_f32_16x16x32_bf16 a[208:211], v[212:215], v[192:195], a[208:211]\n"
"buffer_load_dwordx4 v248, s[12:15], s54 offen offset:3072 nt lds\n"
"v_mfma_f32_16x16x32_bf16 a[212:215], v[212:215], v[196:199], a[212:215]\n"
"v_exp_f32 v152, v152\n"
"v_mfma_f32_16x16x32_bf16 a[216:219], v[212:215], v[200:203], a[216:219]\n"
"s_waitcnt vmcnt(15) lgkmcnt(0)\n"
"s_barrier\n"
"ds_read_b128 v[208:211], v245 offset:33792\n"
"v_mfma_f32_16x16x32_bf16 a[220:223], v[212:215], v[204:207], a[220:223]\n"
"v_mfma_f32_16x16x32_bf16 a[224:227], v[216:219], v[192:195], a[224:227]\n"
"ds_read_b128 v[212:215], v245 offset:34304\n"
"s_add_u32 m0, s44, 37600\n"
"v_mfma_f32_16x16x32_bf16 a[228:231], v[216:219], v[196:199], a[228:231]\n"
"v_exp_f32 v153, v153\n"
"v_mfma_f32_16x16x32_bf16 a[232:235], v[216:219], v[200:203], a[232:235]\n"
"v_mfma_f32_16x16x32_bf16 a[236:239], v[216:219], v[204:207], a[236:239]\n"
"ds_read_b128 v[216:219], v245 offset:33856\n"
"v_mfma_f32_16x16x32_bf16 a[240:243], v[220:223], v[192:195], a[240:243]\n"
"buffer_load_dwordx4 v248, s[12:15], s54 offen offset:3584 nt lds\n"
"s_add_u32 s54, s54, 16384\n"
"v_mfma_f32_16x16x32_bf16 a[244:247], v[220:223], v[196:199], a[244:247]\n"
"v_mfma_f32_16x16x32_bf16 a[248:251], v[220:223], v[200:203], a[248:251]\n"
"v_mfma_f32_16x16x32_bf16 a[252:255], v[220:223], v[204:207], a[252:255]\n"
"ds_read_b128 v[220:223], v245 offset:34368\n"
"s_waitcnt lgkmcnt(3)\n"
"v_mfma_f32_16x16x32_bf16 v[160:163], v[208:211], v[0:3], v[224:227]\n"
"v_cvt_pk_bf16_f32 v192, v128, v129\n"
"v_add_f32 v128, v128, v129\n"
"v_mfma_f32_16x16x32_bf16 v[168:171], v[208:211], v[4:7], v[228:231]\n"
"s_add_u32 m0, s44, 67584\n"
"v_cvt_pk_bf16_f32 v193, v130, v131\n"
"v_mfma_f32_16x16x32_bf16 v[176:179], v[208:211], v[8:11], v[232:235]\n"
"v_add_f32 v130, v130, v131\n"
"v_cvt_pk_bf16_f32 v194, v132, v133\n"
"v_mfma_f32_16x16x32_bf16 v[184:187], v[208:211], v[12:15], v[236:239]\n"
"ds_read_b128 v[208:211], v245 offset:33920\n"
"s_waitcnt lgkmcnt(3)\n"
"v_mfma_f32_16x16x32_bf16 v[164:167], v[212:215], v[0:3], v[224:227]\n"
"v_add_f32 v132, v132, v133\n"
"v_add_f32 v128, v128, v130\n"
"v_mfma_f32_16x16x32_bf16 v[172:175], v[212:215], v[4:7], v[228:231]\n"
"buffer_load_dwordx4 v249, s[16:19], s55 offen nt lds\n"
"v_cvt_pk_bf16_f32 v195, v134, v135\n"
"v_mfma_f32_16x16x32_bf16 v[180:183], v[212:215], v[8:11], v[232:235]\n"
"v_add_f32 v134, v134, v135\n"
"v_add_f32 v128, v128, v132\n"
"v_mfma_f32_16x16x32_bf16 v[188:191], v[212:215], v[12:15], v[236:239]\n"
"ds_read_b128 v[212:215], v245 offset:34432\n"
"s_waitcnt lgkmcnt(2)\n"
"v_mfma_f32_16x16x32_bf16 v[160:163], v[216:219], v[16:19], v[160:163]\n"
"v_add_f32 v128, v128, v134\n"
"v_cvt_pk_bf16_f32 v196, v136, v137\n"
"v_mfma_f32_16x16x32_bf16 v[168:171], v[216:219], v[20:23], v[168:171]\n"
"s_add_u32 m0, s44, 68128\n"
"v_add_f32 v136, v136, v137\n"
"v_mfma_f32_16x16x32_bf16 v[176:179], v[216:219], v[24:27], v[176:179]\n"
"v_cvt_pk_bf16_f32 v197, v138, v139\n"
"v_add_f32 v138, v138, v139\n"
"v_mfma_f32_16x16x32_bf16 v[184:187], v[216:219], v[28:31], v[184:187]\n"
"ds_read_b128 v[216:219], v245 offset:33984\n"
"v_mfma_f32_16x16x32_bf16 v[164:167], v[220:223], v[16:19], v[164:167]\n"
"v_cvt_pk_bf16_f32 v198, v140, v141\n"
"v_add_f32 v140, v140, v141\n"
"v_mfma_f32_16x16x32_bf16 v[172:175], v[220:223], v[20:23], v[172:175]\n"
"buffer_load_dwordx4 v249, s[16:19], s55 offen offset:512 nt lds\n"
"v_add_f32 v136, v136, v138\n"
"v_mfma_f32_16x16x32_bf16 v[180:183], v[220:223], v[24:27], v[180:183]\n"
"v_cvt_pk_bf16_f32 v199, v142, v143\n"
"v_add_f32 v142, v142, v143\n"
"v_mfma_f32_16x16x32_bf16 v[188:191], v[220:223], v[28:31], v[188:191]\n"
"ds_read_b128 v[220:223], v245 offset:34496\n"
"s_waitcnt lgkmcnt(2)\n"
"v_mfma_f32_16x16x32_bf16 v[160:163], v[208:211], v[32:35], v[160:163]\n"
"v_add_f32 v136, v136, v140\n"
"v_add_f32 v136, v136, v142\n"
"v_mfma_f32_16x16x32_bf16 v[168:171], v[208:211], v[36:39], v[168:171]\n"
"s_add_u32 m0, s44, 68672\n"
"v_mfma_f32_16x16x32_bf16 v[176:179], v[208:211], v[40:43], v[176:179]\n"
"v_exp_f32 v154, v154\n"
"v_mfma_f32_16x16x32_bf16 v[184:187], v[208:211], v[44:47], v[184:187]\n"
"ds_read_b128 v[208:211], v245 offset:34048\n"
"v_mfma_f32_16x16x32_bf16 v[164:167], v[212:215], v[32:35], v[164:167]\n"
"v_cvt_pk_bf16_f32 v200, v144, v145\n"
"v_add_f32 v144, v144, v145\n"
"v_mfma_f32_16x16x32_bf16 v[172:175], v[212:215], v[36:39], v[172:175]\n"
"buffer_load_dwordx4 v249, s[16:19], s55 offen offset:1024 nt lds\n"
"v_mfma_f32_16x16x32_bf16 v[180:183], v[212:215], v[40:43], v[180:183]\n"
"v_exp_f32 v155, v155\n"
"v_mfma_f32_16x16x32_bf16 v[188:191], v[212:215], v[44:47], v[188:191]\n"
"ds_read_b128 v[212:215], v245 offset:34560\n"
"s_waitcnt lgkmcnt(2)\n"
"v_mfma_f32_16x16x32_bf16 v[160:163], v[216:219], v[48:51], v[160:163]\n"
"v_cvt_pk_bf16_f32 v201, v146, v147\n"
"v_add_f32 v146, v146, v147\n"
"v_mfma_f32_16x16x32_bf16 v[168:171], v[216:219], v[52:55], v[168:171]\n"
"s_add_u32 m0, s44, 69216\n"
"v_mfma_f32_16x16x32_bf16 v[176:179], v[216:219], v[56:59], v[176:179]\n"
"v_exp_f32 v156, v156\n"
"v_mfma_f32_16x16x32_bf16 v[184:187], v[216:219], v[60:63], v[184:187]\n"
"ds_read_b128 v[216:219], v245 offset:34112\n"
"v_mfma_f32_16x16x32_bf16 v[164:167], v[220:223], v[48:51], v[164:167]\n"
"v_cvt_pk_bf16_f32 v202, v148, v149\n"
"v_add_f32 v148, v148, v149\n"
"v_mfma_f32_16x16x32_bf16 v[172:175], v[220:223], v[52:55], v[172:175]\n"
"buffer_load_dwordx4 v249, s[16:19], s55 offen offset:1536 nt lds\n"
"v_mfma_f32_16x16x32_bf16 v[180:183], v[220:223], v[56:59], v[180:183]\n"
"v_exp_f32 v157, v157\n"
"v_mfma_f32_16x16x32_bf16 v[188:191], v[220:223], v[60:63], v[188:191]\n"
"ds_read_b128 v[220:223], v245 offset:34624\n"
"s_waitcnt lgkmcnt(2)\n"
"v_mfma_f32_16x16x32_bf16 v[160:163], v[208:211], v[64:67], v[160:163]\n"
"v_add_f32 v144, v144, v146\n"
"v_cvt_pk_bf16_f32 v203, v150, v151\n"
"v_mfma_f32_16x16x32_bf16 v[168:171], v[208:211], v[68:71], v[168:171]\n"
"s_add_u32 m0, s44, 69760\n"
"v_mfma_f32_16x16x32_bf16 v[176:179], v[208:211], v[72:75], v[176:179]\n"
"v_exp_f32 v158, v158\n"
"v_mfma_f32_16x16x32_bf16 v[184:187], v[208:211], v[76:79], v[184:187]\n"
"ds_read_b128 v[208:211], v245 offset:34176\n"
"v_mfma_f32_16x16x32_bf16 v[164:167], v[212:215], v[64:67], v[164:167]\n"
"v_add_f32 v150, v150, v151\n"
"v_add_f32 v144, v144, v148\n"
"v_mfma_f32_16x16x32_bf16 v[172:175], v[212:215], v[68:71], v[172:175]\n"
"buffer_load_dwordx4 v249, s[16:19], s55 offen offset:2048 nt lds\n"
"v_mfma_f32_16x16x32_bf16 v[180:183], v[212:215], v[72:75], v[180:183]\n"
"v_exp_f32 v159, v159\n"
"v_mfma_f32_16x16x32_bf16 v[188:191], v[212:215], v[76:79], v[188:191]\n"
"ds_read_b128 v[212:215], v245 offset:34688\n"
"s_waitcnt lgkmcnt(2)\n"
"v_mfma_f32_16x16x32_bf16 v[160:163], v[216:219], v[80:83], v[160:163]\n"
"v_add_f32 v144, v144, v150\n"
"v_cvt_pk_bf16_f32 v204, v152, v153\n"
"v_mfma_f32_16x16x32_bf16 v[168:171], v[216:219], v[84:87], v[168:171]\n"
"s_add_u32 m0, s44, 70304\n"
"v_add_f32 v152, v152, v153\n"
"v_mfma_f32_16x16x32_bf16 v[176:179], v[216:219], v[88:91], v[176:179]\n"
"v_cvt_pk_bf16_f32 v205, v154, v155\n"
"v_add_f32 v154, v154, v155\n"
"v_mfma_f32_16x16x32_bf16 v[184:187], v[216:219], v[92:95], v[184:187]\n"
"ds_read_b128 v[216:219], v245 offset:34240\n"
"v_mfma_f32_16x16x32_bf16 v[164:167], v[220:223], v[80:83], v[164:167]\n"
"v_cvt_pk_bf16_f32 v206, v156, v157\n"
"v_add_f32 v156, v156, v157\n"
"v_mfma_f32_16x16x32_bf16 v[172:175], v[220:223], v[84:87], v[172:175]\n"
"buffer_load_dwordx4 v249, s[16:19], s55 offen offset:2560 nt lds\n"
"v_add_f32 v152, v152, v154\n"
"v_mfma_f32_16x16x32_bf16 v[180:183], v[220:223], v[88:91], v[180:183]\n"
"v_cvt_pk_bf16_f32 v207, v158, v159\n"
"v_add_f32 v158, v158, v159\n"
"v_mfma_f32_16x16x32_bf16 v[188:191], v[220:223], v[92:95], v[188:191]\n"
"ds_read_b128 v[220:223], v245 offset:34752\n"
"s_waitcnt lgkmcnt(3)\n"
"v_mfma_f32_16x16x32_bf16 v[160:163], v[208:211], v[96:99], v[160:163]\n"
"v_add_f32 v152, v152, v156\n"
"v_add_f32 v152, v152, v158\n"
"v_mfma_f32_16x16x32_bf16 v[168:171], v[208:211], v[100:103], v[168:171]\n"
"s_add_u32 m0, s44, 70848\n"
"v_max3_f32 v129, v128, v136, v144\n"
"v_mfma_f32_16x16x32_bf16 v[176:179], v[208:211], v[104:107], v[176:179]\n"
"v_max_f32 v129, v129, v152\n"
"v_cmp_nge_f32 vcc, 0x43800000, v129\n"
"v_mfma_f32_16x16x32_bf16 v[184:187], v[208:211], v[108:111], v[184:187]\n"
"s_waitcnt lgkmcnt(2)\n"
"v_mfma_f32_16x16x32_bf16 v[164:167], v[212:215], v[96:99], v[164:167]\n"
"v_mfma_f32_16x16x32_bf16 v[172:175], v[212:215], v[100:103], v[172:175]\n"
"buffer_load_dwordx4 v249, s[16:19], s55 offen offset:3072 nt lds\n"
"v_mfma_f32_16x16x32_bf16 v[180:183], v[212:215], v[104:107], v[180:183]\n"
"s_waitcnt vmcnt(15) lgkmcnt(0)\n"
"s_barrier\n"
"ds_read_b64_tr_b16 v[208:209], v246 offset:0\n"
"v_mfma_f32_16x16x32_bf16 v[188:191], v[212:215], v[108:111], v[188:191]\n"
"ds_read_b64_tr_b16 v[210:211], v246 offset:8448\n"
"v_mfma_f32_16x16x32_bf16 v[160:163], v[216:219], v[112:115], v[160:163]\n"
"ds_read_b64_tr_b16 v[212:213], v246 offset:32\n"
"v_mfma_f32_16x16x32_bf16 v[168:171], v[216:219], v[116:119], v[168:171]\n"
"ds_read_b64_tr_b16 v[214:215], v246 offset:8480\n"
"s_add_u32 m0, s44, 71392\n"
"v_mfma_f32_16x16x32_bf16 v[176:179], v[216:219], v[120:123], v[176:179]\n"
"v_mfma_f32_16x16x32_bf16 v[184:187], v[216:219], v[124:127], v[184:187]\n"
"ds_read_b64_tr_b16 v[216:217], v246 offset:64\n"
"v_mfma_f32_16x16x32_bf16 v[164:167], v[220:223], v[112:115], v[164:167]\n"
"ds_read_b64_tr_b16 v[218:219], v246 offset:8512\n"
"v_mfma_f32_16x16x32_bf16 v[172:175], v[220:223], v[116:119], v[172:175]\n"
"buffer_load_dwordx4 v249, s[16:19], s55 offen offset:3584 nt lds\n"
"s_add_u32 s55, s55, 16384\n"
"v_mfma_f32_16x16x32_bf16 v[180:183], v[220:223], v[120:123], v[180:183]\n"
"v_mfma_f32_16x16x32_bf16 v[188:191], v[220:223], v[124:127], v[188:191]\n"
"ds_read_b64_tr_b16 v[220:221], v246 offset:96\n"
"ds_read_b64_tr_b16 v[222:223], v246 offset:8544\n"
"s_cbranch_vccz LR_3_%=\n"
"s_getpc_b64 s[72:73]\n"
"s_branch LP10_%=\n"
"LR_3_%=:\n"
"s_waitcnt lgkmcnt(6)\n"
"v_mfma_f32_16x16x32_bf16 a[0:3], v[208:211], v[192:195], a[0:3]\n"
"s_add_u32 m0, s44, 101376\n"
"v_mfma_f32_16x16x32_bf16 a[4:7], v[208:211], v[196:199], a[4:7]\n"
"v_add_f32 v240, v240, v128\n"
"v_add_f32 v241, v241, v136\n"
"v_mfma_f32_16x16x32_bf16 a[8:11], v[208:211], v[200:203], a[8:11]\n"
"v_add_f32 v242, v242, v144\n"
"v_add_f32 v243, v243, v152\n"
"v_mfma_f32_16x16x32_bf16 a[12:15], v[208:211], v[204:207], a[12:15]\n"
"ds_read_b64_tr_b16 v[208:209], v246 offset:128\n"
"s_waitcnt lgkmcnt(5)\n"
"v_mfma_f32_16x16x32_bf16 a[16:19], v[212:215], v[192:195], a[16:19]\n"
"ds_read_b64_tr_b16 v[210:211], v246 offset:8576\n"
"buffer_load_dwordx4 v248, s[12:15], s54 offen nt lds\n"
"v_mfma_f32_16x16x32_bf16 a[20:23], v[212:215], v[196:199], a[20:23]\n"
"v_exp_f32 v160, v160\n"
"v_mfma_f32_16x16x32_bf16 a[24:27], v[212:215], v[200:203], a[24:27]\n"
"v_exp_f32 v161, v161\n"
"v_mfma_f32_16x16x32_bf16 a[28:31], v[212:215], v[204:207], a[28:31]\n"
"ds_read_b64_tr_b16 v[212:213], v246 offset:160\n"
"s_waitcnt lgkmcnt(5)\n"
"v_mfma_f32_16x16x32_bf16 a[32:35], v[216:219], v[192:195], a[32:35]\n"
"ds_read_b64_tr_b16 v[214:215], v246 offset:8608\n"
"s_add_u32 m0, s44, 101920\n"
"v_mfma_f32_16x16x32_bf16 a[36:39], v[216:219], v[196:199], a[36:39]\n"
"v_exp_f32 v162, v162\n"
"v_mfma_f32_16x16x32_bf16 a[40:43], v[216:219], v[200:203], a[40:43]\n"
"v_exp_f32 v163, v163\n"
"v_mfma_f32_16x16x32_bf16 a[44:47], v[216:219], v[204:207], a[44:47]\n"
"ds_read_b64_tr_b16 v[216:217], v246 offset:192\n"
"s_waitcnt lgkmcnt(5)\n"
"v_mfma_f32_16x16x32_bf16 a[48:51], v[220:223], v[192:195], a[48:51]\n"
"ds_read_b64_tr_b16 v[218:219], v246 offset:8640\n"
"buffer_load_dwordx4 v248, s[12:15], s54 offen offset:512 nt lds\n"
"v_mfma_f32_16x16x32_bf16 a[52:55], v[220:223], v[196:199], a[52:55]\n"
"v_exp_f32 v164, v164\n"
"v_mfma_f32_16x16x32_bf16 a[56:59], v[220:223], v[200:203], a[56:59]\n"
"v_exp_f32 v165, v165\n"
"v_mfma_f32_16x16x32_bf16 a[60:63], v[220:223], v[204:207], a[60:63]\n"
"ds_read_b64_tr_b16 v[220:221], v246 offset:224\n"
"s_waitcnt lgkmcnt(5)\n"
"v_mfma_f32_16x16x32_bf16 a[64:67], v[208:211], v[192:195], a[64:67]\n"
"ds_read_b64_tr_b16 v[222:223], v246 offset:8672\n"
"s_add_u32 m0, s44, 102464\n"
"v_mfma_f32_16x16x32_bf16 a[68:71], v[208:211], v[196:199], a[68:71]\n"
"v_exp_f32 v166, v166\n"
"v_mfma_f32_16x16x32_bf16 a[72:75], v[208:211], v[200:203], a[72:75]\n"
"v_exp_f32 v167, v167\n"
"v_mfma_f32_16x16x32_bf16 a[76:79], v[208:211], v[204:207], a[76:79]\n"
"ds_read_b64_tr_b16 v[208:209], v246 offset:256\n"
"s_waitcnt lgkmcnt(5)\n"
"v_mfma_f32_16x16x32_bf16 a[80:83], v[212:215], v[192:195], a[80:83]\n"
"ds_read_b64_tr_b16 v[210:211], v246 offset:8704\n"
"buffer_load_dwordx4 v248, s[12:15], s54 offen offset:1024 nt lds\n"
"v_mfma_f32_16x16x32_bf16 a[84:87], v[212:215], v[196:199], a[84:87]\n"
"v_exp_f32 v168, v168\n"
"v_mfma_f32_16x16x32_bf16 a[88:91], v[212:215], v[200:203], a[88:91]\n"
"v_exp_f32 v169, v169\n"
"v_mfma_f32_16x16x32_bf16 a[92:95], v[212:215], v[204:207], a[92:95]\n"
"ds_read_b64_tr_b16 v[212:213], v246 offset:288\n"
"s_waitcnt lgkmcnt(5)\n"
"v_mfma_f32_16x16x32_bf16 a[96:99], v[216:219], v[192:195], a[96:99]\n"
"ds_read_b64_tr_b16 v[214:215], v246 offset:8736\n"
"s_add_u32 m0, s44, 103008\n"
"v_mfma_f32_16x16x32_bf16 a[100:103], v[216:219], v[196:199], a[100:103]\n"
"v_exp_f32 v170, v170\n"
"v_mfma_f32_16x16x32_bf16 a[104:107], v[216:219], v[200:203], a[104:107]\n"
"v_exp_f32 v171, v171\n"
"v_mfma_f32_16x16x32_bf16 a[108:111], v[216:219], v[204:207], a[108:111]\n"
"ds_read_b64_tr_b16 v[216:217], v246 offset:320\n"
"s_waitcnt lgkmcnt(5)\n"
"v_mfma_f32_16x16x32_bf16 a[112:115], v[220:223], v[192:195], a[112:115]\n"
"ds_read_b64_tr_b16 v[218:219], v246 offset:8768\n"
"buffer_load_dwordx4 v248, s[12:15], s54 offen offset:1536 nt lds\n"
"v_mfma_f32_16x16x32_bf16 a[116:119], v[220:223], v[196:199], a[116:119]\n"
"v_exp_f32 v172, v172\n"
"v_mfma_f32_16x16x32_bf16 a[120:123], v[220:223], v[200:203], a[120:123]\n"
"v_exp_f32 v173, v173\n"
"v_mfma_f32_16x16x32_bf16 a[124:127], v[220:223], v[204:207], a[124:127]\n"
"ds_read_b64_tr_b16 v[220:221], v246 offset:352\n"
"s_waitcnt lgkmcnt(5)\n"
"v_mfma_f32_16x16x32_bf16 a[128:131], v[208:211], v[192:195], a[128:131]\n"
"ds_read_b64_tr_b16 v[222:223], v246 offset:8800\n"
"s_add_u32 m0, s44, 103552\n"
"v_mfma_f32_16x16x32_bf16 a[132:135], v[208:211], v[196:199], a[132:135]\n"
"v_exp_f32 v174, v174\n"
"v_mfma_f32_16x16x32_bf16 a[136:139], v[208:211], v[200:203], a[136:139]\n"
"v_exp_f32 v175, v175\n"
"v_mfma_f32_16x16x32_bf16 a[140:143], v[208:211], v[204:207], a[140:143]\n"
"ds_read_b64_tr_b16 v[208:209], v246 offset:384\n"
"s_waitcnt lgkmcnt(5)\n"
"v_mfma_f32_16x16x32_bf16 a[144:147], v[212:215], v[192:195], a[144:147]\n"
"ds_read_b64_tr_b16 v[210:211], v246 offset:8832\n"
"buffer_load_dwordx4 v248, s[12:15], s54 offen offset:2048 nt lds\n"
"v_mfma_f32_16x16x32_bf16 a[148:151], v[212:215], v[196:199], a[148:151]\n"
"v_exp_f32 v176, v176\n"
"v_mfma_f32_16x16x32_bf16 a[152:155], v[212:215], v[200:203], a[152:155]\n"
"v_exp_f32 v177, v177\n"
"v_mfma_f32_16x16x32_bf16 a[156:159], v[212:215], v[204:207], a[156:159]\n"
"ds_read_b64_tr_b16 v[212:213], v246 offset:416\n"
"s_waitcnt lgkmcnt(5)\n"
"v_mfma_f32_16x16x32_bf16 a[160:163], v[216:219], v[192:195], a[160:163]\n"
"ds_read_b64_tr_b16 v[214:215], v246 offset:8864\n"
"s_add_u32 m0, s44, 104096\n"
"v_mfma_f32_16x16x32_bf16 a[164:167], v[216:219], v[196:199], a[164:167]\n"
"v_exp_f32 v178, v178\n"
"v_mfma_f32_16x16x32_bf16 a[168:171], v[216:219], v[200:203], a[168:171]\n"
"v_exp_f32 v179, v179\n"
"v_mfma_f32_16x16x32_bf16 a[172:175], v[216:219], v[204:207], a[172:175]\n"
"ds_read_b64_tr_b16 v[216:217], v246 offset:448\n"
"s_waitcnt lgkmcnt(5)\n"
"v_mfma_f32_16x16x32_bf16 a[176:179], v[220:223], v[192:195], a[176:179]\n"
"ds_read_b64_tr_b16 v[218:219], v246 offset:8896\n"
"buffer_load_dwordx4 v248, s[12:15], s54 offen offset:2560 nt lds\n"
"v_mfma_f32_16x16x32_bf16 a[180:183], v[220:223], v[196:199], a[180:183]\n"
"v_exp_f32 v180, v180\n"
"v_mfma_f32_16x16x32_bf16 a[184:187], v[220:223], v[200:203], a[184:187]\n"
"v_exp_f32 v181, v181\n"
"v_mfma_f32_16x16x32_bf16 a[188:191], v[220:223], v[204:207], a[188:191]\n"
"ds_read_b64_tr_b16 v[220:221], v246 offset:480\n"
"s_waitcnt lgkmcnt(5)\n"
"v_mfma_f32_16x16x32_bf16 a[192:195], v[208:211], v[192:195], a[192:195]\n"
"ds_read_b64_tr_b16 v[222:223], v246 offset:8928\n"
"s_add_u32 m0, s44, 104640\n"
"v_mfma_f32_16x16x32_bf16 a[196:199], v[208:211], v[196:199], a[196:199]\n"
"v_exp_f32 v182, v182\n"
"v_mfma_f32_16x16x32_bf16 a[200:203], v[208:211], v[200:203], a[200:203]\n"
"v_exp_f32 v183, v183\n"
"v_mfma_f32_16x16x32_bf16 a[204:207], v[208:211], v[204:207], a[204:207]\n"
"s_waitcnt lgkmcnt(4)\n"
"v_mfma_f32_16x16x32_bf16 a[208:211], v[212:215], v[192:195], a[208:211]\n"
"buffer_load_dwordx4 v248, s[12:15], s54 offen offset:3072 nt lds\n"
"v_mfma_f32_16x16x32_bf16 a[212:215], v[212:215], v[196:199], a[212:215]\n"
"v_exp_f32 v184, v184\n"
"v_mfma_f32_16x16x32_bf16 a[216:219], v[212:215], v[200:203], a[216:219]\n"
"s_waitcnt vmcnt(15) lgkmcnt(0)\n"
"s_barrier\n"
"ds_read_b128 v[208:211], v244 offset:33792\n"
"v_mfma_f32_16x16x32_bf16 a[220:223], v[212:215], v[204:207], a[220:223]\n"
"v_mfma_f32_16x16x32_bf16 a[224:227], v[216:219], v[192:195], a[224:227]\n"
"ds_read_b128 v[212:215], v244 offset:34304\n"
"s_add_u32 m0, s44, 105184\n"
"v_mfma_f32_16x16x32_bf16 a[228:231], v[216:219], v[196:199], a[228:231]\n"
"v_exp_f32 v185, v185\n"
"v_mfma_f32_16x16x32_bf16 a[232:235], v[216:219], v[200:203], a[232:235]\n"
"v_mfma_f32_16x16x32_bf16 a[236:239], v[216:219], v[204:207], a[236:239]\n"
"ds_read_b128 v[216:219], v244 offset:33856\n"
"v_mfma_f32_16x16x32_bf16 a[240:243], v[220:223], v[192:195], a[240:243]\n"
"buffer_load_dwordx4 v248, s[12:15], s54 offen offset:3584 nt lds\n"
"s_add_u32 s54, s54, 16384\n"
"v_mfma_f32_16x16x32_bf16 a[244:247], v[220:223], v[196:199], a[244:247]\n"
"v_mfma_f32_16x16x32_bf16 a[248:251], v[220:223], v[200:203], a[248:251]\n"
"v_mfma_f32_16x16x32_bf16 a[252:255], v[220:223], v[204:207], a[252:255]\n"
"ds_read_b128 v[220:223], v244 offset:34368\n"
"s_sub_u32 s64, s64, 1\n"
"s_cmp_lg_u32 s64, 0\n"
"s_cbranch_scc1 LA_%=\n"
"s_waitcnt lgkmcnt(3)\n"
"v_mfma_f32_16x16x32_bf16 v[128:131], v[208:211], v[0:3], v[224:227]\n"
"v_cvt_pk_bf16_f32 v192, v160, v161\n"
"v_add_f32 v160, v160, v161\n"
"v_mfma_f32_16x16x32_bf16 v[136:139], v[208:211], v[4:7], v[228:231]\n"
"s_add_u32 m0, s44, 0\n"
"v_cvt_pk_bf16_f32 v193, v162, v163\n"
"v_mfma_f32_16x16x32_bf16 v[144:147], v[208:211], v[8:11], v[232:235]\n"
"v_add_f32 v162, v162, v163\n"
"v_cvt_pk_bf16_f32 v194, v164, v165\n"
"v_mfma_f32_16x16x32_bf16 v[152:155], v[208:211], v[12:15], v[236:239]\n"
"ds_read_b128 v[208:211], v244 offset:33920\n"
"s_waitcnt lgkmcnt(3)\n"
"v_mfma_f32_16x16x32_bf16 v[132:135], v[212:215], v[0:3], v[224:227]\n"
"v_add_f32 v164, v164, v165\n"
"v_add_f32 v160, v160, v162\n"
"v_mfma_f32_16x16x32_bf16 v[140:143], v[212:215], v[4:7], v[228:231]\n"
"buffer_load_dwordx4 v249, s[16:19], s55 offen nt lds\n"
"v_cvt_pk_bf16_f32 v195, v166, v167\n"
"v_mfma_f32_16x16x32_bf16 v[148:151], v[212:215], v[8:11], v[232:235]\n"
"v_add_f32 v166, v166, v167\n"
"v_add_f32 v160, v160, v164\n"
"v_mfma_f32_16x16x32_bf16 v[156:159], v[212:215], v[12:15], v[236:239]\n"
"ds_read_b128 v[212:215], v244 offset:34432\n"
"s_waitcnt lgkmcnt(2)\n"
"v_mfma_f32_16x16x32_bf16 v[128:131], v[216:219], v[16:19], v[128:131]\n"
"v_add_f32 v160, v160, v166\n"
"v_cvt_pk_bf16_f32 v196, v168, v169\n"
"v_mfma_f32_16x16x32_bf16 v[136:139], v[216:219], v[20:23], v[136:139]\n"
"s_add_u32 m0, s44, 544\n"
"v_add_f32 v168, v168, v169\n"
"v_mfma_f32_16x16x32_bf16 v[144:147], v[216:219], v[24:27], v[144:147]\n"
"v_cvt_pk_bf16_f32 v197, v170, v171\n"
"v_add_f32 v170, v170, v171\n"
"v_mfma_f32_16x16x32_bf16 v[152:155], v[216:219], v[28:31], v[152:155]\n"
"ds_read_b128 v[216:219], v244 offset:33984\n"
"v_mfma_f32_16x16x32_bf16 v[132:135], v[220:223], v[16:19], v[132:135]\n"
"v_cvt_pk_bf16_f32 v198, v172, v173\n"
"v_add_f32 v172, v172, v173\n"
"v_mfma_f32_16x16x32_bf16 v[140:143], v[220:223], v[20:23], v[140:143]\n"
"buffer_load_dwordx4 v249, s[16:19], s55 offen offset:512 nt lds\n"
"v_add_f32 v168, v168, v170\n"
"v_mfma_f32_16x16x32_bf16 v[148:151], v[220:223], v[24:27], v[148:151]\n"
"v_cvt_pk_bf16_f32 v199, v174, v175\n"
"v_add_f32 v174, v174, v175\n"
"v_mfma_f32_16x16x32_bf16 v[156:159], v[220:223], v[28:31], v[156:159]\n"
"ds_read_b128 v[220:223], v244 offset:34496\n"
"s_waitcnt lgkmcnt(2)\n"
"v_mfma_f32_16x16x32_bf16 v[128:131], v[208:211], v[32:35], v[128:131]\n"
"v_add_f32 v168, v168, v172\n"
"v_add_f32 v168, v168, v174\n"
"v_mfma_f32_16x16x32_bf16 v[136:139], v[208:211], v[36:39], v[136:139]\n"
"s_add_u32 m0, s44, 1088\n"
"v_mfma_f32_16x16x32_bf16 v[144:147], v[208:211], v[40:43], v[144:147]\n"
"v_exp_f32 v186, v186\n"
"v_mfma_f32_16x16x32_bf16 v[152:155], v[208:211], v[44:47], v[152:155]\n"
"ds_read_b128 v[208:211], v244 offset:34048\n"
"v_mfma_f32_16x16x32_bf16 v[132:135], v[212:215], v[32:35], v[132:135]\n"
"v_cvt_pk_bf16_f32 v200, v176, v177\n"
"v_add_f32 v176, v176, v177\n"
"v_mfma_f32_16x16x32_bf16 v[140:143], v[212:215], v[36:39], v[140:143]\n"
"buffer_load_dwordx4 v249, s[16:19], s55 offen offset:1024 nt lds\n"
"v_mfma_f32_16x16x32_bf16 v[148:151], v[212:215], v[40:43], v[148:151]\n"
"v_exp_f32 v187, v187\n"
"v_mfma_f32_16x16x32_bf16 v[156:159], v[212:215], v[44:47], v[156:159]\n"
"ds_read_b128 v[212:215], v244 offset:34560\n"
"s_waitcnt lgkmcnt(2)\n"
"v_mfma_f32_16x16x32_bf16 v[128:131], v[216:219], v[48:51], v[128:131]\n"
"v_cvt_pk_bf16_f32 v201, v178, v179\n"
"v_add_f32 v178, v178, v179\n"
"v_mfma_f32_16x16x32_bf16 v[136:139], v[216:219], v[52:55], v[136:139]\n"
"s_add_u32 m0, s44, 1632\n"
"v_mfma_f32_16x16x32_bf16 v[144:147], v[216:219], v[56:59], v[144:147]\n"
"v_exp_f32 v188, v188\n"
"v_mfma_f32_16x16x32_bf16 v[152:155], v[216:219], v[60:63], v[152:155]\n"
"ds_read_b128 v[216:219], v244 offset:34112\n"
"v_mfma_f32_16x16x32_bf16 v[132:135], v[220:223], v[48:51], v[132:135]\n"
"v_cvt_pk_bf16_f32 v202, v180, v181\n"
"v_add_f32 v180, v180, v181\n"
"v_mfma_f32_16x16x32_bf16 v[140:143], v[220:223], v[52:55], v[140:143]\n"
"buffer_load_dwordx4 v249, s[16:19], s55 offen offset:1536 nt lds\n"
"v_mfma_f32_16x16x32_bf16 v[148:151], v[220:223], v[56:59], v[148:151]\n"
"v_exp_f32 v189, v189\n"
"v_mfma_f32_16x16x32_bf16 v[156:159], v[220:223], v[60:63], v[156:159]\n"
"ds_read_b128 v[220:223], v244 offset:34624\n"
"s_waitcnt lgkmcnt(2)\n"
"v_mfma_f32_16x16x32_bf16 v[128:131], v[208:211], v[64:67], v[128:131]\n"
"v_add_f32 v176, v176, v178\n"
"v_cvt_pk_bf16_f32 v203, v182, v183\n"
"v_mfma_f32_16x16x32_bf16 v[136:139], v[208:211], v[68:71], v[136:139]\n"
"s_add_u32 m0, s44, 2176\n"
"v_mfma_f32_16x16x32_bf16 v[144:147], v[208:211], v[72:75], v[144:147]\n"
"v_exp_f32 v190, v190\n"
"v_mfma_f32_16x16x32_bf16 v[152:155], v[208:211], v[76:79], v[152:155]\n"
"ds_read_b128 v[208:211], v244 offset:34176\n"
"v_mfma_f32_16x16x32_bf16 v[132:135], v[212:215], v[64:67], v[132:135]\n"
"v_add_f32 v182, v182, v183\n"
"v_add_f32 v176, v176, v180\n"
"v_mfma_f32_16x16x32_bf16 v[140:143], v[212:215], v[68:71], v[140:143]\n"
"buffer_load_dwordx4 v249, s[16:19], s55 offen offset:2048 nt lds\n"
"v_mfma_f32_16x16x32_bf16 v[148:151], v[212:215], v[72:75], v[148:151]\n"
"v_exp_f32 v191, v191\n"
"v_mfma_f32_16x16x32_bf16 v[156:159], v[212:215], v[76:79], v[156:159]\n"
"ds_read_b128 v[212:215], v244 offset:34688\n"
"s_waitcnt lgkmcnt(2)\n"
"v_mfma_f32_16x16x32_bf16 v[128:131], v[216:219], v[80:83], v[128:131]\n"
"v_add_f32 v176, v176, v182\n"
"v_cvt_pk_bf16_f32 v204, v184, v185\n"
"v_mfma_f32_16x16x32_bf16 v[136:139], v[216:219], v[84:87], v[136:139]\n"
"s_add_u32 m0, s44, 2720\n"
"v_add_f32 v184, v184, v185\n"
"v_mfma_f32_16x16x32_bf16 v[144:147], v[216:219], v[88:91], v[144:147]\n"
"v_cvt_pk_bf16_f32 v205, v186, v187\n"
"v_add_f32 v186, v186, v187\n"
"v_mfma_f32_16x16x32_bf16 v[152:155], v[216:219], v[92:95], v[152:155]\n"
"ds_read_b128 v[216:219], v244 offset:34240\n"
"v_mfma_f32_16x16x32_bf16 v[132:135], v[220:223], v[80:83], v[132:135]\n"
"v_cvt_pk_bf16_f32 v206, v188, v189\n"
"v_add_f32 v188, v188, v189\n"
"v_mfma_f32_16x16x32_bf16 v[140:143], v[220:223], v[84:87], v[140:143]\n"
"buffer_load_dwordx4 v249, s[16:19], s55 offen offset:2560 nt lds\n"
"v_add_f32 v184, v184, v186\n"
"v_mfma_f32_16x16x32_bf16 v[148:151], v[220:223], v[88:91], v[148:151]\n"
"v_cvt_pk_bf16_f32 v207, v190, v191\n"
"v_add_f32 v190, v190, v191\n"
"v_mfma_f32_16x16x32_bf16 v[156:159], v[220:223], v[92:95], v[156:159]\n"
"ds_read_b128 v[220:223], v244 offset:34752\n"
"s_waitcnt lgkmcnt(3)\n"
"v_mfma_f32_16x16x32_bf16 v[128:131], v[208:211], v[96:99], v[128:131]\n"
"v_add_f32 v184, v184, v188\n"
"v_add_f32 v184, v184, v190\n"
"v_mfma_f32_16x16x32_bf16 v[136:139], v[208:211], v[100:103], v[136:139]\n"
"s_add_u32 m0, s44, 3264\n"
"v_max3_f32 v161, v160, v168, v176\n"
"v_mfma_f32_16x16x32_bf16 v[144:147], v[208:211], v[104:107], v[144:147]\n"
"v_max_f32 v161, v161, v184\n"
"v_cmp_nge_f32 vcc, 0x43800000, v161\n"
"v_mfma_f32_16x16x32_bf16 v[152:155], v[208:211], v[108:111], v[152:155]\n"
"s_waitcnt lgkmcnt(2)\n"
"v_mfma_f32_16x16x32_bf16 v[132:135], v[212:215], v[96:99], v[132:135]\n"
"v_mfma_f32_16x16x32_bf16 v[140:143], v[212:215], v[100:103], v[140:143]\n"
"buffer_load_dwordx4 v249, s[16:19], s55 offen offset:3072 nt lds\n"
"v_mfma_f32_16x16x32_bf16 v[148:151], v[212:215], v[104:107], v[148:151]\n"
"s_waitcnt vmcnt(15) lgkmcnt(0)\n"
"s_barrier\n"
"ds_read_b64_tr_b16 v[208:209], v247 offset:0\n"
"v_mfma_f32_16x16x32_bf16 v[156:159], v[212:215], v[108:111], v[156:159]\n"
"ds_read_b64_tr_b16 v[210:211], v247 offset:8448\n"
"v_mfma_f32_16x16x32_bf16 v[128:131], v[216:219], v[112:115], v[128:131]\n"
"ds_read_b64_tr_b16 v[212:213], v247 offset:32\n"
"v_mfma_f32_16x16x32_bf16 v[136:139], v[216:219], v[116:119], v[136:139]\n"
"ds_read_b64_tr_b16 v[214:215], v247 offset:8480\n"
"s_add_u32 m0, s44, 3808\n"
"v_mfma_f32_16x16x32_bf16 v[144:147], v[216:219], v[120:123], v[144:147]\n"
"v_mfma_f32_16x16x32_bf16 v[152:155], v[216:219], v[124:127], v[152:155]\n"
"ds_read_b64_tr_b16 v[216:217], v247 offset:64\n"
"v_mfma_f32_16x16x32_bf16 v[132:135], v[220:223], v[112:115], v[132:135]\n"
"ds_read_b64_tr_b16 v[218:219], v247 offset:8512\n"
"v_mfma_f32_16x16x32_bf16 v[140:143], v[220:223], v[116:119], v[140:143]\n"
"buffer_load_dwordx4 v249, s[16:19], s55 offen offset:3584 nt lds\n"
"s_add_u32 s55, s55, 16384\n"
"v_mfma_f32_16x16x32_bf16 v[148:151], v[220:223], v[120:123], v[148:151]\n"
"v_mfma_f32_16x16x32_bf16 v[156:159], v[220:223], v[124:127], v[156:159]\n"
"ds_read_b64_tr_b16 v[220:221], v247 offset:96\n"
"ds_read_b64_tr_b16 v[222:223], v247 offset:8544\n"
"s_cbranch_vccz LR_14_%=\n"
"s_getpc_b64 s[72:73]\n"
"s_branch LP11_%=\n"
"LR_14_%=:\n"
"s_waitcnt lgkmcnt(6)\n"
"v_mfma_f32_16x16x32_bf16 a[0:3], v[208:211], v[192:195], a[0:3]\n"
"s_add_u32 m0, s44, 33792\n"
"v_mfma_f32_16x16x32_bf16 a[4:7], v[208:211], v[196:199], a[4:7]\n"
"v_add_f32 v240, v240, v160\n"
"v_add_f32 v241, v241, v168\n"
"v_mfma_f32_16x16x32_bf16 a[8:11], v[208:211], v[200:203], a[8:11]\n"
"v_add_f32 v242, v242, v176\n"
"v_add_f32 v243, v243, v184\n"
"v_mfma_f32_16x16x32_bf16 a[12:15], v[208:211], v[204:207], a[12:15]\n"
"ds_read_b64_tr_b16 v[208:209], v247 offset:128\n"
"s_waitcnt lgkmcnt(5)\n"
"v_mfma_f32_16x16x32_bf16 a[16:19], v[212:215], v[192:195], a[16:19]\n"
"ds_read_b64_tr_b16 v[210:211], v247 offset:8576\n"
"buffer_load_dwordx4 v249, s[16:19], s55 offen nt lds\n"
"v_mfma_f32_16x16x32_bf16 a[20:23], v[212:215], v[196:199], a[20:23]\n"
"v_exp_f32 v128, v128\n"
"v_mfma_f32_16x16x32_bf16 a[24:27], v[212:215], v[200:203], a[24:27]\n"
"v_exp_f32 v129, v129\n"
"v_mfma_f32_16x16x32_bf16 a[28:31], v[212:215], v[204:207], a[28:31]\n"
"ds_read_b64_tr_b16 v[212:213], v247 offset:160\n"
"s_waitcnt lgkmcnt(5)\n"
"v_mfma_f32_16x16x32_bf16 a[32:35], v[216:219], v[192:195], a[32:35]\n"
"ds_read_b64_tr_b16 v[214:215], v247 offset:8608\n"
"s_add_u32 m0, s44, 34336\n"
"v_mfma_f32_16x16x32_bf16 a[36:39], v[216:219], v[196:199], a[36:39]\n"
"v_exp_f32 v130, v130\n"
"v_mfma_f32_16x16x32_bf16 a[40:43], v[216:219], v[200:203], a[40:43]\n"
"v_exp_f32 v131, v131\n"
"v_mfma_f32_16x16x32_bf16 a[44:47], v[216:219], v[204:207], a[44:47]\n"
"ds_read_b64_tr_b16 v[216:217], v247 offset:192\n"
"s_waitcnt lgkmcnt(5)\n"
"v_mfma_f32_16x16x32_bf16 a[48:51], v[220:223], v[192:195], a[48:51]\n"
"ds_read_b64_tr_b16 v[218:219], v247 offset:8640\n"
"buffer_load_dwordx4 v249, s[16:19], s55 offen offset:512 nt lds\n"
"v_mfma_f32_16x16x32_bf16 a[52:55], v[220:223], v[196:199], a[52:55]\n"
"v_exp_f32 v132, v132\n"
"v_mfma_f32_16x16x32_bf16 a[56:59], v[220:223], v[200:203], a[56:59]\n"
"v_exp_f32 v133, v133\n"
"v_mfma_f32_16x16x32_bf16 a[60:63], v[220:223], v[204:207], a[60:63]\n"
"ds_read_b64_tr_b16 v[220:221], v247 offset:224\n"
"s_waitcnt lgkmcnt(5)\n"
"v_mfma_f32_16x16x32_bf16 a[64:67], v[208:211], v[192:195], a[64:67]\n"
"ds_read_b64_tr_b16 v[222:223], v247 offset:8672\n"
"s_add_u32 m0, s44, 34880\n"
"v_mfma_f32_16x16x32_bf16 a[68:71], v[208:211], v[196:199], a[68:71]\n"
"v_exp_f32 v134, v134\n"
"v_mfma_f32_16x16x32_bf16 a[72:75], v[208:211], v[200:203], a[72:75]\n"
"v_exp_f32 v135, v135\n"
"v_mfma_f32_16x16x32_bf16 a[76:79], v[208:211], v[204:207], a[76:79]\n"
"ds_read_b64_tr_b16 v[208:209], v247 offset:256\n"
"s_waitcnt lgkmcnt(5)\n"
"v_mfma_f32_16x16x32_bf16 a[80:83], v[212:215], v[192:195], a[80:83]\n"
"ds_read_b64_tr_b16 v[210:211], v247 offset:8704\n"
"buffer_load_dwordx4 v249, s[16:19], s55 offen offset:1024 nt lds\n"
"v_mfma_f32_16x16x32_bf16 a[84:87], v[212:215], v[196:199], a[84:87]\n"
"v_exp_f32 v136, v136\n"
"v_mfma_f32_16x16x32_bf16 a[88:91], v[212:215], v[200:203], a[88:91]\n"
"v_exp_f32 v137, v137\n"
"v_mfma_f32_16x16x32_bf16 a[92:95], v[212:215], v[204:207], a[92:95]\n"
"ds_read_b64_tr_b16 v[212:213], v247 offset:288\n"
"s_waitcnt lgkmcnt(5)\n"
"v_mfma_f32_16x16x32_bf16 a[96:99], v[216:219], v[192:195], a[96:99]\n"
"ds_read_b64_tr_b16 v[214:215], v247 offset:8736\n"
"s_add_u32 m0, s44, 35424\n"
"v_mfma_f32_16x16x32_bf16 a[100:103], v[216:219], v[196:199], a[100:103]\n"
"v_exp_f32 v138, v138\n"
"v_mfma_f32_16x16x32_bf16 a[104:107], v[216:219], v[200:203], a[104:107]\n"
"v_exp_f32 v139, v139\n"
"v_mfma_f32_16x16x32_bf16 a[108:111], v[216:219], v[204:207], a[108:111]\n"
"ds_read_b64_tr_b16 v[216:217], v247 offset:320\n"
"s_waitcnt lgkmcnt(5)\n"
"v_mfma_f32_16x16x32_bf16 a[112:115], v[220:223], v[192:195], a[112:115]\n"
"ds_read_b64_tr_b16 v[218:219], v247 offset:8768\n"
"buffer_load_dwordx4 v249, s[16:19], s55 offen offset:1536 nt lds\n"
"v_mfma_f32_16x16x32_bf16 a[116:119], v[220:223], v[196:199], a[116:119]\n"
"v_exp_f32 v140, v140\n"
"v_mfma_f32_16x16x32_bf16 a[120:123], v[220:223], v[200:203], a[120:123]\n"
"v_exp_f32 v141, v141\n"
"v_mfma_f32_16x16x32_bf16 a[124:127], v[220:223], v[204:207], a[124:127]\n"
"ds_read_b64_tr_b16 v[220:221], v247 offset:352\n"
"s_waitcnt lgkmcnt(5)\n"
"v_mfma_f32_16x16x32_bf16 a[128:131], v[208:211], v[192:195], a[128:131]\n"
"ds_read_b64_tr_b16 v[222:223], v247 offset:8800\n"
"s_add_u32 m0, s44, 35968\n"
"v_mfma_f32_16x16x32_bf16 a[132:135], v[208:211], v[196:199], a[132:135]\n"
"v_exp_f32 v142, v142\n"
"v_mfma_f32_16x16x32_bf16 a[136:139], v[208:211], v[200:203], a[136:139]\n"
"v_exp_f32 v143, v143\n"
"v_mfma_f32_16x16x32_bf16 a[140:143], v[208:211], v[204:207], a[140:143]\n"
"ds_read_b64_tr_b16 v[208:209], v247 offset:384\n"
"s_waitcnt lgkmcnt(5)\n"
"v_mfma_f32_16x16x32_bf16 a[144:147], v[212:215], v[192:195], a[144:147]\n"
"ds_read_b64_tr_b16 v[210:211], v247 offset:8832\n"
"buffer_load_dwordx4 v249, s[16:19], s55 offen offset:2048 nt lds\n"
"v_mfma_f32_16x16x32_bf16 a[148:151], v[212:215], v[196:199], a[148:151]\n"
"v_exp_f32 v144, v144\n"
"v_mfma_f32_16x16x32_bf16 a[152:155], v[212:215], v[200:203], a[152:155]\n"
"v_exp_f32 v145, v145\n"
"v_mfma_f32_16x16x32_bf16 a[156:159], v[212:215], v[204:207], a[156:159]\n"
"ds_read_b64_tr_b16 v[212:213], v247 offset:416\n"
"s_waitcnt lgkmcnt(5)\n"
"v_mfma_f32_16x16x32_bf16 a[160:163], v[216:219], v[192:195], a[160:163]\n"
"ds_read_b64_tr_b16 v[214:215], v247 offset:8864\n"
"s_add_u32 m0, s44, 36512\n"
"v_mfma_f32_16x16x32_bf16 a[164:167], v[216:219], v[196:199], a[164:167]\n"
"v_exp_f32 v146, v146\n"
"v_mfma_f32_16x16x32_bf16 a[168:171], v[216:219], v[200:203], a[168:171]\n"
"v_exp_f32 v147, v147\n"
"v_mfma_f32_16x16x32_bf16 a[172:175], v[216:219], v[204:207], a[172:175]\n"
"ds_read_b64_tr_b16 v[216:217], v247 offset:448\n"
"s_waitcnt lgkmcnt(5)\n"
"v_mfma_f32_16x16x32_bf16 a[176:179], v[220:223], v[192:195], a[176:179]\n"
"ds_read_b64_tr_b16 v[218:219], v247 offset:8896\n"
"buffer_load_dwordx4 v249, s[16:19], s55 offen offset:2560 nt lds\n"
"v_mfma_f32_16x16x32_bf16 a[180:183], v[220:223], v[196:199], a[180:183]\n"
"v_exp_f32 v148, v148\n"
"v_mfma_f32_16x16x32_bf16 a[184:187], v[220:223], v[200:203], a[184:187]\n"
"v_exp_f32 v149, v149\n"
"v_mfma_f32_16x16x32_bf16 a[188:191], v[220:223], v[204:207], a[188:191]\n"
"ds_read_b64_tr_b16 v[220:221], v247 offset:480\n"
"s_waitcnt lgkmcnt(5)\n"
"v_mfma_f32_16x16x32_bf16 a[192:195], v[208:211], v[192:195], a[192:195]\n"
"ds_read_b64_tr_b16 v[222:223], v247 offset:8928\n"
"s_add_u32 m0, s44, 37056\n"
"v_mfma_f32_16x16x32_bf16 a[196:199], v[208:211], v[196:199], a[196:199]\n"
"v_exp_f32 v150, v150\n"
"v_mfma_f32_16x16x32_bf16 a[200:203], v[208:211], v[200:203], a[200:203]\n"
"v_exp_f32 v151, v151\n"
"v_mfma_f32_16x16x32_bf16 a[204:207], v[208:211], v[204:207], a[204:207]\n"
"s_waitcnt lgkmcnt(4)\n"
"v_mfma_f32_16x16x32_bf16 a[208:211], v[212:215], v[192:195], a[208:211]\n"
"buffer_load_dwordx4 v249, s[16:19], s55 offen offset:3072 nt lds\n"
"v_mfma_f32_16x16x32_bf16 a[212:215], v[212:215], v[196:199], a[212:215]\n"
"v_exp_f32 v152, v152\n"
"v_mfma_f32_16x16x32_bf16 a[216:219], v[212:215], v[200:203], a[216:219]\n"
"s_waitcnt vmcnt(15) lgkmcnt(0)\n"
"s_barrier\n"
"ds_read_b128 v[208:211], v245 offset:33792\n"
"v_mfma_f32_16x16x32_bf16 a[220:223], v[212:215], v[204:207], a[220:223]\n"
"v_mfma_f32_16x16x32_bf16 a[224:227], v[216:219], v[192:195], a[224:227]\n"
"ds_read_b128 v[212:215], v245 offset:34304\n"
"s_add_u32 m0, s44, 37600\n"
"v_mfma_f32_16x16x32_bf16 a[228:231], v[216:219], v[196:199], a[228:231]\n"
"v_exp_f32 v153, v153\n"
"v_mfma_f32_16x16x32_bf16 a[232:235], v[216:219], v[200:203], a[232:235]\n"
"v_mfma_f32_16x16x32_bf16 a[236:239], v[216:219], v[204:207], a[236:239]\n"
"ds_read_b128 v[216:219], v245 offset:33856\n"
"v_mfma_f32_16x16x32_bf16 a[240:243], v[220:223], v[192:195], a[240:243]\n"
"buffer_load_dwordx4 v249, s[16:19], s55 offen offset:3584 nt lds\n"
"s_add_u32 s55, s55, 16384\n"
"v_mfma_f32_16x16x32_bf16 a[244:247], v[220:223], v[196:199], a[244:247]\n"
"v_mfma_f32_16x16x32_bf16 a[248:251], v[220:223], v[200:203], a[248:251]\n"
"v_mfma_f32_16x16x32_bf16 a[252:255], v[220:223], v[204:207], a[252:255]\n"
"ds_read_b128 v[220:223], v245 offset:34368\n"
"s_waitcnt lgkmcnt(3)\n"
"v_mfma_f32_16x16x32_bf16 v[160:163], v[208:211], v[0:3], v[224:227]\n"
"v_cvt_pk_bf16_f32 v192, v128, v129\n"
"v_add_f32 v128, v128, v129\n"
"v_mfma_f32_16x16x32_bf16 v[168:171], v[208:211], v[4:7], v[228:231]\n"
"s_add_u32 s56, s58, s59\n"
"s_add_u32 m0, s44, 67584\n"
"s_add_u32 s57, s56, 3584\n"
"v_mfma_f32_16x16x32_bf16 v[176:179], v[208:211], v[8:11], v[232:235]\n"
"v_cvt_pk_bf16_f32 v193, v130, v131\n"
"v_add_f32 v130, v130, v131\n"
"v_mfma_f32_16x16x32_bf16 v[184:187], v[208:211], v[12:15], v[236:239]\n"
"ds_read_b128 v[208:211], v245 offset:33920\n"
"s_waitcnt lgkmcnt(3)\n"
"v_mfma_f32_16x16x32_bf16 v[164:167], v[212:215], v[0:3], v[224:227]\n"
"v_cvt_pk_bf16_f32 v194, v132, v133\n"
"v_add_f32 v132, v132, v133\n"
"v_bfe_u32 v249, v253, 1, 4\n"
"v_mfma_f32_16x16x32_bf16 v[172:175], v[212:215], v[4:7], v[228:231]\n"
"buffer_load_dwordx4 v250, s[24:27], s56 offen lds\n"
"v_add_f32 v128, v128, v130\n"
"v_mul_u32_u24 v249, 1792, v249\n"
"v_mfma_f32_16x16x32_bf16 v[180:183], v[212:215], v[8:11], v[232:235]\n"
"v_cvt_pk_bf16_f32 v195, v134, v135\n"
"v_add_f32 v134, v134, v135\n"
"v_and_b32 v248, 1, v253\n"
"v_mfma_f32_16x16x32_bf16 v[188:191], v[212:215], v[12:15], v[236:239]\n"
"ds_read_b128 v[212:215], v245 offset:34432\n"
"v_lshl_add_u32 v249, v248, 5, v249\n"
"s_waitcnt lgkmcnt(2)\n"
"v_mfma_f32_16x16x32_bf16 v[160:163], v[216:219], v[16:19], v[160:163]\n"
"v_add_f32 v128, v128, v132\n"
"v_add_f32 v128, v128, v134\n"
"v_lshrrev_b32 v248, 5, v253\n"
"v_mfma_f32_16x16x32_bf16 v[168:171], v[216:219], v[20:23], v[168:171]\n"
"s_add_u32 m0, s44, 67744\n"
"v_cvt_pk_bf16_f32 v196, v136, v137\n"
"v_lshl_add_u32 v249, v248, 3, v249\n"
"v_mfma_f32_16x16x32_bf16 v[176:179], v[216:219], v[24:27], v[176:179]\n"
"v_add_f32 v136, v136, v137\n"
"v_cvt_pk_bf16_f32 v197, v138, v139\n"
"v_lshlrev_b32 v248, 3, v253\n"
"v_mfma_f32_16x16x32_bf16 v[184:187], v[216:219], v[28:31], v[184:187]\n"
"ds_read_b128 v[216:219], v245 offset:33984\n"
"s_mul_i32 s8, s43, 917504\n"
"v_mfma_f32_16x16x32_bf16 v[164:167], v[220:223], v[16:19], v[164:167]\n"
"v_add_f32 v138, v138, v139\n"
"v_cvt_pk_bf16_f32 v198, v140, v141\n"
"s_mul_i32 s65, s40, 14336\n"
"v_mfma_f32_16x16x32_bf16 v[172:175], v[220:223], v[20:23], v[172:175]\n"
"buffer_load_dwordx4 v250, s[24:27], s56 offen offset:896 lds\n"
"v_add_f32 v140, v140, v141\n"
"s_add_u32 s8, s8, s65\n"
"v_mfma_f32_16x16x32_bf16 v[180:183], v[220:223], v[24:27], v[180:183]\n"
"v_add_f32 v136, v136, v138\n"
"v_cvt_pk_bf16_f32 v199, v142, v143\n"
"s_lshl_b32 s65, s38, 9\n"
"v_mfma_f32_16x16x32_bf16 v[188:191], v[220:223], v[28:31], v[188:191]\n"
"ds_read_b128 v[220:223], v245 offset:34496\n"
"s_add_u32 s8, s8, s65\n"
"s_waitcnt lgkmcnt(2)\n"
"v_mfma_f32_16x16x32_bf16 v[160:163], v[208:211], v[32:35], v[160:163]\n"
"v_add_f32 v142, v142, v143\n"
"v_add_f32 v136, v136, v140\n"
"s_add_u32 s8, s8, 262144\n"
"v_mfma_f32_16x16x32_bf16 v[168:171], v[208:211], v[36:39], v[168:171]\n"
"s_add_u32 m0, s44, 67904\n"
"v_add_f32 v136, v136, v142\n"
"s_lshl_b32 s10, s39, 8\n"
"v_mfma_f32_16x16x32_bf16 v[176:179], v[208:211], v[40:43], v[176:179]\n"
"v_exp_f32 v154, v154\n"
"s_lshl_b32 s65, s41, 7\n"
"v_mfma_f32_16x16x32_bf16 v[184:187], v[208:211], v[44:47], v[184:187]\n"
"ds_read_b128 v[208:211], v245 offset:34048\n"
"s_add_u32 s10, s10, s65\n"
"v_mfma_f32_16x16x32_bf16 v[164:167], v[212:215], v[32:35], v[164:167]\n"
"v_cvt_pk_bf16_f32 v200, v144, v145\n"
"v_add_f32 v144, v144, v145\n"
"s_lshl_b32 s65, s40, 4\n"
"v_mfma_f32_16x16x32_bf16 v[172:175], v[212:215], v[36:39], v[172:175]\n"
"buffer_load_dwordx4 v250, s[24:27], s56 offen offset:1792 lds\n"
"s_add_u32 s10, s10, s65\n"
"s_mul_i32 s10, s10, 1792\n"
"v_mfma_f32_16x16x32_bf16 v[180:183], v[212:215], v[40:43], v[180:183]\n"
"v_exp_f32 v155, v155\n"
"s_mul_i32 s65, s42, 896\n"
"v_mfma_f32_16x16x32_bf16 v[188:191], v[212:215], v[44:47], v[188:191]\n"
"ds_read_b128 v[212:215], v245 offset:34560\n"
"s_add_u32 s10, s10, s65\n"
"s_waitcnt lgkmcnt(2)\n"
"v_mfma_f32_16x16x32_bf16 v[160:163], v[216:219], v[48:51], v[160:163]\n"
"v_cvt_pk_bf16_f32 v201, v146, v147\n"
"v_add_f32 v146, v146, v147\n"
"s_lshl_b32 s65, s36, 6\n"
"v_mfma_f32_16x16x32_bf16 v[168:171], v[216:219], v[52:55], v[168:171]\n"
"s_add_u32 m0, s44, 68064\n"
"s_add_u32 s10, s10, s65\n"
"s_lshl_b32 s65, s37, 4\n"
"v_mfma_f32_16x16x32_bf16 v[176:179], v[216:219], v[56:59], v[176:179]\n"
"v_exp_f32 v156, v156\n"
"s_add_u32 s10, s10, s65\n"
"v_mfma_f32_16x16x32_bf16 v[184:187], v[216:219], v[60:63], v[184:187]\n"
"ds_read_b128 v[216:219], v245 offset:34112\n"
"s_mov_b32 s9, s60\n"
"v_mfma_f32_16x16x32_bf16 v[164:167], v[220:223], v[48:51], v[164:167]\n"
"v_cvt_pk_bf16_f32 v202, v148, v149\n"
"v_add_f32 v148, v148, v149\n"
"s_lshr_b32 s11, s9, 4\n"
"v_mfma_f32_16x16x32_bf16 v[172:175], v[220:223], v[52:55], v[172:175]\n"
"buffer_load_dwordx4 v250, s[24:27], s56 offen offset:2688 lds\n"
"s_mov_b32 s21, 0xffff0000\n"
"s_mov_b32 s69, -2\n"
"v_mfma_f32_16x16x32_bf16 v[180:183], v[220:223], v[56:59], v[180:183]\n"
"v_exp_f32 v157, v157\n"
"v_mfma_f32_16x16x32_bf16 v[188:191], v[220:223], v[60:63], v[188:191]\n"
"ds_read_b128 v[220:223], v245 offset:34624\n"
"s_waitcnt lgkmcnt(2)\n"
"v_mfma_f32_16x16x32_bf16 v[160:163], v[208:211], v[64:67], v[160:163]\n"
"v_add_f32 v144, v144, v146\n"
"v_cvt_pk_bf16_f32 v203, v150, v151\n"
"v_mfma_f32_16x16x32_bf16 v[168:171], v[208:211], v[68:71], v[168:171]\n"
"s_add_u32 m0, s44, 71808\n"
"v_mfma_f32_16x16x32_bf16 v[176:179], v[208:211], v[72:75], v[176:179]\n"
"v_exp_f32 v158, v158\n"
"v_mfma_f32_16x16x32_bf16 v[184:187], v[208:211], v[76:79], v[184:187]\n"
"ds_read_b128 v[208:211], v245 offset:34176\n"
"v_mfma_f32_16x16x32_bf16 v[164:167], v[212:215], v[64:67], v[164:167]\n"
"v_add_f32 v150, v150, v151\n"
"v_add_f32 v144, v144, v148\n"
"v_mfma_f32_16x16x32_bf16 v[172:175], v[212:215], v[68:71], v[172:175]\n"
"buffer_load_dwordx4 v250, s[24:27], s57 offen lds\n"
"v_mfma_f32_16x16x32_bf16 v[180:183], v[212:215], v[72:75], v[180:183]\n"
"v_exp_f32 v159, v159\n"
"v_mfma_f32_16x16x32_bf16 v[188:191], v[212:215], v[76:79], v[188:191]\n"
"ds_read_b128 v[212:215], v245 offset:34688\n"
"s_waitcnt lgkmcnt(2)\n"
"v_mfma_f32_16x16x32_bf16 v[160:163], v[216:219], v[80:83], v[160:163]\n"
"v_add_f32 v144, v144, v150\n"
"v_cvt_pk_bf16_f32 v204, v152, v153\n"
"v_mfma_f32_16x16x32_bf16 v[168:171], v[216:219], v[84:87], v[168:171]\n"
"s_add_u32 m0, s44, 71968\n"
"v_add_f32 v152, v152, v153\n"
"v_mfma_f32_16x16x32_bf16 v[176:179], v[216:219], v[88:91], v[176:179]\n"
"v_cvt_pk_bf16_f32 v205, v154, v155\n"
"v_add_f32 v154, v154, v155\n"
"v_mfma_f32_16x16x32_bf16 v[184:187], v[216:219], v[92:95], v[184:187]\n"
"ds_read_b128 v[216:219], v245 offset:34240\n"
"v_mfma_f32_16x16x32_bf16 v[164:167], v[220:223], v[80:83], v[164:167]\n"
"v_cvt_pk_bf16_f32 v206, v156, v157\n"
"v_add_f32 v156, v156, v157\n"
"v_mfma_f32_16x16x32_bf16 v[172:175], v[220:223], v[84:87], v[172:175]\n"
"buffer_load_dwordx4 v250, s[24:27], s57 offen offset:896 lds\n"
"v_add_f32 v152, v152, v154\n"
"v_mfma_f32_16x16x32_bf16 v[180:183], v[220:223], v[88:91], v[180:183]\n"
"v_cvt_pk_bf16_f32 v207, v158, v159\n"
"v_add_f32 v158, v158, v159\n"
"v_mfma_f32_16x16x32_bf16 v[188:191], v[220:223], v[92:95], v[188:191]\n"
"ds_read_b128 v[220:223], v245 offset:34752\n"
"s_waitcnt lgkmcnt(3)\n"
"v_mfma_f32_16x16x32_bf16 v[160:163], v[208:211], v[96:99], v[160:163]\n"
"v_add_f32 v152, v152, v156\n"
"v_add_f32 v152, v152, v158\n"
"v_mfma_f32_16x16x32_bf16 v[168:171], v[208:211], v[100:103], v[168:171]\n"
"s_add_u32 m0, s44, 72128\n"
"v_max3_f32 v129, v128, v136, v144\n"
"v_mfma_f32_16x16x32_bf16 v[176:179], v[208:211], v[104:107], v[176:179]\n"
"v_max_f32 v129, v129, v152\n"
"v_cmp_nge_f32 vcc, 0x43800000, v129\n"
"v_mfma_f32_16x16x32_bf16 v[184:187], v[208:211], v[108:111], v[184:187]\n"
"s_waitcnt lgkmcnt(2)\n"
"v_mfma_f32_16x16x32_bf16 v[164:167], v[212:215], v[96:99], v[164:167]\n"
"v_mfma_f32_16x16x32_bf16 v[172:175], v[212:215], v[100:103], v[172:175]\n"
"buffer_load_dwordx4 v250, s[24:27], s57 offen offset:1792 lds\n"
"v_mfma_f32_16x16x32_bf16 v[180:183], v[212:215], v[104:107], v[180:183]\n"
"s_waitcnt vmcnt(15) lgkmcnt(0)\n"
"s_barrier\n"
"ds_read_b64_tr_b16 v[208:209], v246 offset:0\n"
"v_mfma_f32_16x16x32_bf16 v[188:191], v[212:215], v[108:111], v[188:191]\n"
"ds_read_b64_tr_b16 v[210:211], v246 offset:8448\n"
"v_mfma_f32_16x16x32_bf16 v[160:163], v[216:219], v[112:115], v[160:163]\n"
"ds_read_b64_tr_b16 v[212:213], v246 offset:32\n"
"v_mfma_f32_16x16x32_bf16 v[168:171], v[216:219], v[116:119], v[168:171]\n"
"ds_read_b64_tr_b16 v[214:215], v246 offset:8480\n"
"s_add_u32 m0, s44, 72288\n"
"v_mfma_f32_16x16x32_bf16 v[176:179], v[216:219], v[120:123], v[176:179]\n"
"v_mfma_f32_16x16x32_bf16 v[184:187], v[216:219], v[124:127], v[184:187]\n"
"ds_read_b64_tr_b16 v[216:217], v246 offset:64\n"
"v_mfma_f32_16x16x32_bf16 v[164:167], v[220:223], v[112:115], v[164:167]\n"
"ds_read_b64_tr_b16 v[218:219], v246 offset:8512\n"
"v_mfma_f32_16x16x32_bf16 v[172:175], v[220:223], v[116:119], v[172:175]\n"
"buffer_load_dwordx4 v250, s[24:27], s57 offen offset:2688 lds\n"
"v_mfma_f32_16x16x32_bf16 v[180:183], v[220:223], v[120:123], v[180:183]\n"
"v_mfma_f32_16x16x32_bf16 v[188:191], v[220:223], v[124:127], v[188:191]\n"
"ds_read_b64_tr_b16 v[220:221], v246 offset:96\n"
"ds_read_b64_tr_b16 v[222:223], v246 offset:8544\n"
"s_cbranch_vccz LR_15_%=\n"
"s_getpc_b64 s[72:73]\n"
"s_branch LP10_%=\n"
"LR_15_%=:\n"
"s_waitcnt lgkmcnt(6)\n"
"v_mfma_f32_16x16x32_bf16 a[0:3], v[208:211], v[192:195], a[0:3]\n"
"s_add_u32 s56, s58, s59\n"
"s_add_u32 s56, s56, 114688\n"
"s_add_u32 m0, s44, 101376\n"
"s_add_u32 s57, s56, 3584\n"
"v_mfma_f32_16x16x32_bf16 a[4:7], v[208:211], v[196:199], a[4:7]\n"
"v_mfma_f32_16x16x32_bf16 a[8:11], v[208:211], v[200:203], a[8:11]\n"
"buffer_load_dwordx4 v250, s[24:27], s56 offen lds\n"
"s_mov_b32 s68, 16384\n"
"v_add_f32 v240, v240, v128\n"
"v_add_f32 v241, v241, v136\n"
"v_mfma_f32_16x16x32_bf16 a[12:15], v[208:211], v[204:207], a[12:15]\n"
"ds_read_b64_tr_b16 v[208:209], v246 offset:128\n"
"v_add_f32 v242, v242, v144\n"
"v_add_f32 v243, v243, v152\n"
"s_waitcnt lgkmcnt(5)\n"
"v_mfma_f32_16x16x32_bf16 a[16:19], v[212:215], v[192:195], a[16:19]\n"
"v_exp_f32 v160, v160\n"
"v_exp_f32 v161, v161\n"
"v_mfma_f32_16x16x32_bf16 a[20:23], v[212:215], v[196:199], a[20:23]\n"
"ds_read_b64_tr_b16 v[210:211], v246 offset:8576\n"
"v_exp_f32 v162, v162\n"
"v_mfma_f32_16x16x32_bf16 a[24:27], v[212:215], v[200:203], a[24:27]\n"
"s_add_u32 m0, s44, 101536\n"
"v_exp_f32 v163, v163\n"
"v_mfma_f32_16x16x32_bf16 a[28:31], v[212:215], v[204:207], a[28:31]\n"
"ds_read_b64_tr_b16 v[212:213], v246 offset:160\n"
"v_exp_f32 v164, v164\n"
"s_waitcnt lgkmcnt(5)\n"
"v_mfma_f32_16x16x32_bf16 a[32:35], v[216:219], v[192:195], a[32:35]\n"
"buffer_load_dwordx4 v250, s[24:27], s56 offen offset:896 lds\n"
"v_exp_f32 v165, v165\n"
"v_mfma_f32_16x16x32_bf16 a[36:39], v[216:219], v[196:199], a[36:39]\n"
"ds_read_b64_tr_b16 v[214:215], v246 offset:8608\n"
"v_exp_f32 v166, v166\n"
"v_mfma_f32_16x16x32_bf16 a[40:43], v[216:219], v[200:203], a[40:43]\n"
"v_exp_f32 v167, v167\n"
"v_exp_f32 v168, v168\n"
"v_mfma_f32_16x16x32_bf16 a[44:47], v[216:219], v[204:207], a[44:47]\n"
"ds_read_b64_tr_b16 v[216:217], v246 offset:192\n"
"v_exp_f32 v169, v169\n"
"s_waitcnt lgkmcnt(5)\n"
"v_mfma_f32_16x16x32_bf16 a[48:51], v[220:223], v[192:195], a[48:51]\n"
"s_add_u32 m0, s44, 101696\n"
"v_exp_f32 v170, v170\n"
"v_mfma_f32_16x16x32_bf16 a[52:55], v[220:223], v[196:199], a[52:55]\n"
"ds_read_b64_tr_b16 v[218:219], v246 offset:8640\n"
"v_exp_f32 v171, v171\n"
"v_mfma_f32_16x16x32_bf16 a[56:59], v[220:223], v[200:203], a[56:59]\n"
"buffer_load_dwordx4 v250, s[24:27], s56 offen offset:1792 lds\n"
"v_exp_f32 v172, v172\n"
"v_mfma_f32_16x16x32_bf16 a[60:63], v[220:223], v[204:207], a[60:63]\n"
"ds_read_b64_tr_b16 v[220:221], v246 offset:224\n"
"v_exp_f32 v173, v173\n"
"s_waitcnt lgkmcnt(5)\n"
"v_mfma_f32_16x16x32_bf16 a[64:67], v[208:211], v[192:195], a[64:67]\n"
"v_exp_f32 v174, v174\n"
"v_exp_f32 v175, v175\n"
"v_mfma_f32_16x16x32_bf16 a[68:71], v[208:211], v[196:199], a[68:71]\n"
"ds_read_b64_tr_b16 v[222:223], v246 offset:8672\n"
"v_exp_f32 v176, v176\n"
"v_mfma_f32_16x16x32_bf16 a[72:75], v[208:211], v[200:203], a[72:75]\n"
"s_add_u32 m0, s44, 101856\n"
"v_exp_f32 v177, v177\n"
"v_mfma_f32_16x16x32_bf16 a[76:79], v[208:211], v[204:207], a[76:79]\n"
"ds_read_b64_tr_b16 v[208:209], v246 offset:256\n"
"v_exp_f32 v178, v178\n"
"s_waitcnt lgkmcnt(5)\n"
"v_mfma_f32_16x16x32_bf16 a[80:83], v[212:215], v[192:195], a[80:83]\n"
"buffer_load_dwordx4 v250, s[24:27], s56 offen offset:2688 lds\n"
"v_exp_f32 v179, v179\n"
"v_mfma_f32_16x16x32_bf16 a[84:87], v[212:215], v[196:199], a[84:87]\n"
"ds_read_b64_tr_b16 v[210:211], v246 offset:8704\n"
"v_exp_f32 v180, v180\n"
"v_mfma_f32_16x16x32_bf16 a[88:91], v[212:215], v[200:203], a[88:91]\n"
"v_exp_f32 v181, v181\n"
"v_exp_f32 v182, v182\n"
"v_mfma_f32_16x16x32_bf16 a[92:95], v[212:215], v[204:207], a[92:95]\n"
"ds_read_b64_tr_b16 v[212:213], v246 offset:288\n"
"v_exp_f32 v183, v183\n"
"s_waitcnt lgkmcnt(5)\n"
"v_mfma_f32_16x16x32_bf16 a[96:99], v[216:219], v[192:195], a[96:99]\n"
"s_add_u32 m0, s44, 105600\n"
"v_exp_f32 v184, v184\n"
"v_mfma_f32_16x16x32_bf16 a[100:103], v[216:219], v[196:199], a[100:103]\n"
"ds_read_b64_tr_b16 v[214:215], v246 offset:8736\n"
"v_exp_f32 v185, v185\n"
"v_cvt_pk_bf16_f32 v128, v160, v161\n"
"v_mfma_f32_16x16x32_bf16 a[104:107], v[216:219], v[200:203], a[104:107]\n"
"buffer_load_dwordx4 v250, s[24:27], s57 offen lds\n"
"v_add_f32 v160, v160, v161\n"
"v_cvt_pk_bf16_f32 v129, v162, v163\n"
"v_add_f32 v162, v162, v163\n"
"v_mfma_f32_16x16x32_bf16 a[108:111], v[216:219], v[204:207], a[108:111]\n"
"ds_read_b64_tr_b16 v[216:217], v246 offset:320\n"
"v_cvt_pk_bf16_f32 v130, v164, v165\n"
"v_add_f32 v164, v164, v165\n"
"s_waitcnt lgkmcnt(5)\n"
"v_mfma_f32_16x16x32_bf16 a[112:115], v[220:223], v[192:195], a[112:115]\n"
"v_add_f32 v160, v160, v162\n"
"v_cvt_pk_bf16_f32 v131, v166, v167\n"
"v_add_f32 v166, v166, v167\n"
"v_add_f32 v160, v160, v164\n"
"v_mfma_f32_16x16x32_bf16 a[116:119], v[220:223], v[196:199], a[116:119]\n"
"ds_read_b64_tr_b16 v[218:219], v246 offset:8768\n"
"v_add_f32 v160, v160, v166\n"
"v_cvt_pk_bf16_f32 v132, v168, v169\n"
"v_add_f32 v168, v168, v169\n"
"v_mfma_f32_16x16x32_bf16 a[120:123], v[220:223], v[200:203], a[120:123]\n"
"s_add_u32 m0, s44, 105760\n"
"v_cvt_pk_bf16_f32 v133, v170, v171\n"
"v_add_f32 v170, v170, v171\n"
"v_cvt_pk_bf16_f32 v134, v172, v173\n"
"v_mfma_f32_16x16x32_bf16 a[124:127], v[220:223], v[204:207], a[124:127]\n"
"ds_read_b64_tr_b16 v[220:221], v246 offset:352\n"
"v_add_f32 v172, v172, v173\n"
"v_add_f32 v168, v168, v170\n"
"s_waitcnt lgkmcnt(5)\n"
"v_mfma_f32_16x16x32_bf16 a[128:131], v[208:211], v[192:195], a[128:131]\n"
"buffer_load_dwordx4 v250, s[24:27], s57 offen offset:896 lds\n"
"v_cvt_pk_bf16_f32 v135, v174, v175\n"
"v_add_f32 v174, v174, v175\n"
"v_add_f32 v168, v168, v172\n"
"v_mfma_f32_16x16x32_bf16 a[132:135], v[208:211], v[196:199], a[132:135]\n"
"ds_read_b64_tr_b16 v[222:223], v246 offset:8800\n"
"v_add_f32 v168, v168, v174\n"
"v_exp_f32 v186, v186\n"
"v_mfma_f32_16x16x32_bf16 a[136:139], v[208:211], v[200:203], a[136:139]\n"
"v_cvt_pk_bf16_f32 v136, v176, v177\n"
"v_add_f32 v176, v176, v177\n"
"v_exp_f32 v187, v187\n"
"v_mfma_f32_16x16x32_bf16 a[140:143], v[208:211], v[204:207], a[140:143]\n"
"ds_read_b64_tr_b16 v[208:209], v246 offset:384\n"
"v_cvt_pk_bf16_f32 v137, v178, v179\n"
"v_add_f32 v178, v178, v179\n"
"s_waitcnt lgkmcnt(5)\n"
"v_mfma_f32_16x16x32_bf16 a[144:147], v[212:215], v[192:195], a[144:147]\n"
"s_add_u32 m0, s44, 105920\n"
"v_exp_f32 v188, v188\n"
"v_cvt_pk_bf16_f32 v138, v180, v181\n"
"v_mfma_f32_16x16x32_bf16 a[148:151], v[212:215], v[196:199], a[148:151]\n"
"ds_read_b64_tr_b16 v[210:211], v246 offset:8832\n"
"v_add_f32 v180, v180, v181\n"
"v_exp_f32 v189, v189\n"
"v_mfma_f32_16x16x32_bf16 a[152:155], v[212:215], v[200:203], a[152:155]\n"
"buffer_load_dwordx4 v250, s[24:27], s57 offen offset:1792 lds\n"
"v_add_f32 v176, v176, v178\n"
"v_cvt_pk_bf16_f32 v139, v182, v183\n"
"v_mfma_f32_16x16x32_bf16 a[156:159], v[212:215], v[204:207], a[156:159]\n"
"ds_read_b64_tr_b16 v[212:213], v246 offset:416\n"
"v_exp_f32 v190, v190\n"
"s_waitcnt lgkmcnt(5)\n"
"v_mfma_f32_16x16x32_bf16 a[160:163], v[216:219], v[192:195], a[160:163]\n"
"v_add_f32 v182, v182, v183\n"
"v_add_f32 v176, v176, v180\n"
"v_exp_f32 v191, v191\n"
"v_mfma_f32_16x16x32_bf16 a[164:167], v[216:219], v[196:199], a[164:167]\n"
"ds_read_b64_tr_b16 v[214:215], v246 offset:8864\n"
"v_add_f32 v176, v176, v182\n"
"v_cvt_pk_bf16_f32 v140, v184, v185\n"
"v_add_f32 v184, v184, v185\n"
"v_mfma_f32_16x16x32_bf16 a[168:171], v[216:219], v[200:203], a[168:171]\n"
"s_add_u32 m0, s44, 106080\n"
"v_cvt_pk_bf16_f32 v141, v186, v187\n"
"v_add_f32 v186, v186, v187\n"
"v_cvt_pk_bf16_f32 v142, v188, v189\n"
"v_mfma_f32_16x16x32_bf16 a[172:175], v[216:219], v[204:207], a[172:175]\n"
"ds_read_b64_tr_b16 v[216:217], v246 offset:448\n"
"v_add_f32 v188, v188, v189\n"
"v_add_f32 v184, v184, v186\n"
"s_waitcnt lgkmcnt(5)\n"
"v_mfma_f32_16x16x32_bf16 a[176:179], v[220:223], v[192:195], a[176:179]\n"
"buffer_load_dwordx4 v250, s[24:27], s57 offen offset:2688 lds\n"
"v_cvt_pk_bf16_f32 v143, v190, v191\n"
"v_add_f32 v190, v190, v191\n"
"v_add_f32 v184, v184, v188\n"
"v_mfma_f32_16x16x32_bf16 a[180:183], v[220:223], v[196:199], a[180:183]\n"
"ds_read_b64_tr_b16 v[218:219], v246 offset:8896\n"
"s_add_u32 s59, s59, 128\n"
"s_cmp_eq_u32 s59, 896\n"
"s_cselect_b32 s59, 0, s59\n"
"v_mfma_f32_16x16x32_bf16 a[184:187], v[220:223], v[200:203], a[184:187]\n"
"v_add_f32 v184, v184, v190\n"
"v_max3_f32 v161, v160, v168, v176\n"
"v_max_f32 v161, v161, v184\n"
"v_cmp_nge_f32 vcc, 0x43800000, v161\n"
"v_mfma_f32_16x16x32_bf16 a[188:191], v[220:223], v[204:207], a[188:191]\n"
"ds_read_b64_tr_b16 v[220:221], v246 offset:480\n"
"s_waitcnt lgkmcnt(5)\n"
"v_mfma_f32_16x16x32_bf16 a[192:195], v[208:211], v[192:195], a[192:195]\n"
"v_mfma_f32_16x16x32_bf16 a[196:199], v[208:211], v[196:199], a[196:199]\n"
"ds_read_b64_tr_b16 v[222:223], v246 offset:8928\n"
"v_mfma_f32_16x16x32_bf16 a[200:203], v[208:211], v[200:203], a[200:203]\n"
"v_mfma_f32_16x16x32_bf16 a[204:207], v[208:211], v[204:207], a[204:207]\n"
"s_waitcnt lgkmcnt(4)\n"
"v_mfma_f32_16x16x32_bf16 a[208:211], v[212:215], v[192:195], a[208:211]\n"
"v_mfma_f32_16x16x32_bf16 a[212:215], v[212:215], v[196:199], a[212:215]\n"
"v_mfma_f32_16x16x32_bf16 a[216:219], v[212:215], v[200:203], a[216:219]\n"
"s_waitcnt vmcnt(16) lgkmcnt(0)\n"
"s_barrier\n"
"ds_read_b64_tr_b16 v[208:209], v246 offset:33792\n"
"v_mfma_f32_16x16x32_bf16 a[220:223], v[212:215], v[204:207], a[220:223]\n"
"ds_read_b64_tr_b16 v[210:211], v246 offset:42240\n"
"v_mfma_f32_16x16x32_bf16 a[224:227], v[216:219], v[192:195], a[224:227]\n"
"ds_read_b64_tr_b16 v[212:213], v246 offset:33824\n"
"v_mfma_f32_16x16x32_bf16 a[228:231], v[216:219], v[196:199], a[228:231]\n"
"ds_read_b64_tr_b16 v[214:215], v246 offset:42272\n"
"v_mfma_f32_16x16x32_bf16 a[232:235], v[216:219], v[200:203], a[232:235]\n"
"v_mfma_f32_16x16x32_bf16 a[236:239], v[216:219], v[204:207], a[236:239]\n"
"ds_read_b64_tr_b16 v[216:217], v246 offset:33856\n"
"v_mfma_f32_16x16x32_bf16 a[240:243], v[220:223], v[192:195], a[240:243]\n"
"ds_read_b64_tr_b16 v[218:219], v246 offset:42304\n"
"v_mfma_f32_16x16x32_bf16 a[244:247], v[220:223], v[196:199], a[244:247]\n"
"v_mfma_f32_16x16x32_bf16 a[248:251], v[220:223], v[200:203], a[248:251]\n"
"v_mfma_f32_16x16x32_bf16 a[252:255], v[220:223], v[204:207], a[252:255]\n"
"ds_read_b64_tr_b16 v[220:221], v246 offset:33888\n"
"ds_read_b64_tr_b16 v[222:223], v246 offset:42336\n"
"s_cbranch_vccz LR_16_%=\n"
"s_getpc_b64 s[72:73]\n"
"s_branch LP12_%=\n"
"LR_16_%=:\n"
"s_waitcnt lgkmcnt(6)\n"
"v_mfma_f32_16x16x32_bf16 a[0:3], v[208:211], v[128:131], a[0:3]\n"
"s_add_u32 s56, s58, s59\n"
"s_add_u32 m0, s44, 0\n"
"s_add_u32 s57, s56, 3584\n"
"v_mfma_f32_16x16x32_bf16 a[4:7], v[208:211], v[132:135], a[4:7]\n"
"v_add_f32 v240, v240, v160\n"
"v_add_f32 v241, v241, v168\n"
"v_add_f32 v242, v242, v176\n"
"v_mfma_f32_16x16x32_bf16 a[8:11], v[208:211], v[136:139], a[8:11]\n"
"buffer_load_dwordx4 v250, s[24:27], s56 offen lds\n"
"v_add_f32 v243, v243, v184\n"
"v_mov_b32 v232, v240\n"
"v_mfma_f32_16x16x32_bf16 a[12:15], v[208:211], v[140:143], a[12:15]\n"
"ds_read_b64_tr_b16 v[208:209], v246 offset:33920\n"
"v_mov_b32 v233, v241\n"
"s_waitcnt lgkmcnt(5)\n"
"v_mfma_f32_16x16x32_bf16 a[16:19], v[212:215], v[128:131], a[16:19]\n"
"v_mov_b32 v234, v242\n"
"v_mov_b32 v235, v243\n"
"v_permlane32_swap_b32 v240, v232\n"
"v_mfma_f32_16x16x32_bf16 a[20:23], v[212:215], v[132:135], a[20:23]\n"
"ds_read_b64_tr_b16 v[210:211], v246 offset:42368\n"
"v_permlane32_swap_b32 v241, v233\n"
"v_permlane32_swap_b32 v242, v234\n"
"v_mfma_f32_16x16x32_bf16 a[24:27], v[212:215], v[136:139], a[24:27]\n"
"s_add_u32 m0, s44, 160\n"
"v_permlane32_swap_b32 v243, v235\n"
"v_add_f32 v240, v240, v232\n"
"v_mfma_f32_16x16x32_bf16 a[28:31], v[212:215], v[140:143], a[28:31]\n"
"ds_read_b64_tr_b16 v[212:213], v246 offset:33952\n"
"v_add_f32 v241, v241, v233\n"
"s_waitcnt lgkmcnt(3)\n"
"v_mfma_f32_16x16x32_bf16 a[32:35], v[216:219], v[128:131], a[32:35]\n"
"buffer_load_dwordx4 v250, s[24:27], s56 offen offset:896 lds\n"
"v_add_f32 v242, v242, v234\n"
"v_add_f32 v243, v243, v235\n"
"v_mfma_f32_16x16x32_bf16 a[36:39], v[216:219], v[132:135], a[36:39]\n"
"ds_read_b64_tr_b16 v[214:215], v246 offset:42400\n"
"v_mov_b32 v232, v240\n"
"v_mov_b32 v233, v241\n"
"v_mfma_f32_16x16x32_bf16 a[40:43], v[216:219], v[136:139], a[40:43]\n"
"v_mov_b32 v234, v242\n"
"v_mov_b32 v235, v243\n"
"v_permlane16_swap_b32 v240, v232\n"
"v_accvgpr_read_b32 v192, a0\n"
"v_accvgpr_read_b32 v193, a1\n"
"v_mfma_f32_16x16x32_bf16 a[44:47], v[216:219], v[140:143], a[44:47]\n"
"ds_read_b64_tr_b16 v[216:217], v246 offset:33984\n"
"v_permlane16_swap_b32 v241, v233\n"
"v_accvgpr_read_b32 v194, a2\n"
"v_accvgpr_read_b32 v195, a3\n"
"v_mfma_f32_16x16x32_bf16 a[48:51], v[220:223], v[128:131], a[48:51]\n"
"s_add_u32 m0, s44, 320\n"
"v_permlane16_swap_b32 v242, v234\n"
"v_permlane16_swap_b32 v243, v235\n"
"v_accvgpr_read_b32 v196, a16\n"
"v_accvgpr_read_b32 v197, a17\n"
"v_mfma_f32_16x16x32_bf16 a[52:55], v[220:223], v[132:135], a[52:55]\n"
"ds_read_b64_tr_b16 v[218:219], v246 offset:42432\n"
"v_add_f32 v240, v240, v232\n"
"v_add_f32 v241, v241, v233\n"
"v_accvgpr_read_b32 v198, a18\n"
"v_accvgpr_read_b32 v199, a19\n"
"v_mfma_f32_16x16x32_bf16 a[56:59], v[220:223], v[136:139], a[56:59]\n"
"buffer_load_dwordx4 v250, s[24:27], s56 offen offset:1792 lds\n"
"v_add_f32 v242, v242, v234\n"
"v_add_f32 v243, v243, v235\n"
"v_cvt_pk_bf16_f32 v0, v192, v193\n"
"v_cvt_pk_bf16_f32 v1, v194, v195\n"
"v_mfma_f32_16x16x32_bf16 a[60:63], v[220:223], v[140:143], a[60:63]\n"
"ds_read_b64_tr_b16 v[220:221], v246 offset:34016\n"
"ds_write_b64 v254, v[242:243] offset:16384\n"
"v_cvt_pk_bf16_f32 v2, v196, v197\n"
"v_cvt_pk_bf16_f32 v3, v198, v199\n"
"s_waitcnt lgkmcnt(4)\n"
"v_mfma_f32_16x16x32_bf16 a[64:67], v[208:211], v[128:131], a[64:67]\n"
"s_waitcnt lgkmcnt(0)\n"
"v_rcp_f32 v242, v240\n"
"v_accvgpr_read_b32 v200, a4\n"
"v_accvgpr_read_b32 v201, a5\n"
"v_accvgpr_read_b32 v202, a6\n"
"v_mfma_f32_16x16x32_bf16 a[68:71], v[208:211], v[132:135], a[68:71]\n"
"ds_read_b64_tr_b16 v[222:223], v246 offset:42464\n"
"v_rcp_f32 v243, v241\n"
"v_accvgpr_read_b32 v203, a7\n"
"v_accvgpr_read_b32 v204, a20\n"
"v_mfma_f32_16x16x32_bf16 a[72:75], v[208:211], v[136:139], a[72:75]\n"
"s_add_u32 m0, s44, 480\n"
"v_and_b32 v232, 15, v253\n"
"s_lshl_b32 s96, s43, 3\n"
"v_accvgpr_read_b32 v205, a21\n"
"v_accvgpr_read_b32 v206, a22\n"
"v_mfma_f32_16x16x32_bf16 a[76:79], v[208:211], v[140:143], a[76:79]\n"
"ds_read_b64_tr_b16 v[208:209], v246 offset:34048\n"
"s_add_u32 s96, s96, s40\n"
"v_accvgpr_read_b32 v207, a23\n"
"v_cvt_pk_bf16_f32 v4, v200, v201\n"
"v_mfma_f32_16x16x32_bf16 a[80:83], v[212:215], v[128:131], a[80:83]\n"
"buffer_load_dwordx4 v250, s[24:27], s56 offen offset:2688 lds\n"
"s_lshl_b32 s96, s96, 2\n"
"s_lshl_b32 s97, s37, 1\n"
"v_cvt_pk_bf16_f32 v5, v202, v203\n"
"v_cvt_pk_bf16_f32 v6, v204, v205\n"
"v_mfma_f32_16x16x32_bf16 a[84:87], v[212:215], v[132:135], a[84:87]\n"
"ds_read_b64_tr_b16 v[210:211], v246 offset:42496\n"
"s_add_u32 s97, s97, s36\n"
"s_add_u32 s96, s96, s97\n"
"v_cvt_pk_bf16_f32 v7, v206, v207\n"
"v_accvgpr_read_b32 v192, a8\n"
"v_mfma_f32_16x16x32_bf16 a[88:91], v[212:215], v[136:139], a[88:91]\n"
"s_lshl_b32 s96, s96, 7\n"
"s_add_u32 s96, s96, 65536\n"
"v_lshlrev_b32 v232, 2, v232\n"
"v_accvgpr_read_b32 v193, a9\n"
"v_accvgpr_read_b32 v194, a10\n"
"v_mfma_f32_16x16x32_bf16 a[92:95], v[212:215], v[140:143], a[92:95]\n"
"ds_read_b64_tr_b16 v[212:213], v246 offset:34080\n"
"buffer_store_dword v242, v232, s[28:31], s96 offen offset:0 sc1\n"
"v_accvgpr_read_b32 v195, a11\n"
"v_accvgpr_read_b32 v196, a24\n"
"s_waitcnt lgkmcnt(3)\n"
"v_mfma_f32_16x16x32_bf16 a[96:99], v[216:219], v[128:131], a[96:99]\n"
"s_add_u32 m0, s44, 4224\n"
"buffer_store_dword v243, v232, s[28:31], s96 offen offset:64 sc1\n"
"v_accvgpr_read_b32 v197, a25\n"
"v_accvgpr_read_b32 v198, a26\n"
"v_accvgpr_read_b32 v199, a27\n"
"v_mfma_f32_16x16x32_bf16 a[100:103], v[216:219], v[132:135], a[100:103]\n"
"ds_read_b64_tr_b16 v[214:215], v246 offset:42528\n"
"v_cvt_pk_bf16_f32 v8, v192, v193\n"
"v_cvt_pk_bf16_f32 v9, v194, v195\n"
"v_cvt_pk_bf16_f32 v10, v196, v197\n"
"v_cvt_pk_bf16_f32 v11, v198, v199\n"
"v_mfma_f32_16x16x32_bf16 a[104:107], v[216:219], v[136:139], a[104:107]\n"
"buffer_load_dwordx4 v250, s[24:27], s57 offen lds\n"
"v_accvgpr_read_b32 v200, a12\n"
"v_accvgpr_read_b32 v201, a13\n"
"v_accvgpr_read_b32 v202, a14\n"
"v_accvgpr_read_b32 v203, a15\n"
"v_mfma_f32_16x16x32_bf16 a[108:111], v[216:219], v[140:143], a[108:111]\n"
"ds_read_b64_tr_b16 v[216:217], v246 offset:34112\n"
"v_accvgpr_read_b32 v204, a28\n"
"v_accvgpr_read_b32 v205, a29\n"
"v_accvgpr_read_b32 v206, a30\n"
"v_mfma_f32_16x16x32_bf16 a[112:115], v[220:223], v[128:131], a[112:115]\n"
"v_accvgpr_read_b32 v207, a31\n"
"v_cvt_pk_bf16_f32 v12, v200, v201\n"
"v_cvt_pk_bf16_f32 v13, v202, v203\n"
"v_cvt_pk_bf16_f32 v14, v204, v205\n"
"v_cvt_pk_bf16_f32 v15, v206, v207\n"
"v_mfma_f32_16x16x32_bf16 a[116:119], v[220:223], v[132:135], a[116:119]\n"
"ds_read_b64_tr_b16 v[218:219], v246 offset:42560\n"
"v_accvgpr_read_b32 v192, a32\n"
"v_accvgpr_read_b32 v193, a33\n"
"v_accvgpr_read_b32 v194, a34\n"
"v_accvgpr_read_b32 v195, a35\n"
"v_mfma_f32_16x16x32_bf16 a[120:123], v[220:223], v[136:139], a[120:123]\n"
"s_add_u32 m0, s44, 4384\n"
"v_accvgpr_read_b32 v196, a48\n"
"v_accvgpr_read_b32 v197, a49\n"
"v_accvgpr_read_b32 v198, a50\n"
"v_accvgpr_read_b32 v199, a51\n"
"v_mfma_f32_16x16x32_bf16 a[124:127], v[220:223], v[140:143], a[124:127]\n"
"ds_read_b64_tr_b16 v[220:221], v246 offset:34144\n"
"v_cvt_pk_bf16_f32 v16, v192, v193\n"
"v_cvt_pk_bf16_f32 v17, v194, v195\n"
"v_cvt_pk_bf16_f32 v18, v196, v197\n"
"s_waitcnt lgkmcnt(3)\n"
"v_mfma_f32_16x16x32_bf16 a[128:131], v[208:211], v[128:131], a[128:131]\n"
"buffer_load_dwordx4 v250, s[24:27], s57 offen offset:896 lds\n"
"v_cvt_pk_bf16_f32 v19, v198, v199\n"
"v_accvgpr_read_b32 v200, a36\n"
"v_accvgpr_read_b32 v201, a37\n"
"v_accvgpr_read_b32 v202, a38\n"
"v_mfma_f32_16x16x32_bf16 a[132:135], v[208:211], v[132:135], a[132:135]\n"
"ds_read_b64_tr_b16 v[222:223], v246 offset:42592\n"
"v_accvgpr_read_b32 v203, a39\n"
"v_accvgpr_read_b32 v204, a52\n"
"v_accvgpr_read_b32 v205, a53\n"
"v_accvgpr_read_b32 v206, a54\n"
"v_mfma_f32_16x16x32_bf16 a[136:139], v[208:211], v[136:139], a[136:139]\n"
"v_accvgpr_read_b32 v207, a55\n"
"v_cvt_pk_bf16_f32 v20, v200, v201\n"
"v_cvt_pk_bf16_f32 v21, v202, v203\n"
"v_cvt_pk_bf16_f32 v22, v204, v205\n"
"v_cvt_pk_bf16_f32 v23, v206, v207\n"
"v_mfma_f32_16x16x32_bf16 a[140:143], v[208:211], v[140:143], a[140:143]\n"
"ds_read_b64_tr_b16 v[208:209], v246 offset:34176\n"
"v_accvgpr_read_b32 v192, a40\n"
"v_accvgpr_read_b32 v193, a41\n"
"v_accvgpr_read_b32 v194, a42\n"
"v_mfma_f32_16x16x32_bf16 a[144:147], v[212:215], v[128:131], a[144:147]\n"
"s_add_u32 m0, s44, 4544\n"
"v_accvgpr_read_b32 v195, a43\n"
"v_accvgpr_read_b32 v196, a56\n"
"v_accvgpr_read_b32 v197, a57\n"
"v_accvgpr_read_b32 v198, a58\n"
"v_mfma_f32_16x16x32_bf16 a[148:151], v[212:215], v[132:135], a[148:151]\n"
"ds_read_b64_tr_b16 v[210:211], v246 offset:42624\n"
"v_accvgpr_read_b32 v199, a59\n"
"v_cvt_pk_bf16_f32 v24, v192, v193\n"
"v_cvt_pk_bf16_f32 v25, v194, v195\n"
"v_cvt_pk_bf16_f32 v26, v196, v197\n"
"v_mfma_f32_16x16x32_bf16 a[152:155], v[212:215], v[136:139], a[152:155]\n"
"buffer_load_dwordx4 v250, s[24:27], s57 offen offset:1792 lds\n"
"v_cvt_pk_bf16_f32 v27, v198, v199\n"
"v_accvgpr_read_b32 v200, a44\n"
"v_accvgpr_read_b32 v201, a45\n"
"v_accvgpr_read_b32 v202, a46\n"
"v_mfma_f32_16x16x32_bf16 a[156:159], v[212:215], v[140:143], a[156:159]\n"
"ds_read_b64_tr_b16 v[212:213], v246 offset:34208\n"
"v_accvgpr_read_b32 v203, a47\n"
"v_accvgpr_read_b32 v204, a60\n"
"v_accvgpr_read_b32 v205, a61\n"
"s_waitcnt lgkmcnt(3)\n"
"v_mfma_f32_16x16x32_bf16 a[160:163], v[216:219], v[128:131], a[160:163]\n"
"v_accvgpr_read_b32 v206, a62\n"
"v_accvgpr_read_b32 v207, a63\n"
"v_cvt_pk_bf16_f32 v28, v200, v201\n"
"v_cvt_pk_bf16_f32 v29, v202, v203\n"
"v_cvt_pk_bf16_f32 v30, v204, v205\n"
"v_mfma_f32_16x16x32_bf16 a[164:167], v[216:219], v[132:135], a[164:167]\n"
"ds_read_b64_tr_b16 v[214:215], v246 offset:42656\n"
"v_cvt_pk_bf16_f32 v31, v206, v207\n"
"v_accvgpr_read_b32 v192, a64\n"
"v_accvgpr_read_b32 v193, a65\n"
"v_accvgpr_read_b32 v194, a66\n"
"v_mfma_f32_16x16x32_bf16 a[168:171], v[216:219], v[136:139], a[168:171]\n"
"s_add_u32 m0, s44, 4704\n"
"v_accvgpr_read_b32 v195, a67\n"
"v_accvgpr_read_b32 v196, a80\n"
"v_accvgpr_read_b32 v197, a81\n"
"v_accvgpr_read_b32 v198, a82\n"
"v_mfma_f32_16x16x32_bf16 a[172:175], v[216:219], v[140:143], a[172:175]\n"
"ds_read_b64_tr_b16 v[216:217], v246 offset:34240\n"
"v_accvgpr_read_b32 v199, a83\n"
"v_cvt_pk_bf16_f32 v32, v192, v193\n"
"v_cvt_pk_bf16_f32 v33, v194, v195\n"
"v_mfma_f32_16x16x32_bf16 a[176:179], v[220:223], v[128:131], a[176:179]\n"
"buffer_load_dwordx4 v250, s[24:27], s57 offen offset:2688 lds\n"
"v_cvt_pk_bf16_f32 v34, v196, v197\n"
"v_cvt_pk_bf16_f32 v35, v198, v199\n"
"v_accvgpr_read_b32 v200, a68\n"
"v_accvgpr_read_b32 v201, a69\n"
"v_mfma_f32_16x16x32_bf16 a[180:183], v[220:223], v[132:135], a[180:183]\n"
"ds_read_b64_tr_b16 v[218:219], v246 offset:42688\n"
"v_accvgpr_read_b32 v202, a70\n"
"v_accvgpr_read_b32 v203, a71\n"
"v_accvgpr_read_b32 v204, a84\n"
"v_accvgpr_read_b32 v205, a85\n"
"v_mfma_f32_16x16x32_bf16 a[184:187], v[220:223], v[136:139], a[184:187]\n"
"v_accvgpr_read_b32 v206, a86\n"
"v_accvgpr_read_b32 v207, a87\n"
"v_cvt_pk_bf16_f32 v36, v200, v201\n"
"v_cvt_pk_bf16_f32 v37, v202, v203\n"
"v_cvt_pk_bf16_f32 v38, v204, v205\n"
"v_mfma_f32_16x16x32_bf16 a[188:191], v[220:223], v[140:143], a[188:191]\n"
"ds_read_b64_tr_b16 v[220:221], v246 offset:34272\n"
"v_cvt_pk_bf16_f32 v39, v206, v207\n"
"v_accvgpr_read_b32 v192, a72\n"
"v_accvgpr_read_b32 v193, a73\n"
"s_waitcnt lgkmcnt(5)\n"
"v_mfma_f32_16x16x32_bf16 a[192:195], v[208:211], v[128:131], a[192:195]\n"
"v_accvgpr_read_b32 v194, a74\n"
"v_accvgpr_read_b32 v195, a75\n"
"v_accvgpr_read_b32 v196, a88\n"
"v_accvgpr_read_b32 v197, a89\n"
"v_accvgpr_read_b32 v198, a90\n"
"v_mfma_f32_16x16x32_bf16 a[196:199], v[208:211], v[132:135], a[196:199]\n"
"ds_read_b64_tr_b16 v[222:223], v246 offset:42720\n"
"v_accvgpr_read_b32 v199, a91\n"
"v_cvt_pk_bf16_f32 v40, v192, v193\n"
"v_cvt_pk_bf16_f32 v41, v194, v195\n"
"v_cvt_pk_bf16_f32 v42, v196, v197\n"
"v_mfma_f32_16x16x32_bf16 a[200:203], v[208:211], v[136:139], a[200:203]\n"
"v_cvt_pk_bf16_f32 v43, v198, v199\n"
"v_accvgpr_read_b32 v200, a76\n"
"v_accvgpr_read_b32 v201, a77\n"
"v_accvgpr_read_b32 v202, a78\n"
"v_accvgpr_read_b32 v203, a79\n"
"v_mfma_f32_16x16x32_bf16 a[204:207], v[208:211], v[140:143], a[204:207]\n"
"v_accvgpr_read_b32 v204, a92\n"
"v_accvgpr_read_b32 v205, a93\n"
"v_accvgpr_read_b32 v206, a94\n"
"v_accvgpr_read_b32 v207, a95\n"
"s_waitcnt lgkmcnt(4)\n"
"v_mfma_f32_16x16x32_bf16 a[208:211], v[212:215], v[128:131], a[208:211]\n"
"v_cvt_pk_bf16_f32 v44, v200, v201\n"
"v_cvt_pk_bf16_f32 v45, v202, v203\n"
"v_cvt_pk_bf16_f32 v46, v204, v205\n"
"v_cvt_pk_bf16_f32 v47, v206, v207\n"
"v_accvgpr_read_b32 v192, a96\n"
"v_mfma_f32_16x16x32_bf16 a[212:215], v[212:215], v[132:135], a[212:215]\n"
"v_accvgpr_read_b32 v193, a97\n"
"v_accvgpr_read_b32 v194, a98\n"
"v_accvgpr_read_b32 v195, a99\n"
"v_accvgpr_read_b32 v196, a112\n"
"v_accvgpr_read_b32 v197, a113\n"
"v_mfma_f32_16x16x32_bf16 a[216:219], v[212:215], v[136:139], a[216:219]\n"
"s_waitcnt vmcnt(18) lgkmcnt(0)\n"
"s_barrier\n"
"ds_read_b64_tr_b16 v[208:209], v252 offset:0\n"
"v_accvgpr_read_b32 v198, a114\n"
"v_accvgpr_read_b32 v199, a115\n"
"v_cvt_pk_bf16_f32 v48, v192, v193\n"
"v_mfma_f32_16x16x32_bf16 a[220:223], v[212:215], v[140:143], a[220:223]\n"
"ds_read_b64_tr_b16 v[210:211], v252 offset:256\n"
"v_cvt_pk_bf16_f32 v49, v194, v195\n"
"v_cvt_pk_bf16_f32 v50, v196, v197\n"
"v_cvt_pk_bf16_f32 v51, v198, v199\n"
"v_mfma_f32_16x16x32_bf16 a[224:227], v[216:219], v[128:131], a[224:227]\n"
"ds_read_b64_tr_b16 v[212:213], v252 offset:32\n"
"v_accvgpr_read_b32 v200, a100\n"
"v_accvgpr_read_b32 v201, a101\n"
"v_accvgpr_read_b32 v202, a102\n"
"v_accvgpr_read_b32 v203, a103\n"
"v_mfma_f32_16x16x32_bf16 a[228:231], v[216:219], v[132:135], a[228:231]\n"
"ds_read_b64_tr_b16 v[214:215], v252 offset:288\n"
"v_accvgpr_read_b32 v204, a116\n"
"v_accvgpr_read_b32 v205, a117\n"
"v_accvgpr_read_b32 v206, a118\n"
"v_accvgpr_read_b32 v207, a119\n"
"v_mfma_f32_16x16x32_bf16 a[232:235], v[216:219], v[136:139], a[232:235]\n"
"v_cvt_pk_bf16_f32 v52, v200, v201\n"
"v_cvt_pk_bf16_f32 v53, v202, v203\n"
"v_cvt_pk_bf16_f32 v54, v204, v205\n"
"v_cvt_pk_bf16_f32 v55, v206, v207\n"
"v_accvgpr_read_b32 v192, a104\n"
"v_mfma_f32_16x16x32_bf16 a[236:239], v[216:219], v[140:143], a[236:239]\n"
"ds_read_b64_tr_b16 v[216:217], v252 offset:64\n"
"v_accvgpr_read_b32 v193, a105\n"
"v_accvgpr_read_b32 v194, a106\n"
"v_accvgpr_read_b32 v195, a107\n"
"v_mfma_f32_16x16x32_bf16 a[240:243], v[220:223], v[128:131], a[240:243]\n"
"ds_read_b64_tr_b16 v[218:219], v252 offset:320\n"
"v_accvgpr_read_b32 v196, a120\n"
"v_accvgpr_read_b32 v197, a121\n"
"v_accvgpr_read_b32 v198, a122\n"
"v_accvgpr_read_b32 v199, a123\n"
"v_mfma_f32_16x16x32_bf16 a[244:247], v[220:223], v[132:135], a[244:247]\n"
"v_cvt_pk_bf16_f32 v56, v192, v193\n"
"v_cvt_pk_bf16_f32 v57, v194, v195\n"
"v_cvt_pk_bf16_f32 v58, v196, v197\n"
"v_cvt_pk_bf16_f32 v59, v198, v199\n"
"v_accvgpr_read_b32 v200, a108\n"
"v_mfma_f32_16x16x32_bf16 a[248:251], v[220:223], v[136:139], a[248:251]\n"
"v_accvgpr_read_b32 v201, a109\n"
"v_accvgpr_read_b32 v202, a110\n"
"v_accvgpr_read_b32 v203, a111\n"
"v_accvgpr_read_b32 v204, a124\n"
"v_accvgpr_read_b32 v205, a125\n"
"v_mfma_f32_16x16x32_bf16 a[252:255], v[220:223], v[140:143], a[252:255]\n"
"ds_read_b64_tr_b16 v[220:221], v252 offset:96\n"
"ds_read_b64_tr_b16 v[222:223], v252 offset:352\n"
"v_accvgpr_read_b32 v206, a126\n"
"v_accvgpr_read_b32 v207, a127\n"
"s_waitcnt lgkmcnt(6)\n"
"v_mfma_f32_16x16x32_bf16 v[160:163], v[208:211], v[0:3], 0\n"
"s_add_u32 s56, s58, s59\n"
"s_add_u32 s56, s56, 114688\n"
"s_add_u32 m0, s44, 33792\n"
"s_add_u32 s57, s56, 3584\n"
"v_cvt_pk_bf16_f32 v60, v200, v201\n"
"v_mfma_f32_16x16x32_bf16 v[164:167], v[208:211], v[4:7], 0\n"
"ds_read_b64 v[226:227], v255 offset:16384\n"
"v_cvt_pk_bf16_f32 v61, v202, v203\n"
"v_cvt_pk_bf16_f32 v62, v204, v205\n"
"v_cvt_pk_bf16_f32 v63, v206, v207\n"
"v_accvgpr_read_b32 v192, a128\n"
"v_mfma_f32_16x16x32_bf16 v[128:131], v[208:211], v[8:11], 0\n"
"buffer_load_dwordx4 v250, s[24:27], s56 offen lds\n"
"v_accvgpr_read_b32 v193, a129\n"
"v_accvgpr_read_b32 v194, a130\n"
"v_accvgpr_read_b32 v195, a131\n"
"v_accvgpr_read_b32 v196, a144\n"
"v_mfma_f32_16x16x32_bf16 v[132:135], v[208:211], v[12:15], 0\n"
"ds_read_b64_tr_b16 v[208:209], v252 offset:512\n"
"v_accvgpr_read_b32 v197, a145\n"
"v_accvgpr_read_b32 v198, a146\n"
"v_accvgpr_read_b32 v199, a147\n"
"s_waitcnt lgkmcnt(6)\n"
"v_mfma_f32_16x16x32_bf16 v[168:171], v[212:215], v[0:3], 0\n"
"s_cmp_eq_u32 s38, 0\n"
"s_cselect_b64 s[46:47], -1, 0\n"
"v_cvt_pk_bf16_f32 v64, v192, v193\n"
"v_cvt_pk_bf16_f32 v65, v194, v195\n"
"v_cvt_pk_bf16_f32 v66, v196, v197\n"
"v_mfma_f32_16x16x32_bf16 v[172:175], v[212:215], v[4:7], 0\n"
"ds_read_b64_tr_b16 v[210:211], v252 offset:768\n"
"v_and_b32 v228, 7, v253\n"
"v_cvt_pk_bf16_f32 v67, v198, v199\n"
"v_accvgpr_read_b32 v200, a132\n"
"v_accvgpr_read_b32 v201, a133\n"
"v_mfma_f32_16x16x32_bf16 v[136:139], v[212:215], v[8:11], 0\n"
"s_add_u32 m0, s44, 33952\n"
"s_lshl_b32 s65, s40, 2\n"
"v_accvgpr_read_b32 v202, a134\n"
"v_accvgpr_read_b32 v203, a135\n"
"v_accvgpr_read_b32 v204, a148\n"
"v_mfma_f32_16x16x32_bf16 v[140:143], v[212:215], v[12:15], 0\n"
"ds_read_b64_tr_b16 v[212:213], v252 offset:544\n"
"v_accvgpr_read_b32 v205, a149\n"
"v_accvgpr_read_b32 v206, a150\n"
"v_accvgpr_read_b32 v207, a151\n"
"s_waitcnt lgkmcnt(4)\n"
"v_mfma_f32_16x16x32_bf16 v[176:179], v[216:219], v[0:3], 0\n"
"buffer_load_dwordx4 v250, s[24:27], s56 offen offset:896 lds\n"
"v_lshl_add_u32 v244, v228, 5, s65\n"
"v_cvt_pk_bf16_f32 v68, v200, v201\n"
"v_cvt_pk_bf16_f32 v69, v202, v203\n"
"v_cvt_pk_bf16_f32 v70, v204, v205\n"
"v_mfma_f32_16x16x32_bf16 v[180:183], v[216:219], v[4:7], 0\n"
"ds_read_b64_tr_b16 v[214:215], v252 offset:800\n"
"s_lshl_b32 s65, s40, 5\n"
"v_cvt_pk_bf16_f32 v71, v206, v207\n"
"v_accvgpr_read_b32 v192, a136\n"
"v_accvgpr_read_b32 v193, a137\n"
"v_mfma_f32_16x16x32_bf16 v[144:147], v[216:219], v[8:11], 0\n"
"v_lshl_add_u32 v245, v228, 2, s65\n"
"s_sub_u32 s65, 256, s65\n"
"v_accvgpr_read_b32 v194, a138\n"
"v_accvgpr_read_b32 v195, a139\n"
"v_accvgpr_read_b32 v196, a152\n"
"v_mfma_f32_16x16x32_bf16 v[148:151], v[216:219], v[12:15], 0\n"
"ds_read_b64_tr_b16 v[216:217], v252 offset:576\n"
"v_accvgpr_read_b32 v197, a153\n"
"v_accvgpr_read_b32 v198, a154\n"
"v_accvgpr_read_b32 v199, a155\n"
"v_mfma_f32_16x16x32_bf16 v[184:187], v[220:223], v[0:3], 0\n"
"s_add_u32 m0, s44, 34112\n"
"v_cvt_pk_bf16_f32 v72, v192, v193\n"
"v_cvt_pk_bf16_f32 v73, v194, v195\n"
"v_cvt_pk_bf16_f32 v74, v196, v197\n"
"v_cvt_pk_bf16_f32 v75, v198, v199\n"
"v_mfma_f32_16x16x32_bf16 v[188:191], v[220:223], v[4:7], 0\n"
"ds_read_b64_tr_b16 v[218:219], v252 offset:832\n"
"v_accvgpr_read_b32 v200, a140\n"
"v_accvgpr_read_b32 v201, a141\n"
"v_accvgpr_read_b32 v202, a142\n"
"v_accvgpr_read_b32 v203, a143\n"
"v_mfma_f32_16x16x32_bf16 v[152:155], v[220:223], v[8:11], 0\n"
"buffer_load_dwordx4 v250, s[24:27], s56 offen offset:1792 lds\n"
"v_accvgpr_read_b32 v204, a156\n"
"v_accvgpr_read_b32 v205, a157\n"
"v_accvgpr_read_b32 v206, a158\n"
"v_accvgpr_read_b32 v207, a159\n"
"v_mfma_f32_16x16x32_bf16 v[156:159], v[220:223], v[12:15], 0\n"
"ds_read_b64_tr_b16 v[220:221], v252 offset:608\n"
"v_cvt_pk_bf16_f32 v76, v200, v201\n"
"v_cvt_pk_bf16_f32 v77, v202, v203\n"
"v_cvt_pk_bf16_f32 v78, v204, v205\n"
"s_waitcnt lgkmcnt(3)\n"
"v_mfma_f32_16x16x32_bf16 v[160:163], v[208:211], v[16:19], v[160:163]\n"
"v_rcp_f32 v226, v226\n"
"v_cvt_pk_bf16_f32 v79, v206, v207\n"
"v_accvgpr_read_b32 v192, a160\n"
"v_accvgpr_read_b32 v193, a161\n"
"v_mfma_f32_16x16x32_bf16 v[164:167], v[208:211], v[20:23], v[164:167]\n"
"ds_read_b64_tr_b16 v[222:223], v252 offset:864\n"
"v_accvgpr_read_b32 v194, a162\n"
"v_accvgpr_read_b32 v195, a163\n"
"v_accvgpr_read_b32 v196, a176\n"
"v_accvgpr_read_b32 v197, a177\n"
"v_mfma_f32_16x16x32_bf16 v[128:131], v[208:211], v[24:27], v[128:131]\n"
"s_add_u32 m0, s44, 34272\n"
"v_accvgpr_read_b32 v198, a178\n"
"v_accvgpr_read_b32 v199, a179\n"
"v_cvt_pk_bf16_f32 v80, v192, v193\n"
"v_cvt_pk_bf16_f32 v81, v194, v195\n"
"v_mfma_f32_16x16x32_bf16 v[132:135], v[208:211], v[28:31], v[132:135]\n"
"ds_read_b64_tr_b16 v[208:209], v252 offset:8448\n"
"v_cvt_pk_bf16_f32 v82, v196, v197\n"
"v_cvt_pk_bf16_f32 v83, v198, v199\n"
"v_accvgpr_read_b32 v200, a164\n"
"v_mfma_f32_16x16x32_bf16 v[168:171], v[212:215], v[16:19], v[168:171]\n"
"buffer_load_dwordx4 v250, s[24:27], s56 offen offset:2688 lds\n"
"v_accvgpr_read_b32 v201, a165\n"
"v_accvgpr_read_b32 v202, a166\n"
"v_accvgpr_read_b32 v203, a167\n"
"v_accvgpr_read_b32 v204, a180\n"
"v_mfma_f32_16x16x32_bf16 v[172:175], v[212:215], v[20:23], v[172:175]\n"
"ds_read_b64_tr_b16 v[210:211], v252 offset:8704\n"
"v_accvgpr_read_b32 v205, a181\n"
"v_accvgpr_read_b32 v206, a182\n"
"v_accvgpr_read_b32 v207, a183\n"
"v_cvt_pk_bf16_f32 v84, v200, v201\n"
"v_mfma_f32_16x16x32_bf16 v[136:139], v[212:215], v[24:27], v[136:139]\n"
"v_rcp_f32 v227, v227\n"
"v_cvt_pk_bf16_f32 v85, v202, v203\n"
"v_cvt_pk_bf16_f32 v86, v204, v205\n"
"v_cvt_pk_bf16_f32 v87, v206, v207\n"
"v_mfma_f32_16x16x32_bf16 v[140:143], v[212:215], v[28:31], v[140:143]\n"
"ds_read_b64_tr_b16 v[212:213], v252 offset:8480\n"
"v_accvgpr_read_b32 v192, a168\n"
"v_accvgpr_read_b32 v193, a169\n"
"v_accvgpr_read_b32 v194, a170\n"
"s_waitcnt lgkmcnt(3)\n"
"v_mfma_f32_16x16x32_bf16 v[176:179], v[216:219], v[16:19], v[176:179]\n"
"s_add_u32 m0, s44, 38016\n"
"v_mul_f32 v224, v240, v226\n"
"v_accvgpr_read_b32 v195, a171\n"
"v_accvgpr_read_b32 v196, a184\n"
"v_accvgpr_read_b32 v197, a185\n"
"v_mfma_f32_16x16x32_bf16 v[180:183], v[216:219], v[20:23], v[180:183]\n"
"ds_read_b64_tr_b16 v[214:215], v252 offset:8736\n"
"v_mul_f32 v225, v241, v227\n"
"v_accvgpr_read_b32 v198, a186\n"
"v_accvgpr_read_b32 v199, a187\n"
"v_cvt_pk_bf16_f32 v88, v192, v193\n"
"v_mfma_f32_16x16x32_bf16 v[144:147], v[216:219], v[24:27], v[144:147]\n"
"buffer_load_dwordx4 v250, s[24:27], s57 offen lds\n"
"v_cvt_pk_bf16_f32 v89, v194, v195\n"
"v_cvt_pk_bf16_f32 v90, v196, v197\n"
"v_cvt_pk_bf16_f32 v91, v198, v199\n"
"v_accvgpr_read_b32 v200, a172\n"
"v_mfma_f32_16x16x32_bf16 v[148:151], v[216:219], v[28:31], v[148:151]\n"
"ds_read_b64_tr_b16 v[216:217], v252 offset:8512\n"
"v_accvgpr_read_b32 v201, a173\n"
"v_accvgpr_read_b32 v202, a174\n"
"v_accvgpr_read_b32 v203, a175\n"
"v_mfma_f32_16x16x32_bf16 v[184:187], v[220:223], v[16:19], v[184:187]\n"
"s_add_u32 s12, s74, s65\n"
"s_addc_u32 s13, s75, 0\n"
"v_accvgpr_read_b32 v204, a188\n"
"v_accvgpr_read_b32 v205, a189\n"
"v_accvgpr_read_b32 v206, a190\n"
"v_mfma_f32_16x16x32_bf16 v[188:191], v[220:223], v[20:23], v[188:191]\n"
"ds_read_b64_tr_b16 v[218:219], v252 offset:8768\n"
"global_load_dword v230, v245, s[12:13] sc1\n"
"v_accvgpr_read_b32 v207, a191\n"
"v_cvt_pk_bf16_f32 v92, v200, v201\n"
"v_cvt_pk_bf16_f32 v93, v202, v203\n"
"v_mfma_f32_16x16x32_bf16 v[152:155], v[220:223], v[24:27], v[152:155]\n"
"s_add_u32 m0, s44, 38176\n"
"v_cvt_pk_bf16_f32 v94, v204, v205\n"
"v_cvt_pk_bf16_f32 v95, v206, v207\n"
"v_accvgpr_read_b32 v192, a192\n"
"v_accvgpr_read_b32 v193, a193\n"
"v_mfma_f32_16x16x32_bf16 v[156:159], v[220:223], v[28:31], v[156:159]\n"
"ds_read_b64_tr_b16 v[220:221], v252 offset:8544\n"
"v_accvgpr_read_b32 v194, a194\n"
"v_accvgpr_read_b32 v195, a195\n"
"v_accvgpr_read_b32 v196, a208\n"
"s_waitcnt lgkmcnt(3)\n"
"v_mfma_f32_16x16x32_bf16 v[160:163], v[208:211], v[32:35], v[160:163]\n"
"buffer_load_dwordx4 v250, s[24:27], s57 offen offset:896 lds\n"
"v_accvgpr_read_b32 v197, a209\n"
"v_accvgpr_read_b32 v198, a210\n"
"v_accvgpr_read_b32 v199, a211\n"
"v_cvt_pk_bf16_f32 v96, v192, v193\n"
"v_mfma_f32_16x16x32_bf16 v[164:167], v[208:211], v[36:39], v[164:167]\n"
"ds_read_b64_tr_b16 v[222:223], v252 offset:8800\n"
"v_cvt_pk_bf16_f32 v97, v194, v195\n"
"v_cvt_pk_bf16_f32 v98, v196, v197\n"
"v_cvt_pk_bf16_f32 v99, v198, v199\n"
"v_accvgpr_read_b32 v200, a196\n"
"v_mfma_f32_16x16x32_bf16 v[128:131], v[208:211], v[40:43], v[128:131]\n"
"v_accvgpr_read_b32 v201, a197\n"
"v_accvgpr_read_b32 v202, a198\n"
"v_accvgpr_read_b32 v203, a199\n"
"v_accvgpr_read_b32 v204, a212\n"
"v_accvgpr_read_b32 v205, a213\n"
"v_mfma_f32_16x16x32_bf16 v[132:135], v[208:211], v[44:47], v[132:135]\n"
"ds_read_b64_tr_b16 v[208:209], v252 offset:8960\n"
"v_accvgpr_read_b32 v206, a214\n"
"v_accvgpr_read_b32 v207, a215\n"
"v_cvt_pk_bf16_f32 v100, v200, v201\n"
"v_mfma_f32_16x16x32_bf16 v[168:171], v[212:215], v[32:35], v[168:171]\n"
"s_add_u32 m0, s44, 38336\n"
"v_cvt_pk_bf16_f32 v101, v202, v203\n"
"v_cvt_pk_bf16_f32 v102, v204, v205\n"
"v_cvt_pk_bf16_f32 v103, v206, v207\n"
"v_accvgpr_read_b32 v192, a200\n"
"v_mfma_f32_16x16x32_bf16 v[172:175], v[212:215], v[36:39], v[172:175]\n"
"ds_read_b64_tr_b16 v[210:211], v252 offset:9216\n"
"v_accvgpr_read_b32 v193, a201\n"
"v_accvgpr_read_b32 v194, a202\n"
"v_accvgpr_read_b32 v195, a203\n"
"v_accvgpr_read_b32 v196, a216\n"
"v_mfma_f32_16x16x32_bf16 v[136:139], v[212:215], v[40:43], v[136:139]\n"
"buffer_load_dwordx4 v250, s[24:27], s57 offen offset:1792 lds\n"
"v_accvgpr_read_b32 v197, a217\n"
"v_accvgpr_read_b32 v198, a218\n"
"v_accvgpr_read_b32 v199, a219\n"
"v_cvt_pk_bf16_f32 v104, v192, v193\n"
"v_mfma_f32_16x16x32_bf16 v[140:143], v[212:215], v[44:47], v[140:143]\n"
"ds_read_b64_tr_b16 v[212:213], v252 offset:8992\n"
"v_cvt_pk_bf16_f32 v105, v194, v195\n"
"v_cvt_pk_bf16_f32 v106, v196, v197\n"
"v_cvt_pk_bf16_f32 v107, v198, v199\n"
"s_waitcnt lgkmcnt(3)\n"
"v_mfma_f32_16x16x32_bf16 v[176:179], v[216:219], v[32:35], v[176:179]\n"
"v_accvgpr_read_b32 v200, a204\n"
"v_accvgpr_read_b32 v201, a205\n"
"v_accvgpr_read_b32 v202, a206\n"
"v_accvgpr_read_b32 v203, a207\n"
"v_accvgpr_read_b32 v204, a220\n"
"v_mfma_f32_16x16x32_bf16 v[180:183], v[216:219], v[36:39], v[180:183]\n"
"ds_read_b64_tr_b16 v[214:215], v252 offset:9248\n"
"v_accvgpr_read_b32 v205, a221\n"
"v_accvgpr_read_b32 v206, a222\n"
"v_accvgpr_read_b32 v207, a223\n"
"v_cvt_pk_bf16_f32 v108, v200, v201\n"
"v_mfma_f32_16x16x32_bf16 v[144:147], v[216:219], v[40:43], v[144:147]\n"
"s_add_u32 m0, s44, 38496\n"
"v_cvt_pk_bf16_f32 v109, v202, v203\n"
"v_cvt_pk_bf16_f32 v110, v204, v205\n"
"v_cvt_pk_bf16_f32 v111, v206, v207\n"
"v_accvgpr_read_b32 v192, a224\n"
"v_mfma_f32_16x16x32_bf16 v[148:151], v[216:219], v[44:47], v[148:151]\n"
"ds_read_b64_tr_b16 v[216:217], v252 offset:9024\n"
"v_accvgpr_read_b32 v193, a225\n"
"v_accvgpr_read_b32 v194, a226\n"
"v_accvgpr_read_b32 v195, a227\n"
"v_mfma_f32_16x16x32_bf16 v[184:187], v[220:223], v[32:35], v[184:187]\n"
"buffer_load_dwordx4 v250, s[24:27], s57 offen offset:2688 lds\n"
"v_accvgpr_read_b32 v196, a240\n"
"v_accvgpr_read_b32 v197, a241\n"
"v_accvgpr_read_b32 v198, a242\n"
"v_accvgpr_read_b32 v199, a243\n"
"v_mfma_f32_16x16x32_bf16 v[188:191], v[220:223], v[36:39], v[188:191]\n"
"ds_read_b64_tr_b16 v[218:219], v252 offset:9280\n"
"s_add_u32 s59, s59, 128\n"
"s_cmp_eq_u32 s59, 896\n"
"s_cselect_b32 s59, 0, s59\n"
"v_cvt_pk_bf16_f32 v112, v192, v193\n"
"v_mfma_f32_16x16x32_bf16 v[152:155], v[220:223], v[40:43], v[152:155]\n"
"v_cvt_pk_bf16_f32 v113, v194, v195\n"
"v_cvt_pk_bf16_f32 v114, v196, v197\n"
"v_cvt_pk_bf16_f32 v115, v198, v199\n"
"v_accvgpr_read_b32 v200, a228\n"
"v_accvgpr_read_b32 v201, a229\n"
"v_mfma_f32_16x16x32_bf16 v[156:159], v[220:223], v[44:47], v[156:159]\n"
"ds_read_b64_tr_b16 v[220:221], v252 offset:9056\n"
"v_accvgpr_read_b32 v202, a230\n"
"v_accvgpr_read_b32 v203, a231\n"
"v_accvgpr_read_b32 v204, a244\n"
"s_waitcnt lgkmcnt(5)\n"
"v_mfma_f32_16x16x32_bf16 v[160:163], v[208:211], v[48:51], v[160:163]\n"
"v_accvgpr_read_b32 v205, a245\n"
"v_accvgpr_read_b32 v206, a246\n"
"v_accvgpr_read_b32 v207, a247\n"
"v_cvt_pk_bf16_f32 v116, v200, v201\n"
"v_cvt_pk_bf16_f32 v117, v202, v203\n"
"v_mfma_f32_16x16x32_bf16 v[164:167], v[208:211], v[52:55], v[164:167]\n"
"ds_read_b64_tr_b16 v[222:223], v252 offset:9312\n"
"v_cvt_pk_bf16_f32 v118, v204, v205\n"
"v_cvt_pk_bf16_f32 v119, v206, v207\n"
"v_accvgpr_read_b32 v192, a232\n"
"v_accvgpr_read_b32 v193, a233\n"
"v_mfma_f32_16x16x32_bf16 v[128:131], v[208:211], v[56:59], v[128:131]\n"
"v_accvgpr_read_b32 v194, a234\n"
"v_accvgpr_read_b32 v195, a235\n"
"v_accvgpr_read_b32 v196, a248\n"
"v_accvgpr_read_b32 v197, a249\n"
"v_accvgpr_read_b32 v198, a250\n"
"v_mfma_f32_16x16x32_bf16 v[132:135], v[208:211], v[60:63], v[132:135]\n"
"v_accvgpr_read_b32 v199, a251\n"
"v_cvt_pk_bf16_f32 v120, v192, v193\n"
"v_cvt_pk_bf16_f32 v121, v194, v195\n"
"v_cvt_pk_bf16_f32 v122, v196, v197\n"
"s_waitcnt lgkmcnt(4)\n"
"v_mfma_f32_16x16x32_bf16 v[168:171], v[212:215], v[48:51], v[168:171]\n"
"v_cvt_pk_bf16_f32 v123, v198, v199\n"
"v_accvgpr_read_b32 v200, a236\n"
"v_accvgpr_read_b32 v201, a237\n"
"v_accvgpr_read_b32 v202, a238\n"
"v_accvgpr_read_b32 v203, a239\n"
"v_mfma_f32_16x16x32_bf16 v[172:175], v[212:215], v[52:55], v[172:175]\n"
"v_accvgpr_read_b32 v204, a252\n"
"v_accvgpr_read_b32 v205, a253\n"
"v_accvgpr_read_b32 v206, a254\n"
"v_accvgpr_read_b32 v207, a255\n"
"v_cvt_pk_bf16_f32 v124, v200, v201\n"
"v_mfma_f32_16x16x32_bf16 v[136:139], v[212:215], v[56:59], v[136:139]\n"
"s_waitcnt vmcnt(19) lgkmcnt(0)\n"
"s_barrier\n"
"ds_read_b64_tr_b16 v[208:209], v252 offset:33792\n"
"v_cvt_pk_bf16_f32 v125, v202, v203\n"
"v_cvt_pk_bf16_f32 v126, v204, v205\n"
"v_cvt_pk_bf16_f32 v127, v206, v207\n"
"v_mfma_f32_16x16x32_bf16 v[140:143], v[212:215], v[60:63], v[140:143]\n"
"ds_read_b64_tr_b16 v[210:211], v252 offset:34048\n"
"v_mfma_f32_16x16x32_bf16 v[176:179], v[216:219], v[48:51], v[176:179]\n"
"ds_read_b64_tr_b16 v[212:213], v252 offset:33824\n"
"v_mfma_f32_16x16x32_bf16 v[180:183], v[216:219], v[52:55], v[180:183]\n"
"ds_read_b64_tr_b16 v[214:215], v252 offset:34080\n"
"v_mfma_f32_16x16x32_bf16 v[144:147], v[216:219], v[56:59], v[144:147]\n"
"v_mfma_f32_16x16x32_bf16 v[148:151], v[216:219], v[60:63], v[148:151]\n"
"ds_read_b64_tr_b16 v[216:217], v252 offset:34304\n"
"v_mfma_f32_16x16x32_bf16 v[184:187], v[220:223], v[48:51], v[184:187]\n"
"ds_read_b64_tr_b16 v[218:219], v252 offset:34560\n"
"v_mfma_f32_16x16x32_bf16 v[188:191], v[220:223], v[52:55], v[188:191]\n"
"v_mfma_f32_16x16x32_bf16 v[152:155], v[220:223], v[56:59], v[152:155]\n"
"v_mfma_f32_16x16x32_bf16 v[156:159], v[220:223], v[60:63], v[156:159]\n"
"ds_read_b64_tr_b16 v[220:221], v252 offset:34336\n"
"ds_read_b64_tr_b16 v[222:223], v252 offset:34592\n"
"s_waitcnt lgkmcnt(6)\n"
"v_mfma_f32_16x16x32_bf16 v[160:163], v[208:211], v[64:67], v[160:163]\n"
"s_add_u32 s56, s58, s59\n"
"s_add_u32 m0, s44, 67584\n"
"s_add_u32 s57, s56, 3584\n"
"v_mfma_f32_16x16x32_bf16 v[164:167], v[208:211], v[68:71], v[164:167]\n"
"v_mfma_f32_16x16x32_bf16 v[128:131], v[208:211], v[72:75], v[128:131]\n"
"buffer_load_dwordx4 v250, s[24:27], s56 offen lds\n"
"v_mfma_f32_16x16x32_bf16 v[132:135], v[208:211], v[76:79], v[132:135]\n"
"ds_read_b64_tr_b16 v[208:209], v252 offset:42240\n"
"s_waitcnt lgkmcnt(5)\n"
"v_mfma_f32_16x16x32_bf16 v[168:171], v[212:215], v[64:67], v[168:171]\n"
"v_mfma_f32_16x16x32_bf16 v[172:175], v[212:215], v[68:71], v[172:175]\n"
"ds_read_b64_tr_b16 v[210:211], v252 offset:42496\n"
"v_mfma_f32_16x16x32_bf16 v[136:139], v[212:215], v[72:75], v[136:139]\n"
"s_add_u32 m0, s44, 67744\n"
"v_mfma_f32_16x16x32_bf16 v[140:143], v[212:215], v[76:79], v[140:143]\n"
"ds_read_b64_tr_b16 v[212:213], v252 offset:42272\n"
"s_waitcnt lgkmcnt(3)\n"
"v_mfma_f32_16x16x32_bf16 v[160:163], v[216:219], v[80:83], v[160:163]\n"
"buffer_load_dwordx4 v250, s[24:27], s56 offen offset:896 lds\n"
"v_mfma_f32_16x16x32_bf16 v[164:167], v[216:219], v[84:87], v[164:167]\n"
"ds_read_b64_tr_b16 v[214:215], v252 offset:42528\n"
"v_mfma_f32_16x16x32_bf16 v[128:131], v[216:219], v[88:91], v[128:131]\n"
"v_mfma_f32_16x16x32_bf16 v[132:135], v[216:219], v[92:95], v[132:135]\n"
"ds_read_b64_tr_b16 v[216:217], v252 offset:42752\n"
"v_mfma_f32_16x16x32_bf16 v[168:171], v[220:223], v[80:83], v[168:171]\n"
"s_add_u32 m0, s44, 67904\n"
"v_mfma_f32_16x16x32_bf16 v[172:175], v[220:223], v[84:87], v[172:175]\n"
"ds_read_b64_tr_b16 v[218:219], v252 offset:43008\n"
"v_mfma_f32_16x16x32_bf16 v[136:139], v[220:223], v[88:91], v[136:139]\n"
"buffer_load_dwordx4 v250, s[24:27], s56 offen offset:1792 lds\n"
"v_mfma_f32_16x16x32_bf16 v[140:143], v[220:223], v[92:95], v[140:143]\n"
"ds_read_b64_tr_b16 v[220:221], v252 offset:42784\n"
"s_waitcnt lgkmcnt(3)\n"
"v_mfma_f32_16x16x32_bf16 v[160:163], v[208:211], v[96:99], v[160:163]\n"
"v_mfma_f32_16x16x32_bf16 v[164:167], v[208:211], v[100:103], v[164:167]\n"
"ds_read_b64_tr_b16 v[222:223], v252 offset:43040\n"
"v_mfma_f32_16x16x32_bf16 v[128:131], v[208:211], v[104:107], v[128:131]\n"
"s_add_u32 m0, s44, 68064\n"
"v_mfma_f32_16x16x32_bf16 v[132:135], v[208:211], v[108:111], v[132:135]\n"
"ds_read_b64_tr_b16 v[208:209], v252 offset:33856\n"
"v_mfma_f32_16x16x32_bf16 v[168:171], v[212:215], v[96:99], v[168:171]\n"
"buffer_load_dwordx4 v250, s[24:27], s56 offen offset:2688 lds\n"
"v_mfma_f32_16x16x32_bf16 v[172:175], v[212:215], v[100:103], v[172:175]\n"
"ds_read_b64_tr_b16 v[210:211], v252 offset:34112\n"
"v_mfma_f32_16x16x32_bf16 v[136:139], v[212:215], v[104:107], v[136:139]\n"
"v_mfma_f32_16x16x32_bf16 v[140:143], v[212:215], v[108:111], v[140:143]\n"
"ds_read_b64_tr_b16 v[212:213], v252 offset:33888\n"
"s_waitcnt lgkmcnt(3)\n"
"v_mfma_f32_16x16x32_bf16 v[160:163], v[216:219], v[112:115], v[160:163]\n"
"s_add_u32 m0, s44, 71808\n"
"v_mfma_f32_16x16x32_bf16 v[164:167], v[216:219], v[116:119], v[164:167]\n"
"ds_read_b64_tr_b16 v[214:215], v252 offset:34144\n"
"v_mfma_f32_16x16x32_bf16 v[128:131], v[216:219], v[120:123], v[128:131]\n"
"buffer_load_dwordx4 v250, s[24:27], s57 offen lds\n"
"v_mfma_f32_16x16x32_bf16 v[132:135], v[216:219], v[124:127], v[132:135]\n"
"ds_read_b64_tr_b16 v[216:217], v252 offset:34368\n"
"v_mfma_f32_16x16x32_bf16 v[168:171], v[220:223], v[112:115], v[168:171]\n"
"v_mfma_f32_16x16x32_bf16 v[172:175], v[220:223], v[116:119], v[172:175]\n"
"ds_read_b64_tr_b16 v[218:219], v252 offset:34624\n"
"v_mfma_f32_16x16x32_bf16 v[136:139], v[220:223], v[120:123], v[136:139]\n"
"s_add_u32 m0, s44, 71968\n"
"v_mfma_f32_16x16x32_bf16 v[140:143], v[220:223], v[124:127], v[140:143]\n"
"ds_read_b64_tr_b16 v[220:221], v252 offset:34400\n"
"s_waitcnt lgkmcnt(3)\n"
"v_mfma_f32_16x16x32_bf16 v[176:179], v[208:211], v[64:67], v[176:179]\n"
"buffer_load_dwordx4 v250, s[24:27], s57 offen offset:896 lds\n"
"v_mfma_f32_16x16x32_bf16 v[180:183], v[208:211], v[68:71], v[180:183]\n"
"ds_read_b64_tr_b16 v[222:223], v252 offset:34656\n"
"v_mfma_f32_16x16x32_bf16 v[144:147], v[208:211], v[72:75], v[144:147]\n"
"v_mfma_f32_16x16x32_bf16 v[148:151], v[208:211], v[76:79], v[148:151]\n"
"ds_read_b64_tr_b16 v[208:209], v252 offset:42304\n"
"v_cvt_pk_bf16_f32 v200, v128, v129\n"
"v_mfma_f32_16x16x32_bf16 v[184:187], v[212:215], v[64:67], v[184:187]\n"
"s_add_u32 m0, s44, 72128\n"
"s_add_u32 s69, s69, 0\n"
"v_cvt_pk_bf16_f32 v201, v130, v131\n"
"v_mfma_f32_16x16x32_bf16 v[188:191], v[212:215], v[68:71], v[188:191]\n"
"ds_read_b64_tr_b16 v[210:211], v252 offset:42560\n"
"s_max_i32 s15, s69, 0\n"
"v_cvt_pk_bf16_f32 v202, v136, v137\n"
"v_mfma_f32_16x16x32_bf16 v[152:155], v[212:215], v[72:75], v[152:155]\n"
"buffer_load_dwordx4 v250, s[24:27], s57 offen offset:1792 lds\n"
"s_waitcnt vmcnt(10)\n"
"v_or_b32 v228, s15, v230\n"
"v_cvt_pk_bf16_f32 v203, v138, v139\n"
"v_mfma_f32_16x16x32_bf16 v[156:159], v[212:215], v[76:79], v[156:159]\n"
"ds_read_b64_tr_b16 v[212:213], v252 offset:42336\n"
"v_cvt_pk_bf16_f32 v204, v132, v133\n"
"s_waitcnt lgkmcnt(3)\n"
"v_mfma_f32_16x16x32_bf16 v[176:179], v[216:219], v[80:83], v[176:179]\n"
"s_mov_b64 exec, s[46:47]\n"
"global_store_dword v245, v228, s[74:75] sc1\n"
"s_mov_b64 exec, -1\n"
"v_mfma_f32_16x16x32_bf16 v[180:183], v[216:219], v[84:87], v[180:183]\n"
"ds_read_b64_tr_b16 v[214:215], v252 offset:42592\n"
"v_cvt_pk_bf16_f32 v205, v134, v135\n"
"v_cvt_pk_bf16_f32 v206, v140, v141\n"
"v_mfma_f32_16x16x32_bf16 v[144:147], v[216:219], v[88:91], v[144:147]\n"
"s_add_u32 m0, s44, 72288\n"
"v_cvt_pk_bf16_f32 v207, v142, v143\n"
"v_mfma_f32_16x16x32_bf16 v[148:151], v[216:219], v[92:95], v[148:151]\n"
"ds_read_b64_tr_b16 v[216:217], v252 offset:42816\n"
"v_mfma_f32_16x16x32_bf16 v[184:187], v[220:223], v[80:83], v[184:187]\n"
"buffer_load_dwordx4 v250, s[24:27], s57 offen offset:2688 lds\n"
"v_mfma_f32_16x16x32_bf16 v[188:191], v[220:223], v[84:87], v[188:191]\n"
"ds_read_b64_tr_b16 v[218:219], v252 offset:43072\n"
"v_mfma_f32_16x16x32_bf16 v[152:155], v[220:223], v[88:91], v[152:155]\n"
"v_mfma_f32_16x16x32_bf16 v[156:159], v[220:223], v[92:95], v[156:159]\n"
"ds_read_b64_tr_b16 v[220:221], v252 offset:42848\n"
"s_waitcnt lgkmcnt(5)\n"
"v_mfma_f32_16x16x32_bf16 v[176:179], v[208:211], v[96:99], v[176:179]\n"
"ds_write_b128 v254, v[200:203] offset:18432\n"
"v_mfma_f32_16x16x32_bf16 v[180:183], v[208:211], v[100:103], v[180:183]\n"
"ds_read_b64_tr_b16 v[222:223], v252 offset:43104\n"
"v_mfma_f32_16x16x32_bf16 v[144:147], v[208:211], v[104:107], v[144:147]\n"
"ds_write_b128 v254, v[204:207] offset:19456\n"
"v_mfma_f32_16x16x32_bf16 v[148:151], v[208:211], v[108:111], v[148:151]\n"
"s_waitcnt lgkmcnt(6)\n"
"v_mfma_f32_16x16x32_bf16 v[184:187], v[212:215], v[96:99], v[184:187]\n"
"v_mfma_f32_16x16x32_bf16 v[188:191], v[212:215], v[100:103], v[188:191]\n"
"v_mfma_f32_16x16x32_bf16 v[152:155], v[212:215], v[104:107], v[152:155]\n"
"s_waitcnt lgkmcnt(0)\n"
"s_barrier\n"
"ds_read_b64_tr_b16 v[208:209], v251 offset:0\n"
"v_mfma_f32_16x16x32_bf16 v[156:159], v[212:215], v[108:111], v[156:159]\n"
"ds_read_b64_tr_b16 v[210:211], v251 offset:256\n"
"v_mfma_f32_16x16x32_bf16 v[176:179], v[216:219], v[112:115], v[176:179]\n"
"ds_read_b64_tr_b16 v[212:213], v251 offset:32\n"
"v_mfma_f32_16x16x32_bf16 v[180:183], v[216:219], v[116:119], v[180:183]\n"
"ds_read_b64_tr_b16 v[214:215], v251 offset:288\n"
"v_mfma_f32_16x16x32_bf16 v[144:147], v[216:219], v[120:123], v[144:147]\n"
"v_mfma_f32_16x16x32_bf16 v[148:151], v[216:219], v[124:127], v[148:151]\n"
"ds_read_b64_tr_b16 v[216:217], v251 offset:512\n"
"v_mfma_f32_16x16x32_bf16 v[184:187], v[220:223], v[112:115], v[184:187]\n"
"ds_read_b64_tr_b16 v[218:219], v251 offset:768\n"
"v_mfma_f32_16x16x32_bf16 v[188:191], v[220:223], v[116:119], v[188:191]\n"
"v_mfma_f32_16x16x32_bf16 v[152:155], v[220:223], v[120:123], v[152:155]\n"
"v_mfma_f32_16x16x32_bf16 v[156:159], v[220:223], v[124:127], v[156:159]\n"
"ds_read_b64_tr_b16 v[220:221], v251 offset:544\n"
"ds_read_b64_tr_b16 v[222:223], v251 offset:800\n"
"s_mov_b32 s64, 2\n"
"LO_%=:\n"
"s_waitcnt lgkmcnt(6)\n"
"v_mfma_f32_16x16x32_bf16 a[0:3], v[208:211], v[0:3], 0\n"
"s_add_u32 s56, s58, s59\n"
"s_add_u32 s56, s56, 114688\n"
"s_add_u32 m0, s44, 101376\n"
"s_add_u32 s57, s56, 3584\n"
"v_mfma_f32_16x16x32_bf16 a[4:7], v[208:211], v[4:7], 0\n"
"s_add_u32 s66, s61, s60\n"
"ds_read_b128 v[192:195], v255 offset:18432\n"
"v_mfma_f32_16x16x32_bf16 v[128:131], v[208:211], v[8:11], 0\n"
"buffer_load_dwordx4 v250, s[24:27], s56 offen lds\n"
"ds_read_b128 v[196:199], v255 offset:19456\n"
"v_mfma_f32_16x16x32_bf16 v[132:135], v[208:211], v[12:15], 0\n"
"ds_read_b64_tr_b16 v[208:209], v251 offset:8448\n"
"s_waitcnt lgkmcnt(7)\n"
"v_mfma_f32_16x16x32_bf16 a[8:11], v[212:215], v[0:3], 0\n"
"v_cvt_pk_bf16_f32 v200, v144, v145\n"
"v_cvt_pk_bf16_f32 v201, v146, v147\n"
"v_mfma_f32_16x16x32_bf16 a[12:15], v[212:215], v[4:7], 0\n"
"ds_read_b64_tr_b16 v[210:211], v251 offset:8704\n"
"v_cvt_pk_bf16_f32 v202, v152, v153\n"
"v_mfma_f32_16x16x32_bf16 v[136:139], v[212:215], v[8:11], 0\n"
"s_add_u32 m0, s44, 101536\n"
"v_cvt_pk_bf16_f32 v203, v154, v155\n"
"v_mfma_f32_16x16x32_bf16 v[140:143], v[212:215], v[12:15], 0\n"
"ds_read_b64_tr_b16 v[212:213], v251 offset:8480\n"
"s_waitcnt lgkmcnt(5)\n"
"v_mfma_f32_16x16x32_bf16 a[0:3], v[216:219], v[16:19], a[0:3]\n"
"buffer_load_dwordx4 v250, s[24:27], s56 offen offset:896 lds\n"
"v_cvt_pk_bf16_f32 v204, v148, v149\n"
"v_mfma_f32_16x16x32_bf16 a[4:7], v[216:219], v[20:23], a[4:7]\n"
"ds_read_b64_tr_b16 v[214:215], v251 offset:8736\n"
"v_cvt_pk_bf16_f32 v205, v150, v151\n"
"v_mfma_f32_16x16x32_bf16 v[128:131], v[216:219], v[24:27], v[128:131]\n"
"v_cvt_pk_bf16_f32 v206, v156, v157\n"
"v_cvt_pk_bf16_f32 v207, v158, v159\n"
"v_mfma_f32_16x16x32_bf16 v[132:135], v[216:219], v[28:31], v[132:135]\n"
"ds_read_b64_tr_b16 v[216:217], v251 offset:8960\n"
"v_mfma_f32_16x16x32_bf16 a[8:11], v[220:223], v[16:19], a[8:11]\n"
"s_add_u32 m0, s44, 101696\n"
"s_waitcnt lgkmcnt(6)\n"
"v_lshlrev_b32 v232, 16, v192\n"
"v_mfma_f32_16x16x32_bf16 a[12:15], v[220:223], v[20:23], a[12:15]\n"
"ds_read_b64_tr_b16 v[218:219], v251 offset:9216\n"
"v_and_b32 v233, s21, v192\n"
"v_mfma_f32_16x16x32_bf16 v[136:139], v[220:223], v[24:27], v[136:139]\n"
"buffer_load_dwordx4 v250, s[24:27], s56 offen offset:1792 lds\n"
"v_fma_f32 v232, v232, v224, v160\n"
"v_mfma_f32_16x16x32_bf16 v[140:143], v[220:223], v[28:31], v[140:143]\n"
"ds_read_b64_tr_b16 v[220:221], v251 offset:8992\n"
"s_waitcnt lgkmcnt(3)\n"
"v_mfma_f32_16x16x32_bf16 a[0:3], v[208:211], v[32:35], a[0:3]\n"
"v_fma_f32 v233, v233, v224, v161\n"
"v_cvt_pk_bf16_f32 v192, v232, v233\n"
"v_mfma_f32_16x16x32_bf16 a[4:7], v[208:211], v[36:39], a[4:7]\n"
"ds_read_b64_tr_b16 v[222:223], v251 offset:9248\n"
"v_lshlrev_b32 v236, 16, v193\n"
"v_mfma_f32_16x16x32_bf16 v[128:131], v[208:211], v[40:43], v[128:131]\n"
"s_add_u32 m0, s44, 101856\n"
"v_and_b32 v237, s21, v193\n"
"v_mfma_f32_16x16x32_bf16 v[132:135], v[208:211], v[44:47], v[132:135]\n"
"ds_read_b64_tr_b16 v[208:209], v251 offset:64\n"
"v_mfma_f32_16x16x32_bf16 a[8:11], v[212:215], v[32:35], a[8:11]\n"
"buffer_load_dwordx4 v250, s[24:27], s56 offen offset:2688 lds\n"
"v_fma_f32 v236, v236, v224, v162\n"
"v_mfma_f32_16x16x32_bf16 a[12:15], v[212:215], v[36:39], a[12:15]\n"
"ds_read_b64_tr_b16 v[210:211], v251 offset:320\n"
"v_fma_f32 v237, v237, v224, v163\n"
"v_mfma_f32_16x16x32_bf16 v[136:139], v[212:215], v[40:43], v[136:139]\n"
"v_cvt_pk_bf16_f32 v193, v236, v237\n"
"v_lshlrev_b32 v232, 16, v194\n"
"v_mfma_f32_16x16x32_bf16 v[140:143], v[212:215], v[44:47], v[140:143]\n"
"ds_read_b64_tr_b16 v[212:213], v251 offset:96\n"
"s_waitcnt lgkmcnt(3)\n"
"v_mfma_f32_16x16x32_bf16 a[0:3], v[216:219], v[48:51], a[0:3]\n"
"s_add_u32 m0, s44, 105600\n"
"v_and_b32 v233, s21, v194\n"
"v_mfma_f32_16x16x32_bf16 a[4:7], v[216:219], v[52:55], a[4:7]\n"
"ds_read_b64_tr_b16 v[214:215], v251 offset:352\n"
"v_fma_f32 v232, v232, v224, v168\n"
"v_mfma_f32_16x16x32_bf16 v[128:131], v[216:219], v[56:59], v[128:131]\n"
"buffer_load_dwordx4 v250, s[24:27], s57 offen lds\n"
"v_fma_f32 v233, v233, v224, v169\n"
"v_mfma_f32_16x16x32_bf16 v[132:135], v[216:219], v[60:63], v[132:135]\n"
"ds_read_b64_tr_b16 v[216:217], v251 offset:576\n"
"v_mfma_f32_16x16x32_bf16 a[8:11], v[220:223], v[48:51], a[8:11]\n"
"v_cvt_pk_bf16_f32 v194, v232, v233\n"
"v_lshlrev_b32 v236, 16, v195\n"
"v_mfma_f32_16x16x32_bf16 a[12:15], v[220:223], v[52:55], a[12:15]\n"
"ds_read_b64_tr_b16 v[218:219], v251 offset:832\n"
"v_and_b32 v237, s21, v195\n"
"v_mfma_f32_16x16x32_bf16 v[136:139], v[220:223], v[56:59], v[136:139]\n"
"s_add_u32 m0, s44, 105760\n"
"v_fma_f32 v236, v236, v224, v170\n"
"v_mfma_f32_16x16x32_bf16 v[140:143], v[220:223], v[60:63], v[140:143]\n"
"ds_read_b64_tr_b16 v[220:221], v251 offset:608\n"
"s_waitcnt lgkmcnt(3)\n"
"v_mfma_f32_16x16x32_bf16 a[16:19], v[208:211], v[0:3], 0\n"
"buffer_load_dwordx4 v250, s[24:27], s57 offen offset:896 lds\n"
"v_fma_f32 v237, v237, v224, v171\n"
"v_mfma_f32_16x16x32_bf16 a[20:23], v[208:211], v[4:7], 0\n"
"ds_read_b64_tr_b16 v[222:223], v251 offset:864\n"
"v_cvt_pk_bf16_f32 v195, v236, v237\n"
"v_mfma_f32_16x16x32_bf16 v[144:147], v[208:211], v[8:11], 0\n"
"s_add_u32 s65, s66, 14336\n"
"buffer_store_dwordx4 v[192:195], v255, s[28:31], s66 offen offset:0 sc1\n"
"v_mfma_f32_16x16x32_bf16 v[148:151], v[208:211], v[12:15], 0\n"
"ds_read_b64_tr_b16 v[208:209], v251 offset:8512\n"
"v_mfma_f32_16x16x32_bf16 a[24:27], v[212:215], v[0:3], 0\n"
"s_add_u32 m0, s44, 105920\n"
"v_lshlrev_b32 v232, 16, v196\n"
"v_mfma_f32_16x16x32_bf16 a[28:31], v[212:215], v[4:7], 0\n"
"ds_read_b64_tr_b16 v[210:211], v251 offset:8768\n"
"v_and_b32 v233, s21, v196\n"
"v_mfma_f32_16x16x32_bf16 v[152:155], v[212:215], v[8:11], 0\n"
"buffer_load_dwordx4 v250, s[24:27], s57 offen offset:1792 lds\n"
"v_fma_f32 v232, v232, v225, v164\n"
"v_mfma_f32_16x16x32_bf16 v[156:159], v[212:215], v[12:15], 0\n"
"ds_read_b64_tr_b16 v[212:213], v251 offset:8544\n"
"s_waitcnt lgkmcnt(3)\n"
"v_mfma_f32_16x16x32_bf16 a[16:19], v[216:219], v[16:19], a[16:19]\n"
"v_fma_f32 v233, v233, v225, v165\n"
"v_cvt_pk_bf16_f32 v196, v232, v233\n"
"v_mfma_f32_16x16x32_bf16 a[20:23], v[216:219], v[20:23], a[20:23]\n"
"ds_read_b64_tr_b16 v[214:215], v251 offset:8800\n"
"v_lshlrev_b32 v236, 16, v197\n"
"v_mfma_f32_16x16x32_bf16 v[144:147], v[216:219], v[24:27], v[144:147]\n"
"s_add_u32 m0, s44, 106080\n"
"v_and_b32 v237, s21, v197\n"
"v_mfma_f32_16x16x32_bf16 v[148:151], v[216:219], v[28:31], v[148:151]\n"
"ds_read_b64_tr_b16 v[216:217], v251 offset:9024\n"
"v_mfma_f32_16x16x32_bf16 a[24:27], v[220:223], v[16:19], a[24:27]\n"
"buffer_load_dwordx4 v250, s[24:27], s57 offen offset:2688 lds\n"
"v_fma_f32 v236, v236, v225, v166\n"
"v_mfma_f32_16x16x32_bf16 a[28:31], v[220:223], v[20:23], a[28:31]\n"
"ds_read_b64_tr_b16 v[218:219], v251 offset:9280\n"
"s_add_u32 s59, s59, 128\n"
"s_cmp_eq_u32 s59, 896\n"
"s_cselect_b32 s59, 0, s59\n"
"v_mfma_f32_16x16x32_bf16 v[152:155], v[220:223], v[24:27], v[152:155]\n"
"v_fma_f32 v237, v237, v225, v167\n"
"v_cvt_pk_bf16_f32 v197, v236, v237\n"
"v_mfma_f32_16x16x32_bf16 v[156:159], v[220:223], v[28:31], v[156:159]\n"
"ds_read_b64_tr_b16 v[220:221], v251 offset:9056\n"
"s_waitcnt lgkmcnt(5)\n"
"v_mfma_f32_16x16x32_bf16 a[16:19], v[208:211], v[32:35], a[16:19]\n"
"ds_write_b128 v254, v[200:203] offset:16384\n"
"v_lshlrev_b32 v232, 16, v198\n"
"v_mfma_f32_16x16x32_bf16 a[20:23], v[208:211], v[36:39], a[20:23]\n"
"ds_read_b64_tr_b16 v[222:223], v251 offset:9312\n"
"v_and_b32 v233, s21, v198\n"
"v_mfma_f32_16x16x32_bf16 v[144:147], v[208:211], v[40:43], v[144:147]\n"
"ds_write_b128 v254, v[204:207] offset:17408\n"
"v_fma_f32 v232, v232, v225, v172\n"
"v_mfma_f32_16x16x32_bf16 v[148:151], v[208:211], v[44:47], v[148:151]\n"
"v_fma_f32 v233, v233, v225, v173\n"
"s_waitcnt lgkmcnt(6)\n"
"v_mfma_f32_16x16x32_bf16 a[24:27], v[212:215], v[32:35], a[24:27]\n"
"v_cvt_pk_bf16_f32 v198, v232, v233\n"
"v_lshlrev_b32 v236, 16, v199\n"
"v_mfma_f32_16x16x32_bf16 a[28:31], v[212:215], v[36:39], a[28:31]\n"
"v_and_b32 v237, s21, v199\n"
"v_fma_f32 v236, v236, v225, v174\n"
"v_mfma_f32_16x16x32_bf16 v[152:155], v[212:215], v[40:43], v[152:155]\n"
"s_waitcnt vmcnt(18) lgkmcnt(0)\n"
"s_barrier\n"
"ds_read_b64_tr_b16 v[208:209], v251 offset:33792\n"
"v_mfma_f32_16x16x32_bf16 v[156:159], v[212:215], v[44:47], v[156:159]\n"
"ds_read_b64_tr_b16 v[210:211], v251 offset:34048\n"
"v_mfma_f32_16x16x32_bf16 a[16:19], v[216:219], v[48:51], a[16:19]\n"
"ds_read_b64_tr_b16 v[212:213], v251 offset:33824\n"
"v_fma_f32 v237, v237, v225, v175\n"
"v_mfma_f32_16x16x32_bf16 a[20:23], v[216:219], v[52:55], a[20:23]\n"
"ds_read_b64_tr_b16 v[214:215], v251 offset:34080\n"
"v_cvt_pk_bf16_f32 v199, v236, v237\n"
"v_mfma_f32_16x16x32_bf16 v[144:147], v[216:219], v[56:59], v[144:147]\n"
"buffer_store_dwordx4 v[196:199], v255, s[28:31], s65 offen offset:0 sc1\n"
"s_add_u32 s69, s69, 1\n"
"v_mfma_f32_16x16x32_bf16 v[148:151], v[216:219], v[60:63], v[148:151]\n"
"ds_read_b64_tr_b16 v[216:217], v251 offset:34304\n"
"v_mfma_f32_16x16x32_bf16 a[24:27], v[220:223], v[48:51], a[24:27]\n"
"ds_read_b64_tr_b16 v[218:219], v251 offset:34560\n"
"s_max_i32 s15, s69, 0\n"
"v_mfma_f32_16x16x32_bf16 a[28:31], v[220:223], v[52:55], a[28:31]\n"
"v_or_b32 v228, s15, v230\n"
"v_mfma_f32_16x16x32_bf16 v[152:155], v[220:223], v[56:59], v[152:155]\n"
"s_mov_b64 exec, s[46:47]\n"
"global_store_dword v245, v228, s[74:75] sc1\n"
"s_mov_b64 exec, -1\n"
"v_mfma_f32_16x16x32_bf16 v[156:159], v[220:223], v[60:63], v[156:159]\n"
"ds_read_b64_tr_b16 v[220:221], v251 offset:34336\n"
"ds_read_b64_tr_b16 v[222:223], v251 offset:34592\n"
"s_waitcnt lgkmcnt(6)\n"
"v_mfma_f32_16x16x32_bf16 a[0:3], v[208:211], v[64:67], a[0:3]\n"
"s_add_u32 s56, s58, s59\n"
"s_add_u32 m0, s44, 0\n"
"s_add_u32 s57, s56, 3584\n"
"v_mfma_f32_16x16x32_bf16 a[4:7], v[208:211], v[68:71], a[4:7]\n"
"s_add_u32 s66, s61, s60\n"
"s_add_u32 s60, s60, 2048\n"
"s_cmp_eq_u32 s60, 14336\n"
"s_cselect_b32 s60, 0, s60\n"
"ds_read_b128 v[192:195], v255 offset:16384\n"
"v_mfma_f32_16x16x32_bf16 v[128:131], v[208:211], v[72:75], v[128:131]\n"
"buffer_load_dwordx4 v250, s[24:27], s56 offen lds\n"
"ds_read_b128 v[196:199], v255 offset:17408\n"
"v_mfma_f32_16x16x32_bf16 v[132:135], v[208:211], v[76:79], v[132:135]\n"
"ds_read_b64_tr_b16 v[208:209], v251 offset:42240\n"
"s_waitcnt lgkmcnt(7)\n"
"v_mfma_f32_16x16x32_bf16 a[8:11], v[212:215], v[64:67], a[8:11]\n"
"v_mfma_f32_16x16x32_bf16 a[12:15], v[212:215], v[68:71], a[12:15]\n"
"ds_read_b64_tr_b16 v[210:211], v251 offset:42496\n"
"v_mfma_f32_16x16x32_bf16 v[136:139], v[212:215], v[72:75], v[136:139]\n"
"s_add_u32 m0, s44, 160\n"
"v_mfma_f32_16x16x32_bf16 v[140:143], v[212:215], v[76:79], v[140:143]\n"
"ds_read_b64_tr_b16 v[212:213], v251 offset:42272\n"
"s_waitcnt lgkmcnt(5)\n"
"v_mfma_f32_16x16x32_bf16 a[0:3], v[216:219], v[80:83], a[0:3]\n"
"buffer_load_dwordx4 v250, s[24:27], s56 offen offset:896 lds\n"
"s_waitcnt lgkmcnt(4)\n"
"v_lshlrev_b32 v232, 16, v192\n"
"v_mfma_f32_16x16x32_bf16 a[4:7], v[216:219], v[84:87], a[4:7]\n"
"ds_read_b64_tr_b16 v[214:215], v251 offset:42528\n"
"v_and_b32 v233, s21, v192\n"
"v_mfma_f32_16x16x32_bf16 v[128:131], v[216:219], v[88:91], v[128:131]\n"
"v_fma_f32 v232, v232, v224, v176\n"
"v_fma_f32 v233, v233, v224, v177\n"
"v_mfma_f32_16x16x32_bf16 v[132:135], v[216:219], v[92:95], v[132:135]\n"
"ds_read_b64_tr_b16 v[216:217], v251 offset:42752\n"
"v_mfma_f32_16x16x32_bf16 a[8:11], v[220:223], v[80:83], a[8:11]\n"
"s_add_u32 m0, s44, 320\n"
"v_cvt_pk_bf16_f32 v192, v232, v233\n"
"v_mfma_f32_16x16x32_bf16 a[12:15], v[220:223], v[84:87], a[12:15]\n"
"ds_read_b64_tr_b16 v[218:219], v251 offset:43008\n"
"v_lshlrev_b32 v236, 16, v193\n"
"v_mfma_f32_16x16x32_bf16 v[136:139], v[220:223], v[88:91], v[136:139]\n"
"buffer_load_dwordx4 v250, s[24:27], s56 offen offset:1792 lds\n"
"v_and_b32 v237, s21, v193\n"
"v_mfma_f32_16x16x32_bf16 v[140:143], v[220:223], v[92:95], v[140:143]\n"
"ds_read_b64_tr_b16 v[220:221], v251 offset:42784\n"
"s_waitcnt lgkmcnt(3)\n"
"v_mfma_f32_16x16x32_bf16 a[0:3], v[208:211], v[96:99], a[0:3]\n"
"v_fma_f32 v236, v236, v224, v178\n"
"v_fma_f32 v237, v237, v224, v179\n"
"v_mfma_f32_16x16x32_bf16 a[4:7], v[208:211], v[100:103], a[4:7]\n"
"ds_read_b64_tr_b16 v[222:223], v251 offset:43040\n"
"v_cvt_pk_bf16_f32 v193, v236, v237\n"
"v_mfma_f32_16x16x32_bf16 v[128:131], v[208:211], v[104:107], v[128:131]\n"
"s_add_u32 m0, s44, 480\n"
"v_lshlrev_b32 v232, 16, v194\n"
"v_mfma_f32_16x16x32_bf16 v[132:135], v[208:211], v[108:111], v[132:135]\n"
"ds_read_b64_tr_b16 v[208:209], v251 offset:33856\n"
"v_mfma_f32_16x16x32_bf16 a[8:11], v[212:215], v[96:99], a[8:11]\n"
"buffer_load_dwordx4 v250, s[24:27], s56 offen offset:2688 lds\n"
"v_and_b32 v233, s21, v194\n"
"v_mfma_f32_16x16x32_bf16 a[12:15], v[212:215], v[100:103], a[12:15]\n"
"ds_read_b64_tr_b16 v[210:211], v251 offset:34112\n"
"v_fma_f32 v232, v232, v224, v184\n"
"v_mfma_f32_16x16x32_bf16 v[136:139], v[212:215], v[104:107], v[136:139]\n"
"v_fma_f32 v233, v233, v224, v185\n"
"v_cvt_pk_bf16_f32 v194, v232, v233\n"
"v_mfma_f32_16x16x32_bf16 v[140:143], v[212:215], v[108:111], v[140:143]\n"
"ds_read_b64_tr_b16 v[212:213], v251 offset:33888\n"
"s_waitcnt lgkmcnt(3)\n"
"v_mfma_f32_16x16x32_bf16 a[0:3], v[216:219], v[112:115], a[0:3]\n"
"s_add_u32 m0, s44, 4224\n"
"v_lshlrev_b32 v236, 16, v195\n"
"v_mfma_f32_16x16x32_bf16 a[4:7], v[216:219], v[116:119], a[4:7]\n"
"ds_read_b64_tr_b16 v[214:215], v251 offset:34144\n"
"v_and_b32 v237, s21, v195\n"
"v_mfma_f32_16x16x32_bf16 v[128:131], v[216:219], v[120:123], v[128:131]\n"
"buffer_load_dwordx4 v250, s[24:27], s57 offen lds\n"
"v_fma_f32 v236, v236, v224, v186\n"
"v_mfma_f32_16x16x32_bf16 v[132:135], v[216:219], v[124:127], v[132:135]\n"
"ds_read_b64_tr_b16 v[216:217], v251 offset:34368\n"
"v_mfma_f32_16x16x32_bf16 a[8:11], v[220:223], v[112:115], a[8:11]\n"
"v_fma_f32 v237, v237, v224, v187\n"
"v_cvt_pk_bf16_f32 v195, v236, v237\n"
"v_mfma_f32_16x16x32_bf16 a[12:15], v[220:223], v[116:119], a[12:15]\n"
"ds_read_b64_tr_b16 v[218:219], v251 offset:34624\n"
"s_add_u32 s65, s66, 14336\n"
"v_mfma_f32_16x16x32_bf16 v[136:139], v[220:223], v[120:123], v[136:139]\n"
"s_add_u32 m0, s44, 4384\n"
"buffer_store_dwordx4 v[192:195], v255, s[28:31], s66 offen offset:1024 sc1\n"
"v_mfma_f32_16x16x32_bf16 v[140:143], v[220:223], v[124:127], v[140:143]\n"
"ds_read_b64_tr_b16 v[220:221], v251 offset:34400\n"
"s_waitcnt lgkmcnt(3)\n"
"v_mfma_f32_16x16x32_bf16 a[16:19], v[208:211], v[64:67], a[16:19]\n"
"buffer_load_dwordx4 v250, s[24:27], s57 offen offset:896 lds\n"
"v_lshlrev_b32 v232, 16, v196\n"
"v_mfma_f32_16x16x32_bf16 a[20:23], v[208:211], v[68:71], a[20:23]\n"
"ds_read_b64_tr_b16 v[222:223], v251 offset:34656\n"
"v_and_b32 v233, s21, v196\n"
"v_mfma_f32_16x16x32_bf16 v[144:147], v[208:211], v[72:75], v[144:147]\n"
"v_fma_f32 v232, v232, v225, v180\n"
"v_fma_f32 v233, v233, v225, v181\n"
"v_mfma_f32_16x16x32_bf16 v[148:151], v[208:211], v[76:79], v[148:151]\n"
"ds_read_b64_tr_b16 v[208:209], v251 offset:42304\n"
"v_mfma_f32_16x16x32_bf16 a[24:27], v[212:215], v[64:67], a[24:27]\n"
"s_add_u32 m0, s44, 4544\n"
"v_cvt_pk_bf16_f32 v200, v128, v129\n"
"v_mfma_f32_16x16x32_bf16 a[28:31], v[212:215], v[68:71], a[28:31]\n"
"ds_read_b64_tr_b16 v[210:211], v251 offset:42560\n"
"v_cvt_pk_bf16_f32 v201, v130, v131\n"
"v_mfma_f32_16x16x32_bf16 v[152:155], v[212:215], v[72:75], v[152:155]\n"
"buffer_load_dwordx4 v250, s[24:27], s57 offen offset:1792 lds\n"
"v_cvt_pk_bf16_f32 v202, v136, v137\n"
"v_mfma_f32_16x16x32_bf16 v[156:159], v[212:215], v[76:79], v[156:159]\n"
"ds_read_b64_tr_b16 v[212:213], v251 offset:42336\n"
"s_waitcnt lgkmcnt(3)\n"
"v_mfma_f32_16x16x32_bf16 a[16:19], v[216:219], v[80:83], a[16:19]\n"
"v_cvt_pk_bf16_f32 v203, v138, v139\n"
"v_cvt_pk_bf16_f32 v204, v132, v133\n"
"v_mfma_f32_16x16x32_bf16 a[20:23], v[216:219], v[84:87], a[20:23]\n"
"ds_read_b64_tr_b16 v[214:215], v251 offset:42592\n"
"v_cvt_pk_bf16_f32 v205, v134, v135\n"
"v_mfma_f32_16x16x32_bf16 v[144:147], v[216:219], v[88:91], v[144:147]\n"
"s_add_u32 m0, s44, 4704\n"
"v_cvt_pk_bf16_f32 v206, v140, v141\n"
"v_mfma_f32_16x16x32_bf16 v[148:151], v[216:219], v[92:95], v[148:151]\n"
"ds_read_b64_tr_b16 v[216:217], v251 offset:42816\n"
"v_mfma_f32_16x16x32_bf16 a[24:27], v[220:223], v[80:83], a[24:27]\n"
"buffer_load_dwordx4 v250, s[24:27], s57 offen offset:2688 lds\n"
"v_cvt_pk_bf16_f32 v207, v142, v143\n"
"v_mfma_f32_16x16x32_bf16 a[28:31], v[220:223], v[84:87], a[28:31]\n"
"ds_read_b64_tr_b16 v[218:219], v251 offset:43072\n"
"v_cvt_pk_bf16_f32 v196, v232, v233\n"
"v_mfma_f32_16x16x32_bf16 v[152:155], v[220:223], v[88:91], v[152:155]\n"
"v_lshlrev_b32 v236, 16, v197\n"
"v_and_b32 v237, s21, v197\n"
"v_mfma_f32_16x16x32_bf16 v[156:159], v[220:223], v[92:95], v[156:159]\n"
"ds_read_b64_tr_b16 v[220:221], v251 offset:42848\n"
"s_waitcnt lgkmcnt(5)\n"
"v_mfma_f32_16x16x32_bf16 a[16:19], v[208:211], v[96:99], a[16:19]\n"
"ds_write_b128 v254, v[200:203] offset:18432\n"
"v_fma_f32 v236, v236, v225, v182\n"
"v_mfma_f32_16x16x32_bf16 a[20:23], v[208:211], v[100:103], a[20:23]\n"
"ds_read_b64_tr_b16 v[222:223], v251 offset:43104\n"
"v_fma_f32 v237, v237, v225, v183\n"
"v_mfma_f32_16x16x32_bf16 v[144:147], v[208:211], v[104:107], v[144:147]\n"
"ds_write_b128 v254, v[204:207] offset:19456\n"
"v_cvt_pk_bf16_f32 v197, v236, v237\n"
"v_mfma_f32_16x16x32_bf16 v[148:151], v[208:211], v[108:111], v[148:151]\n"
"v_lshlrev_b32 v232, 16, v198\n"
"s_waitcnt lgkmcnt(6)\n"
"v_mfma_f32_16x16x32_bf16 a[24:27], v[212:215], v[96:99], a[24:27]\n"
"v_and_b32 v233, s21, v198\n"
"v_fma_f32 v232, v232, v225, v188\n"
"v_mfma_f32_16x16x32_bf16 a[28:31], v[212:215], v[100:103], a[28:31]\n"
"v_fma_f32 v233, v233, v225, v189\n"
"v_cvt_pk_bf16_f32 v198, v232, v233\n"
"v_mfma_f32_16x16x32_bf16 v[152:155], v[212:215], v[104:107], v[152:155]\n"
"s_waitcnt vmcnt(20) lgkmcnt(0)\n"
"s_barrier\n"
"ds_read_b64_tr_b16 v[208:209], v252 offset:0\n"
"v_mfma_f32_16x16x32_bf16 v[156:159], v[212:215], v[108:111], v[156:159]\n"
"ds_read_b64_tr_b16 v[210:211], v252 offset:256\n"
"v_mfma_f32_16x16x32_bf16 a[16:19], v[216:219], v[112:115], a[16:19]\n"
"ds_read_b64_tr_b16 v[212:213], v252 offset:32\n"
"v_lshlrev_b32 v236, 16, v199\n"
"v_mfma_f32_16x16x32_bf16 a[20:23], v[216:219], v[116:119], a[20:23]\n"
"ds_read_b64_tr_b16 v[214:215], v252 offset:288\n"
"v_and_b32 v237, s21, v199\n"
"v_mfma_f32_16x16x32_bf16 v[144:147], v[216:219], v[120:123], v[144:147]\n"
"v_fma_f32 v236, v236, v225, v190\n"
"v_fma_f32 v237, v237, v225, v191\n"
"v_mfma_f32_16x16x32_bf16 v[148:151], v[216:219], v[124:127], v[148:151]\n"
"ds_read_b64_tr_b16 v[216:217], v252 offset:512\n"
"v_mfma_f32_16x16x32_bf16 a[24:27], v[220:223], v[112:115], a[24:27]\n"
"ds_read_b64_tr_b16 v[218:219], v252 offset:768\n"
"v_cvt_pk_bf16_f32 v199, v236, v237\n"
"v_mfma_f32_16x16x32_bf16 a[28:31], v[220:223], v[116:119], a[28:31]\n"
"buffer_store_dwordx4 v[196:199], v255, s[28:31], s65 offen offset:1024 sc1\n"
"v_mfma_f32_16x16x32_bf16 v[152:155], v[220:223], v[120:123], v[152:155]\n"
"v_mfma_f32_16x16x32_bf16 v[156:159], v[220:223], v[124:127], v[156:159]\n"
"ds_read_b64_tr_b16 v[220:221], v252 offset:544\n"
"ds_read_b64_tr_b16 v[222:223], v252 offset:800\n"
"s_waitcnt lgkmcnt(6)\n"
"v_mfma_f32_16x16x32_bf16 v[160:163], v[208:211], v[0:3], 0\n"
"s_add_u32 s56, s58, s59\n"
"s_add_u32 s56, s56, 114688\n"
"s_add_u32 m0, s44, 33792\n"
"s_add_u32 s57, s56, 3584\n"
"v_mfma_f32_16x16x32_bf16 v[164:167], v[208:211], v[4:7], 0\n"
"s_add_u32 s66, s61, s60\n"
"ds_read_b128 v[192:195], v255 offset:18432\n"
"v_mfma_f32_16x16x32_bf16 v[128:131], v[208:211], v[8:11], 0\n"
"buffer_load_dwordx4 v250, s[24:27], s56 offen lds\n"
"ds_read_b128 v[196:199], v255 offset:19456\n"
"v_mfma_f32_16x16x32_bf16 v[132:135], v[208:211], v[12:15], 0\n"
"ds_read_b64_tr_b16 v[208:209], v252 offset:8448\n"
"s_waitcnt lgkmcnt(7)\n"
"v_mfma_f32_16x16x32_bf16 v[168:171], v[212:215], v[0:3], 0\n"
"v_cvt_pk_bf16_f32 v200, v144, v145\n"
"v_cvt_pk_bf16_f32 v201, v146, v147\n"
"v_mfma_f32_16x16x32_bf16 v[172:175], v[212:215], v[4:7], 0\n"
"ds_read_b64_tr_b16 v[210:211], v252 offset:8704\n"
"v_cvt_pk_bf16_f32 v202, v152, v153\n"
"v_mfma_f32_16x16x32_bf16 v[136:139], v[212:215], v[8:11], 0\n"
"s_add_u32 m0, s44, 33952\n"
"v_cvt_pk_bf16_f32 v203, v154, v155\n"
"v_mfma_f32_16x16x32_bf16 v[140:143], v[212:215], v[12:15], 0\n"
"ds_read_b64_tr_b16 v[212:213], v252 offset:8480\n"
"v_accvgpr_read_b32 v234, a0\n"
"s_waitcnt lgkmcnt(5)\n"
"v_mfma_f32_16x16x32_bf16 v[160:163], v[216:219], v[16:19], v[160:163]\n"
"buffer_load_dwordx4 v250, s[24:27], s56 offen offset:896 lds\n"
"v_cvt_pk_bf16_f32 v204, v148, v149\n"
"v_accvgpr_read_b32 v235, a1\n"
"v_mfma_f32_16x16x32_bf16 v[164:167], v[216:219], v[20:23], v[164:167]\n"
"ds_read_b64_tr_b16 v[214:215], v252 offset:8736\n"
"v_cvt_pk_bf16_f32 v205, v150, v151\n"
"s_waitcnt lgkmcnt(5)\n"
"v_lshlrev_b32 v232, 16, v192\n"
"v_mfma_f32_16x16x32_bf16 v[128:131], v[216:219], v[24:27], v[128:131]\n"
"v_cvt_pk_bf16_f32 v206, v156, v157\n"
"v_cvt_pk_bf16_f32 v207, v158, v159\n"
"v_and_b32 v233, s21, v192\n"
"v_mfma_f32_16x16x32_bf16 v[132:135], v[216:219], v[28:31], v[132:135]\n"
"ds_read_b64_tr_b16 v[216:217], v252 offset:8960\n"
"v_fma_f32 v232, v232, v224, v234\n"
"v_mfma_f32_16x16x32_bf16 v[168:171], v[220:223], v[16:19], v[168:171]\n"
"s_add_u32 m0, s44, 34112\n"
"v_fma_f32 v233, v233, v224, v235\n"
"v_cvt_pk_bf16_f32 v192, v232, v233\n"
"v_mfma_f32_16x16x32_bf16 v[172:175], v[220:223], v[20:23], v[172:175]\n"
"ds_read_b64_tr_b16 v[218:219], v252 offset:9216\n"
"v_accvgpr_read_b32 v238, a2\n"
"v_accvgpr_read_b32 v239, a3\n"
"v_mfma_f32_16x16x32_bf16 v[136:139], v[220:223], v[24:27], v[136:139]\n"
"buffer_load_dwordx4 v250, s[24:27], s56 offen offset:1792 lds\n"
"v_lshlrev_b32 v236, 16, v193\n"
"v_and_b32 v237, s21, v193\n"
"v_mfma_f32_16x16x32_bf16 v[140:143], v[220:223], v[28:31], v[140:143]\n"
"ds_read_b64_tr_b16 v[220:221], v252 offset:8992\n"
"v_fma_f32 v236, v236, v224, v238\n"
"s_waitcnt lgkmcnt(3)\n"
"v_mfma_f32_16x16x32_bf16 v[160:163], v[208:211], v[32:35], v[160:163]\n"
"v_fma_f32 v237, v237, v224, v239\n"
"v_cvt_pk_bf16_f32 v193, v236, v237\n"
"v_accvgpr_read_b32 v234, a8\n"
"v_mfma_f32_16x16x32_bf16 v[164:167], v[208:211], v[36:39], v[164:167]\n"
"ds_read_b64_tr_b16 v[222:223], v252 offset:9248\n"
"v_accvgpr_read_b32 v235, a9\n"
"v_lshlrev_b32 v232, 16, v194\n"
"v_mfma_f32_16x16x32_bf16 v[128:131], v[208:211], v[40:43], v[128:131]\n"
"s_add_u32 m0, s44, 34272\n"
"v_and_b32 v233, s21, v194\n"
"v_fma_f32 v232, v232, v224, v234\n"
"v_mfma_f32_16x16x32_bf16 v[132:135], v[208:211], v[44:47], v[132:135]\n"
"ds_read_b64_tr_b16 v[208:209], v252 offset:64\n"
"v_fma_f32 v233, v233, v224, v235\n"
"v_mfma_f32_16x16x32_bf16 v[168:171], v[212:215], v[32:35], v[168:171]\n"
"buffer_load_dwordx4 v250, s[24:27], s56 offen offset:2688 lds\n"
"v_cvt_pk_bf16_f32 v194, v232, v233\n"
"v_accvgpr_read_b32 v238, a10\n"
"v_mfma_f32_16x16x32_bf16 v[172:175], v[212:215], v[36:39], v[172:175]\n"
"ds_read_b64_tr_b16 v[210:211], v252 offset:320\n"
"v_accvgpr_read_b32 v239, a11\n"
"v_lshlrev_b32 v236, 16, v195\n"
"v_mfma_f32_16x16x32_bf16 v[136:139], v[212:215], v[40:43], v[136:139]\n"
"v_and_b32 v237, s21, v195\n"
"v_fma_f32 v236, v236, v224, v238\n"
"v_fma_f32 v237, v237, v224, v239\n"
"v_mfma_f32_16x16x32_bf16 v[140:143], v[212:215], v[44:47], v[140:143]\n"
"ds_read_b64_tr_b16 v[212:213], v252 offset:96\n"
"v_cvt_pk_bf16_f32 v195, v236, v237\n"
"s_waitcnt lgkmcnt(3)\n"
"v_mfma_f32_16x16x32_bf16 v[160:163], v[216:219], v[48:51], v[160:163]\n"
"s_add_u32 m0, s44, 38016\n"
"s_add_u32 s65, s66, 14336\n"
"buffer_store_dwordx4 v[192:195], v255, s[28:31], s66 offen offset:0 sc1\n"
"v_mfma_f32_16x16x32_bf16 v[164:167], v[216:219], v[52:55], v[164:167]\n"
"ds_read_b64_tr_b16 v[214:215], v252 offset:352\n"
"v_accvgpr_read_b32 v234, a4\n"
"v_accvgpr_read_b32 v235, a5\n"
"v_mfma_f32_16x16x32_bf16 v[128:131], v[216:219], v[56:59], v[128:131]\n"
"buffer_load_dwordx4 v250, s[24:27], s57 offen lds\n"
"v_lshlrev_b32 v232, 16, v196\n"
"v_and_b32 v233, s21, v196\n"
"v_mfma_f32_16x16x32_bf16 v[132:135], v[216:219], v[60:63], v[132:135]\n"
"ds_read_b64_tr_b16 v[216:217], v252 offset:576\n"
"v_fma_f32 v232, v232, v225, v234\n"
"v_mfma_f32_16x16x32_bf16 v[168:171], v[220:223], v[48:51], v[168:171]\n"
"v_fma_f32 v233, v233, v225, v235\n"
"v_cvt_pk_bf16_f32 v196, v232, v233\n"
"v_accvgpr_read_b32 v238, a6\n"
"v_mfma_f32_16x16x32_bf16 v[172:175], v[220:223], v[52:55], v[172:175]\n"
"ds_read_b64_tr_b16 v[218:219], v252 offset:832\n"
"v_accvgpr_read_b32 v239, a7\n"
"v_lshlrev_b32 v236, 16, v197\n"
"v_mfma_f32_16x16x32_bf16 v[136:139], v[220:223], v[56:59], v[136:139]\n"
"s_add_u32 m0, s44, 38176\n"
"v_and_b32 v237, s21, v197\n"
"v_fma_f32 v236, v236, v225, v238\n"
"v_mfma_f32_16x16x32_bf16 v[140:143], v[220:223], v[60:63], v[140:143]\n"
"ds_read_b64_tr_b16 v[220:221], v252 offset:608\n"
"v_fma_f32 v237, v237, v225, v239\n"
"s_waitcnt lgkmcnt(3)\n"
"v_mfma_f32_16x16x32_bf16 v[176:179], v[208:211], v[0:3], 0\n"
"buffer_load_dwordx4 v250, s[24:27], s57 offen offset:896 lds\n"
"v_cvt_pk_bf16_f32 v197, v236, v237\n"
"v_accvgpr_read_b32 v234, a12\n"
"v_mfma_f32_16x16x32_bf16 v[180:183], v[208:211], v[4:7], 0\n"
"ds_read_b64_tr_b16 v[222:223], v252 offset:864\n"
"v_accvgpr_read_b32 v235, a13\n"
"v_lshlrev_b32 v232, 16, v198\n"
"v_mfma_f32_16x16x32_bf16 v[144:147], v[208:211], v[8:11], 0\n"
"v_and_b32 v233, s21, v198\n"
"v_fma_f32 v232, v232, v225, v234\n"
"v_fma_f32 v233, v233, v225, v235\n"
"v_mfma_f32_16x16x32_bf16 v[148:151], v[208:211], v[12:15], 0\n"
"ds_read_b64_tr_b16 v[208:209], v252 offset:8512\n"
"v_cvt_pk_bf16_f32 v198, v232, v233\n"
"v_mfma_f32_16x16x32_bf16 v[184:187], v[212:215], v[0:3], 0\n"
"s_add_u32 m0, s44, 38336\n"
"v_accvgpr_read_b32 v238, a14\n"
"v_accvgpr_read_b32 v239, a15\n"
"v_mfma_f32_16x16x32_bf16 v[188:191], v[212:215], v[4:7], 0\n"
"ds_read_b64_tr_b16 v[210:211], v252 offset:8768\n"
"v_lshlrev_b32 v236, 16, v199\n"
"v_and_b32 v237, s21, v199\n"
"v_mfma_f32_16x16x32_bf16 v[152:155], v[212:215], v[8:11], 0\n"
"buffer_load_dwordx4 v250, s[24:27], s57 offen offset:1792 lds\n"
"v_fma_f32 v236, v236, v225, v238\n"
"v_fma_f32 v237, v237, v225, v239\n"
"v_mfma_f32_16x16x32_bf16 v[156:159], v[212:215], v[12:15], 0\n"
"ds_read_b64_tr_b16 v[212:213], v252 offset:8544\n"
"v_cvt_pk_bf16_f32 v199, v236, v237\n"
"s_waitcnt lgkmcnt(3)\n"
"v_mfma_f32_16x16x32_bf16 v[176:179], v[216:219], v[16:19], v[176:179]\n"
"buffer_store_dwordx4 v[196:199], v255, s[28:31], s65 offen offset:0 sc1\n"
"v_mfma_f32_16x16x32_bf16 v[180:183], v[216:219], v[20:23], v[180:183]\n"
"ds_read_b64_tr_b16 v[214:215], v252 offset:8800\n"
"v_mfma_f32_16x16x32_bf16 v[144:147], v[216:219], v[24:27], v[144:147]\n"
"s_add_u32 m0, s44, 38496\n"
"v_mfma_f32_16x16x32_bf16 v[148:151], v[216:219], v[28:31], v[148:151]\n"
"ds_read_b64_tr_b16 v[216:217], v252 offset:9024\n"
"v_mfma_f32_16x16x32_bf16 v[184:187], v[220:223], v[16:19], v[184:187]\n"
"buffer_load_dwordx4 v250, s[24:27], s57 offen offset:2688 lds\n"
"v_mfma_f32_16x16x32_bf16 v[188:191], v[220:223], v[20:23], v[188:191]\n"
"ds_read_b64_tr_b16 v[218:219], v252 offset:9280\n"
"s_add_u32 s59, s59, 128\n"
"s_cmp_eq_u32 s59, 896\n"
"s_cselect_b32 s59, 0, s59\n"
"v_mfma_f32_16x16x32_bf16 v[152:155], v[220:223], v[24:27], v[152:155]\n"
"v_mfma_f32_16x16x32_bf16 v[156:159], v[220:223], v[28:31], v[156:159]\n"
"ds_read_b64_tr_b16 v[220:221], v252 offset:9056\n"
"s_waitcnt lgkmcnt(5)\n"
"v_mfma_f32_16x16x32_bf16 v[176:179], v[208:211], v[32:35], v[176:179]\n"
"ds_write_b128 v254, v[200:203] offset:16384\n"
"v_mfma_f32_16x16x32_bf16 v[180:183], v[208:211], v[36:39], v[180:183]\n"
"ds_read_b64_tr_b16 v[222:223], v252 offset:9312\n"
"v_mfma_f32_16x16x32_bf16 v[144:147], v[208:211], v[40:43], v[144:147]\n"
"ds_write_b128 v254, v[204:207] offset:17408\n"
"v_mfma_f32_16x16x32_bf16 v[148:151], v[208:211], v[44:47], v[148:151]\n"
"s_waitcnt lgkmcnt(6)\n"
"v_mfma_f32_16x16x32_bf16 v[184:187], v[212:215], v[32:35], v[184:187]\n"
"v_mfma_f32_16x16x32_bf16 v[188:191], v[212:215], v[36:39], v[188:191]\n"
"v_mfma_f32_16x16x32_bf16 v[152:155], v[212:215], v[40:43], v[152:155]\n"
"s_waitcnt vmcnt(22) lgkmcnt(0)\n"
"s_barrier\n"
"ds_read_b64_tr_b16 v[208:209], v252 offset:33792\n"
"v_mfma_f32_16x16x32_bf16 v[156:159], v[212:215], v[44:47], v[156:159]\n"
"ds_read_b64_tr_b16 v[210:211], v252 offset:34048\n"
"v_mfma_f32_16x16x32_bf16 v[176:179], v[216:219], v[48:51], v[176:179]\n"
"ds_read_b64_tr_b16 v[212:213], v252 offset:33824\n"
"s_add_u32 s69, s69, 1\n"
"v_mfma_f32_16x16x32_bf16 v[180:183], v[216:219], v[52:55], v[180:183]\n"
"ds_read_b64_tr_b16 v[214:215], v252 offset:34080\n"
"s_max_i32 s15, s69, 0\n"
"v_mfma_f32_16x16x32_bf16 v[144:147], v[216:219], v[56:59], v[144:147]\n"
"v_or_b32 v228, s15, v230\n"
"v_mfma_f32_16x16x32_bf16 v[148:151], v[216:219], v[60:63], v[148:151]\n"
"ds_read_b64_tr_b16 v[216:217], v252 offset:34304\n"
"v_mfma_f32_16x16x32_bf16 v[184:187], v[220:223], v[48:51], v[184:187]\n"
"ds_read_b64_tr_b16 v[218:219], v252 offset:34560\n"
"v_mfma_f32_16x16x32_bf16 v[188:191], v[220:223], v[52:55], v[188:191]\n"
"s_mov_b64 exec, s[46:47]\n"
"global_store_dword v245, v228, s[74:75] sc1\n"
"s_mov_b64 exec, -1\n"
"v_mfma_f32_16x16x32_bf16 v[152:155], v[220:223], v[56:59], v[152:155]\n"
"v_mfma_f32_16x16x32_bf16 v[156:159], v[220:223], v[60:63], v[156:159]\n"
"ds_read_b64_tr_b16 v[220:221], v252 offset:34336\n"
"ds_read_b64_tr_b16 v[222:223], v252 offset:34592\n"
"s_waitcnt lgkmcnt(6)\n"
"v_mfma_f32_16x16x32_bf16 v[160:163], v[208:211], v[64:67], v[160:163]\n"
"s_add_u32 s56, s58, s59\n"
"s_add_u32 m0, s44, 67584\n"
"s_add_u32 s57, s56, 3584\n"
"v_mfma_f32_16x16x32_bf16 v[164:167], v[208:211], v[68:71], v[164:167]\n"
"s_add_u32 s66, s61, s60\n"
"s_add_u32 s60, s60, 2048\n"
"s_cmp_eq_u32 s60, 14336\n"
"s_cselect_b32 s60, 0, s60\n"
"ds_read_b128 v[192:195], v255 offset:16384\n"
"v_mfma_f32_16x16x32_bf16 v[128:131], v[208:211], v[72:75], v[128:131]\n"
"buffer_load_dwordx4 v250, s[24:27], s56 offen lds\n"
"ds_read_b128 v[196:199], v255 offset:17408\n"
"v_mfma_f32_16x16x32_bf16 v[132:135], v[208:211], v[76:79], v[132:135]\n"
"ds_read_b64_tr_b16 v[208:209], v252 offset:42240\n"
"s_waitcnt lgkmcnt(7)\n"
"v_mfma_f32_16x16x32_bf16 v[168:171], v[212:215], v[64:67], v[168:171]\n"
"v_mfma_f32_16x16x32_bf16 v[172:175], v[212:215], v[68:71], v[172:175]\n"
"ds_read_b64_tr_b16 v[210:211], v252 offset:42496\n"
"v_mfma_f32_16x16x32_bf16 v[136:139], v[212:215], v[72:75], v[136:139]\n"
"s_add_u32 m0, s44, 67744\n"
"v_mfma_f32_16x16x32_bf16 v[140:143], v[212:215], v[76:79], v[140:143]\n"
"ds_read_b64_tr_b16 v[212:213], v252 offset:42272\n"
"v_accvgpr_read_b32 v234, a16\n"
"s_waitcnt lgkmcnt(5)\n"
"v_mfma_f32_16x16x32_bf16 v[160:163], v[216:219], v[80:83], v[160:163]\n"
"buffer_load_dwordx4 v250, s[24:27], s56 offen offset:896 lds\n"
"v_accvgpr_read_b32 v235, a17\n"
"s_waitcnt lgkmcnt(4)\n"
"v_lshlrev_b32 v232, 16, v192\n"
"v_mfma_f32_16x16x32_bf16 v[164:167], v[216:219], v[84:87], v[164:167]\n"
"ds_read_b64_tr_b16 v[214:215], v252 offset:42528\n"
"v_and_b32 v233, s21, v192\n"
"v_fma_f32 v232, v232, v224, v234\n"
"v_mfma_f32_16x16x32_bf16 v[128:131], v[216:219], v[88:91], v[128:131]\n"
"v_fma_f32 v233, v233, v224, v235\n"
"v_cvt_pk_bf16_f32 v192, v232, v233\n"
"v_accvgpr_read_b32 v238, a18\n"
"v_mfma_f32_16x16x32_bf16 v[132:135], v[216:219], v[92:95], v[132:135]\n"
"ds_read_b64_tr_b16 v[216:217], v252 offset:42752\n"
"v_accvgpr_read_b32 v239, a19\n"
"v_mfma_f32_16x16x32_bf16 v[168:171], v[220:223], v[80:83], v[168:171]\n"
"s_add_u32 m0, s44, 67904\n"
"v_lshlrev_b32 v236, 16, v193\n"
"v_and_b32 v237, s21, v193\n"
"v_mfma_f32_16x16x32_bf16 v[172:175], v[220:223], v[84:87], v[172:175]\n"
"ds_read_b64_tr_b16 v[218:219], v252 offset:43008\n"
"v_fma_f32 v236, v236, v224, v238\n"
"v_fma_f32 v237, v237, v224, v239\n"
"v_mfma_f32_16x16x32_bf16 v[136:139], v[220:223], v[88:91], v[136:139]\n"
"buffer_load_dwordx4 v250, s[24:27], s56 offen offset:1792 lds\n"
"v_cvt_pk_bf16_f32 v193, v236, v237\n"
"v_accvgpr_read_b32 v234, a24\n"
"v_mfma_f32_16x16x32_bf16 v[140:143], v[220:223], v[92:95], v[140:143]\n"
"ds_read_b64_tr_b16 v[220:221], v252 offset:42784\n"
"v_accvgpr_read_b32 v235, a25\n"
"s_waitcnt lgkmcnt(3)\n"
"v_mfma_f32_16x16x32_bf16 v[160:163], v[208:211], v[96:99], v[160:163]\n"
"v_lshlrev_b32 v232, 16, v194\n"
"v_and_b32 v233, s21, v194\n"
"v_fma_f32 v232, v232, v224, v234\n"
"v_mfma_f32_16x16x32_bf16 v[164:167], v[208:211], v[100:103], v[164:167]\n"
"ds_read_b64_tr_b16 v[222:223], v252 offset:43040\n"
"v_fma_f32 v233, v233, v224, v235\n"
"v_cvt_pk_bf16_f32 v194, v232, v233\n"
"v_mfma_f32_16x16x32_bf16 v[128:131], v[208:211], v[104:107], v[128:131]\n"
"s_add_u32 m0, s44, 68064\n"
"v_accvgpr_read_b32 v238, a26\n"
"v_accvgpr_read_b32 v239, a27\n"
"v_mfma_f32_16x16x32_bf16 v[132:135], v[208:211], v[108:111], v[132:135]\n"
"ds_read_b64_tr_b16 v[208:209], v252 offset:33856\n"
"v_lshlrev_b32 v236, 16, v195\n"
"v_mfma_f32_16x16x32_bf16 v[168:171], v[212:215], v[96:99], v[168:171]\n"
"buffer_load_dwordx4 v250, s[24:27], s56 offen offset:2688 lds\n"
"v_and_b32 v237, s21, v195\n"
"v_fma_f32 v236, v236, v224, v238\n"
"v_mfma_f32_16x16x32_bf16 v[172:175], v[212:215], v[100:103], v[172:175]\n"
"ds_read_b64_tr_b16 v[210:211], v252 offset:34112\n"
"v_fma_f32 v237, v237, v224, v239\n"
"v_cvt_pk_bf16_f32 v195, v236, v237\n"
"v_mfma_f32_16x16x32_bf16 v[136:139], v[212:215], v[104:107], v[136:139]\n"
"s_add_u32 s65, s66, 14336\n"
"buffer_store_dwordx4 v[192:195], v255, s[28:31], s66 offen offset:1024 sc1\n"
"v_accvgpr_read_b32 v234, a20\n"
"v_mfma_f32_16x16x32_bf16 v[140:143], v[212:215], v[108:111], v[140:143]\n"
"ds_read_b64_tr_b16 v[212:213], v252 offset:33888\n"
"v_accvgpr_read_b32 v235, a21\n"
"s_waitcnt lgkmcnt(3)\n"
"v_mfma_f32_16x16x32_bf16 v[160:163], v[216:219], v[112:115], v[160:163]\n"
"s_add_u32 m0, s44, 71808\n"
"v_lshlrev_b32 v232, 16, v196\n"
"v_and_b32 v233, s21, v196\n"
"v_mfma_f32_16x16x32_bf16 v[164:167], v[216:219], v[116:119], v[164:167]\n"
"ds_read_b64_tr_b16 v[214:215], v252 offset:34144\n"
"v_fma_f32 v232, v232, v225, v234\n"
"v_fma_f32 v233, v233, v225, v235\n"
"v_mfma_f32_16x16x32_bf16 v[128:131], v[216:219], v[120:123], v[128:131]\n"
"buffer_load_dwordx4 v250, s[24:27], s57 offen lds\n"
"v_cvt_pk_bf16_f32 v196, v232, v233\n"
"v_accvgpr_read_b32 v238, a22\n"
"v_mfma_f32_16x16x32_bf16 v[132:135], v[216:219], v[124:127], v[132:135]\n"
"ds_read_b64_tr_b16 v[216:217], v252 offset:34368\n"
"v_accvgpr_read_b32 v239, a23\n"
"v_mfma_f32_16x16x32_bf16 v[168:171], v[220:223], v[112:115], v[168:171]\n"
"v_lshlrev_b32 v236, 16, v197\n"
"v_and_b32 v237, s21, v197\n"
"v_fma_f32 v236, v236, v225, v238\n"
"v_mfma_f32_16x16x32_bf16 v[172:175], v[220:223], v[116:119], v[172:175]\n"
"ds_read_b64_tr_b16 v[218:219], v252 offset:34624\n"
"v_fma_f32 v237, v237, v225, v239\n"
"v_cvt_pk_bf16_f32 v197, v236, v237\n"
"v_mfma_f32_16x16x32_bf16 v[136:139], v[220:223], v[120:123], v[136:139]\n"
"s_add_u32 m0, s44, 71968\n"
"v_accvgpr_read_b32 v234, a28\n"
"v_accvgpr_read_b32 v235, a29\n"
"v_mfma_f32_16x16x32_bf16 v[140:143], v[220:223], v[124:127], v[140:143]\n"
"ds_read_b64_tr_b16 v[220:221], v252 offset:34400\n"
"v_lshlrev_b32 v232, 16, v198\n"
"s_waitcnt lgkmcnt(3)\n"
"v_mfma_f32_16x16x32_bf16 v[176:179], v[208:211], v[64:67], v[176:179]\n"
"buffer_load_dwordx4 v250, s[24:27], s57 offen offset:896 lds\n"
"v_and_b32 v233, s21, v198\n"
"v_fma_f32 v232, v232, v225, v234\n"
"v_mfma_f32_16x16x32_bf16 v[180:183], v[208:211], v[68:71], v[180:183]\n"
"ds_read_b64_tr_b16 v[222:223], v252 offset:34656\n"
"v_fma_f32 v233, v233, v225, v235\n"
"v_cvt_pk_bf16_f32 v198, v232, v233\n"
"v_mfma_f32_16x16x32_bf16 v[144:147], v[208:211], v[72:75], v[144:147]\n"
"v_accvgpr_read_b32 v238, a30\n"
"v_accvgpr_read_b32 v239, a31\n"
"v_lshlrev_b32 v236, 16, v199\n"
"v_mfma_f32_16x16x32_bf16 v[148:151], v[208:211], v[76:79], v[148:151]\n"
"ds_read_b64_tr_b16 v[208:209], v252 offset:42304\n"
"v_and_b32 v237, s21, v199\n"
"v_mfma_f32_16x16x32_bf16 v[184:187], v[212:215], v[64:67], v[184:187]\n"
"s_add_u32 m0, s44, 72128\n"
"v_cvt_pk_bf16_f32 v200, v128, v129\n"
"v_fma_f32 v236, v236, v225, v238\n"
"v_mfma_f32_16x16x32_bf16 v[188:191], v[212:215], v[68:71], v[188:191]\n"
"ds_read_b64_tr_b16 v[210:211], v252 offset:42560\n"
"v_cvt_pk_bf16_f32 v201, v130, v131\n"
"v_fma_f32 v237, v237, v225, v239\n"
"v_mfma_f32_16x16x32_bf16 v[152:155], v[212:215], v[72:75], v[152:155]\n"
"buffer_load_dwordx4 v250, s[24:27], s57 offen offset:1792 lds\n"
"v_cvt_pk_bf16_f32 v202, v136, v137\n"
"v_cvt_pk_bf16_f32 v199, v236, v237\n"
"v_mfma_f32_16x16x32_bf16 v[156:159], v[212:215], v[76:79], v[156:159]\n"
"ds_read_b64_tr_b16 v[212:213], v252 offset:42336\n"
"buffer_store_dwordx4 v[196:199], v255, s[28:31], s65 offen offset:1024 sc1\n"
"s_waitcnt lgkmcnt(3)\n"
"v_mfma_f32_16x16x32_bf16 v[176:179], v[216:219], v[80:83], v[176:179]\n"
"v_cvt_pk_bf16_f32 v203, v138, v139\n"
"v_cvt_pk_bf16_f32 v204, v132, v133\n"
"v_mfma_f32_16x16x32_bf16 v[180:183], v[216:219], v[84:87], v[180:183]\n"
"ds_read_b64_tr_b16 v[214:215], v252 offset:42592\n"
"v_cvt_pk_bf16_f32 v205, v134, v135\n"
"v_mfma_f32_16x16x32_bf16 v[144:147], v[216:219], v[88:91], v[144:147]\n"
"s_add_u32 m0, s44, 72288\n"
"v_cvt_pk_bf16_f32 v206, v140, v141\n"
"v_mfma_f32_16x16x32_bf16 v[148:151], v[216:219], v[92:95], v[148:151]\n"
"ds_read_b64_tr_b16 v[216:217], v252 offset:42816\n"
"v_mfma_f32_16x16x32_bf16 v[184:187], v[220:223], v[80:83], v[184:187]\n"
"buffer_load_dwordx4 v250, s[24:27], s57 offen offset:2688 lds\n"
"v_cvt_pk_bf16_f32 v207, v142, v143\n"
"v_mfma_f32_16x16x32_bf16 v[188:191], v[220:223], v[84:87], v[188:191]\n"
"ds_read_b64_tr_b16 v[218:219], v252 offset:43072\n"
"v_mfma_f32_16x16x32_bf16 v[152:155], v[220:223], v[88:91], v[152:155]\n"
"v_mfma_f32_16x16x32_bf16 v[156:159], v[220:223], v[92:95], v[156:159]\n"
"ds_read_b64_tr_b16 v[220:221], v252 offset:42848\n"
"s_waitcnt lgkmcnt(5)\n"
"v_mfma_f32_16x16x32_bf16 v[176:179], v[208:211], v[96:99], v[176:179]\n"
"ds_write_b128 v254, v[200:203] offset:18432\n"
"v_mfma_f32_16x16x32_bf16 v[180:183], v[208:211], v[100:103], v[180:183]\n"
"ds_read_b64_tr_b16 v[222:223], v252 offset:43104\n"
"v_mfma_f32_16x16x32_bf16 v[144:147], v[208:211], v[104:107], v[144:147]\n"
"ds_write_b128 v254, v[204:207] offset:19456\n"
"v_mfma_f32_16x16x32_bf16 v[148:151], v[208:211], v[108:111], v[148:151]\n"
"s_waitcnt lgkmcnt(6)\n"
"v_mfma_f32_16x16x32_bf16 v[184:187], v[212:215], v[96:99], v[184:187]\n"
"v_mfma_f32_16x16x32_bf16 v[188:191], v[212:215], v[100:103], v[188:191]\n"
"v_mfma_f32_16x16x32_bf16 v[152:155], v[212:215], v[104:107], v[152:155]\n"
"s_waitcnt vmcnt(22) lgkmcnt(0)\n"
"s_barrier\n"
"ds_read_b64_tr_b16 v[208:209], v251 offset:0\n"
"v_mfma_f32_16x16x32_bf16 v[156:159], v[212:215], v[108:111], v[156:159]\n"
"ds_read_b64_tr_b16 v[210:211], v251 offset:256\n"
"v_mfma_f32_16x16x32_bf16 v[176:179], v[216:219], v[112:115], v[176:179]\n"
"ds_read_b64_tr_b16 v[212:213], v251 offset:32\n"
"v_mfma_f32_16x16x32_bf16 v[180:183], v[216:219], v[116:119], v[180:183]\n"
"ds_read_b64_tr_b16 v[214:215], v251 offset:288\n"
"v_mfma_f32_16x16x32_bf16 v[144:147], v[216:219], v[120:123], v[144:147]\n"
"v_mfma_f32_16x16x32_bf16 v[148:151], v[216:219], v[124:127], v[148:151]\n"
"ds_read_b64_tr_b16 v[216:217], v251 offset:512\n"
"v_mfma_f32_16x16x32_bf16 v[184:187], v[220:223], v[112:115], v[184:187]\n"
"ds_read_b64_tr_b16 v[218:219], v251 offset:768\n"
"v_mfma_f32_16x16x32_bf16 v[188:191], v[220:223], v[116:119], v[188:191]\n"
"v_mfma_f32_16x16x32_bf16 v[152:155], v[220:223], v[120:123], v[152:155]\n"
"v_mfma_f32_16x16x32_bf16 v[156:159], v[220:223], v[124:127], v[156:159]\n"
"ds_read_b64_tr_b16 v[220:221], v251 offset:544\n"
"ds_read_b64_tr_b16 v[222:223], v251 offset:800\n"
"s_sub_u32 s64, s64, 1\n"
"s_cmp_lg_u32 s64, 0\n"
"s_cbranch_scc1 LO_%=\n"
"s_waitcnt lgkmcnt(6)\n"
"v_mfma_f32_16x16x32_bf16 a[0:3], v[208:211], v[0:3], 0\n"
"s_add_u32 s56, s58, s59\n"
"s_add_u32 s56, s56, 114688\n"
"s_add_u32 m0, s44, 101376\n"
"s_add_u32 s57, s56, 3584\n"
"v_mfma_f32_16x16x32_bf16 a[4:7], v[208:211], v[4:7], 0\n"
"s_add_u32 s66, s61, s60\n"
"ds_read_b128 v[192:195], v255 offset:18432\n"
"v_mfma_f32_16x16x32_bf16 v[128:131], v[208:211], v[8:11], 0\n"
"buffer_load_dwordx4 v250, s[24:27], s56 offen lds\n"
"ds_read_b128 v[196:199], v255 offset:19456\n"
"v_mfma_f32_16x16x32_bf16 v[132:135], v[208:211], v[12:15], 0\n"
"ds_read_b64_tr_b16 v[208:209], v251 offset:8448\n"
"s_waitcnt lgkmcnt(7)\n"
"v_mfma_f32_16x16x32_bf16 a[8:11], v[212:215], v[0:3], 0\n"
"v_cvt_pk_bf16_f32 v200, v144, v145\n"
"v_cvt_pk_bf16_f32 v201, v146, v147\n"
"v_mfma_f32_16x16x32_bf16 a[12:15], v[212:215], v[4:7], 0\n"
"ds_read_b64_tr_b16 v[210:211], v251 offset:8704\n"
"v_cvt_pk_bf16_f32 v202, v152, v153\n"
"v_mfma_f32_16x16x32_bf16 v[136:139], v[212:215], v[8:11], 0\n"
"s_add_u32 m0, s44, 101536\n"
"v_cvt_pk_bf16_f32 v203, v154, v155\n"
"v_mfma_f32_16x16x32_bf16 v[140:143], v[212:215], v[12:15], 0\n"
"ds_read_b64_tr_b16 v[212:213], v251 offset:8480\n"
"s_waitcnt lgkmcnt(5)\n"
"v_mfma_f32_16x16x32_bf16 a[0:3], v[216:219], v[16:19], a[0:3]\n"
"buffer_load_dwordx4 v250, s[24:27], s56 offen offset:896 lds\n"
"v_cvt_pk_bf16_f32 v204, v148, v149\n"
"v_mfma_f32_16x16x32_bf16 a[4:7], v[216:219], v[20:23], a[4:7]\n"
"ds_read_b64_tr_b16 v[214:215], v251 offset:8736\n"
"v_cvt_pk_bf16_f32 v205, v150, v151\n"
"v_mfma_f32_16x16x32_bf16 v[128:131], v[216:219], v[24:27], v[128:131]\n"
"v_cvt_pk_bf16_f32 v206, v156, v157\n"
"v_cvt_pk_bf16_f32 v207, v158, v159\n"
"v_mfma_f32_16x16x32_bf16 v[132:135], v[216:219], v[28:31], v[132:135]\n"
"ds_read_b64_tr_b16 v[216:217], v251 offset:8960\n"
"v_mfma_f32_16x16x32_bf16 a[8:11], v[220:223], v[16:19], a[8:11]\n"
"s_add_u32 m0, s44, 101696\n"
"s_waitcnt lgkmcnt(6)\n"
"v_lshlrev_b32 v232, 16, v192\n"
"v_mfma_f32_16x16x32_bf16 a[12:15], v[220:223], v[20:23], a[12:15]\n"
"ds_read_b64_tr_b16 v[218:219], v251 offset:9216\n"
"v_and_b32 v233, s21, v192\n"
"v_mfma_f32_16x16x32_bf16 v[136:139], v[220:223], v[24:27], v[136:139]\n"
"buffer_load_dwordx4 v250, s[24:27], s56 offen offset:1792 lds\n"
"v_fma_f32 v232, v232, v224, v160\n"
"v_mfma_f32_16x16x32_bf16 v[140:143], v[220:223], v[28:31], v[140:143]\n"
"ds_read_b64_tr_b16 v[220:221], v251 offset:8992\n"
"s_waitcnt lgkmcnt(3)\n"
"v_mfma_f32_16x16x32_bf16 a[0:3], v[208:211], v[32:35], a[0:3]\n"
"v_fma_f32 v233, v233, v224, v161\n"
"v_cvt_pk_bf16_f32 v192, v232, v233\n"
"v_mfma_f32_16x16x32_bf16 a[4:7], v[208:211], v[36:39], a[4:7]\n"
"ds_read_b64_tr_b16 v[222:223], v251 offset:9248\n"
"v_lshlrev_b32 v236, 16, v193\n"
"v_mfma_f32_16x16x32_bf16 v[128:131], v[208:211], v[40:43], v[128:131]\n"
"s_add_u32 m0, s44, 101856\n"
"v_and_b32 v237, s21, v193\n"
"v_mfma_f32_16x16x32_bf16 v[132:135], v[208:211], v[44:47], v[132:135]\n"
"ds_read_b64_tr_b16 v[208:209], v251 offset:64\n"
"v_mfma_f32_16x16x32_bf16 a[8:11], v[212:215], v[32:35], a[8:11]\n"
"buffer_load_dwordx4 v250, s[24:27], s56 offen offset:2688 lds\n"
"v_fma_f32 v236, v236, v224, v162\n"
"v_mfma_f32_16x16x32_bf16 a[12:15], v[212:215], v[36:39], a[12:15]\n"
"ds_read_b64_tr_b16 v[210:211], v251 offset:320\n"
"v_fma_f32 v237, v237, v224, v163\n"
"v_mfma_f32_16x16x32_bf16 v[136:139], v[212:215], v[40:43], v[136:139]\n"
"v_cvt_pk_bf16_f32 v193, v236, v237\n"
"v_lshlrev_b32 v232, 16, v194\n"
"v_mfma_f32_16x16x32_bf16 v[140:143], v[212:215], v[44:47], v[140:143]\n"
"ds_read_b64_tr_b16 v[212:213], v251 offset:96\n"
"s_waitcnt lgkmcnt(3)\n"
"v_mfma_f32_16x16x32_bf16 a[0:3], v[216:219], v[48:51], a[0:3]\n"
"s_add_u32 m0, s44, 105600\n"
"v_and_b32 v233, s21, v194\n"
"v_mfma_f32_16x16x32_bf16 a[4:7], v[216:219], v[52:55], a[4:7]\n"
"ds_read_b64_tr_b16 v[214:215], v251 offset:352\n"
"v_fma_f32 v232, v232, v224, v168\n"
"v_mfma_f32_16x16x32_bf16 v[128:131], v[216:219], v[56:59], v[128:131]\n"
"buffer_load_dwordx4 v250, s[24:27], s57 offen lds\n"
"v_fma_f32 v233, v233, v224, v169\n"
"v_mfma_f32_16x16x32_bf16 v[132:135], v[216:219], v[60:63], v[132:135]\n"
"ds_read_b64_tr_b16 v[216:217], v251 offset:576\n"
"v_mfma_f32_16x16x32_bf16 a[8:11], v[220:223], v[48:51], a[8:11]\n"
"v_cvt_pk_bf16_f32 v194, v232, v233\n"
"v_lshlrev_b32 v236, 16, v195\n"
"v_mfma_f32_16x16x32_bf16 a[12:15], v[220:223], v[52:55], a[12:15]\n"
"ds_read_b64_tr_b16 v[218:219], v251 offset:832\n"
"v_and_b32 v237, s21, v195\n"
"v_mfma_f32_16x16x32_bf16 v[136:139], v[220:223], v[56:59], v[136:139]\n"
"s_add_u32 m0, s44, 105760\n"
"v_fma_f32 v236, v236, v224, v170\n"
"v_mfma_f32_16x16x32_bf16 v[140:143], v[220:223], v[60:63], v[140:143]\n"
"ds_read_b64_tr_b16 v[220:221], v251 offset:608\n"
"s_waitcnt lgkmcnt(3)\n"
"v_mfma_f32_16x16x32_bf16 a[16:19], v[208:211], v[0:3], 0\n"
"buffer_load_dwordx4 v250, s[24:27], s57 offen offset:896 lds\n"
"v_fma_f32 v237, v237, v224, v171\n"
"v_mfma_f32_16x16x32_bf16 a[20:23], v[208:211], v[4:7], 0\n"
"ds_read_b64_tr_b16 v[222:223], v251 offset:864\n"
"v_cvt_pk_bf16_f32 v195, v236, v237\n"
"v_mfma_f32_16x16x32_bf16 v[144:147], v[208:211], v[8:11], 0\n"
"s_add_u32 s65, s66, 14336\n"
"buffer_store_dwordx4 v[192:195], v255, s[28:31], s66 offen offset:0 sc1\n"
"v_mfma_f32_16x16x32_bf16 v[148:151], v[208:211], v[12:15], 0\n"
"ds_read_b64_tr_b16 v[208:209], v251 offset:8512\n"
"v_mfma_f32_16x16x32_bf16 a[24:27], v[212:215], v[0:3], 0\n"
"s_add_u32 m0, s44, 105920\n"
"v_lshlrev_b32 v232, 16, v196\n"
"v_mfma_f32_16x16x32_bf16 a[28:31], v[212:215], v[4:7], 0\n"
"ds_read_b64_tr_b16 v[210:211], v251 offset:8768\n"
"v_and_b32 v233, s21, v196\n"
"v_mfma_f32_16x16x32_bf16 v[152:155], v[212:215], v[8:11], 0\n"
"buffer_load_dwordx4 v250, s[24:27], s57 offen offset:1792 lds\n"
"v_fma_f32 v232, v232, v225, v164\n"
"v_mfma_f32_16x16x32_bf16 v[156:159], v[212:215], v[12:15], 0\n"
"ds_read_b64_tr_b16 v[212:213], v251 offset:8544\n"
"s_waitcnt lgkmcnt(3)\n"
"v_mfma_f32_16x16x32_bf16 a[16:19], v[216:219], v[16:19], a[16:19]\n"
"v_fma_f32 v233, v233, v225, v165\n"
"v_cvt_pk_bf16_f32 v196, v232, v233\n"
"v_mfma_f32_16x16x32_bf16 a[20:23], v[216:219], v[20:23], a[20:23]\n"
"ds_read_b64_tr_b16 v[214:215], v251 offset:8800\n"
"v_lshlrev_b32 v236, 16, v197\n"
"v_mfma_f32_16x16x32_bf16 v[144:147], v[216:219], v[24:27], v[144:147]\n"
"s_add_u32 m0, s44, 106080\n"
"v_and_b32 v237, s21, v197\n"
"v_mfma_f32_16x16x32_bf16 v[148:151], v[216:219], v[28:31], v[148:151]\n"
"ds_read_b64_tr_b16 v[216:217], v251 offset:9024\n"
"v_mfma_f32_16x16x32_bf16 a[24:27], v[220:223], v[16:19], a[24:27]\n"
"buffer_load_dwordx4 v250, s[24:27], s57 offen offset:2688 lds\n"
"v_fma_f32 v236, v236, v225, v166\n"
"v_mfma_f32_16x16x32_bf16 a[28:31], v[220:223], v[20:23], a[28:31]\n"
"ds_read_b64_tr_b16 v[218:219], v251 offset:9280\n"
"s_add_u32 s59, s59, 128\n"
"s_cmp_eq_u32 s59, 896\n"
"s_cselect_b32 s59, 0, s59\n"
"v_mfma_f32_16x16x32_bf16 v[152:155], v[220:223], v[24:27], v[152:155]\n"
"v_fma_f32 v237, v237, v225, v167\n"
"v_cvt_pk_bf16_f32 v197, v236, v237\n"
"v_mfma_f32_16x16x32_bf16 v[156:159], v[220:223], v[28:31], v[156:159]\n"
"ds_read_b64_tr_b16 v[220:221], v251 offset:9056\n"
"s_waitcnt lgkmcnt(5)\n"
"v_mfma_f32_16x16x32_bf16 a[16:19], v[208:211], v[32:35], a[16:19]\n"
"ds_write_b128 v254, v[200:203] offset:16384\n"
"v_lshlrev_b32 v232, 16, v198\n"
"v_mfma_f32_16x16x32_bf16 a[20:23], v[208:211], v[36:39], a[20:23]\n"
"ds_read_b64_tr_b16 v[222:223], v251 offset:9312\n"
"v_and_b32 v233, s21, v198\n"
"v_mfma_f32_16x16x32_bf16 v[144:147], v[208:211], v[40:43], v[144:147]\n"
"ds_write_b128 v254, v[204:207] offset:17408\n"
"v_fma_f32 v232, v232, v225, v172\n"
"v_mfma_f32_16x16x32_bf16 v[148:151], v[208:211], v[44:47], v[148:151]\n"
"v_fma_f32 v233, v233, v225, v173\n"
"s_waitcnt lgkmcnt(6)\n"
"v_mfma_f32_16x16x32_bf16 a[24:27], v[212:215], v[32:35], a[24:27]\n"
"v_cvt_pk_bf16_f32 v198, v232, v233\n"
"v_lshlrev_b32 v236, 16, v199\n"
"v_mfma_f32_16x16x32_bf16 a[28:31], v[212:215], v[36:39], a[28:31]\n"
"v_and_b32 v237, s21, v199\n"
"v_fma_f32 v236, v236, v225, v174\n"
"v_mfma_f32_16x16x32_bf16 v[152:155], v[212:215], v[40:43], v[152:155]\n"
"s_waitcnt vmcnt(20) lgkmcnt(0)\n"
"s_barrier\n"
"ds_read_b64_tr_b16 v[208:209], v251 offset:33792\n"
"v_mfma_f32_16x16x32_bf16 v[156:159], v[212:215], v[44:47], v[156:159]\n"
"ds_read_b64_tr_b16 v[210:211], v251 offset:34048\n"
"v_mfma_f32_16x16x32_bf16 a[16:19], v[216:219], v[48:51], a[16:19]\n"
"ds_read_b64_tr_b16 v[212:213], v251 offset:33824\n"
"v_fma_f32 v237, v237, v225, v175\n"
"v_mfma_f32_16x16x32_bf16 a[20:23], v[216:219], v[52:55], a[20:23]\n"
"ds_read_b64_tr_b16 v[214:215], v251 offset:34080\n"
"v_cvt_pk_bf16_f32 v199, v236, v237\n"
"v_mfma_f32_16x16x32_bf16 v[144:147], v[216:219], v[56:59], v[144:147]\n"
"buffer_store_dwordx4 v[196:199], v255, s[28:31], s65 offen offset:0 sc1\n"
"s_add_u32 s69, s69, 1\n"
"v_mfma_f32_16x16x32_bf16 v[148:151], v[216:219], v[60:63], v[148:151]\n"
"ds_read_b64_tr_b16 v[216:217], v251 offset:34304\n"
"v_mfma_f32_16x16x32_bf16 a[24:27], v[220:223], v[48:51], a[24:27]\n"
"ds_read_b64_tr_b16 v[218:219], v251 offset:34560\n"
"s_max_i32 s15, s69, 0\n"
"v_mfma_f32_16x16x32_bf16 a[28:31], v[220:223], v[52:55], a[28:31]\n"
"v_or_b32 v228, s15, v230\n"
"v_mfma_f32_16x16x32_bf16 v[152:155], v[220:223], v[56:59], v[152:155]\n"
"s_mov_b64 exec, s[46:47]\n"
"global_store_dword v245, v228, s[74:75] sc1\n"
"s_mov_b64 exec, -1\n"
"v_mfma_f32_16x16x32_bf16 v[156:159], v[220:223], v[60:63], v[156:159]\n"
"ds_read_b64_tr_b16 v[220:221], v251 offset:34336\n"
"ds_read_b64_tr_b16 v[222:223], v251 offset:34592\n"
"s_waitcnt lgkmcnt(6)\n"
"v_mfma_f32_16x16x32_bf16 a[0:3], v[208:211], v[64:67], a[0:3]\n"
"s_add_u32 s66, s61, s60\n"
"v_mfma_f32_16x16x32_bf16 a[4:7], v[208:211], v[68:71], a[4:7]\n"
"s_add_u32 s60, s60, 2048\n"
"s_cmp_eq_u32 s60, 14336\n"
"s_cselect_b32 s60, 0, s60\n"
"ds_read_b128 v[192:195], v255 offset:16384\n"
"v_mfma_f32_16x16x32_bf16 v[128:131], v[208:211], v[72:75], v[128:131]\n"
"ds_read_b128 v[196:199], v255 offset:17408\n"
"v_mfma_f32_16x16x32_bf16 v[132:135], v[208:211], v[76:79], v[132:135]\n"
"ds_read_b64_tr_b16 v[208:209], v251 offset:42240\n"
"s_waitcnt lgkmcnt(7)\n"
"v_mfma_f32_16x16x32_bf16 a[8:11], v[212:215], v[64:67], a[8:11]\n"
"v_mfma_f32_16x16x32_bf16 a[12:15], v[212:215], v[68:71], a[12:15]\n"
"ds_read_b64_tr_b16 v[210:211], v251 offset:42496\n"
"v_mfma_f32_16x16x32_bf16 v[136:139], v[212:215], v[72:75], v[136:139]\n"
"v_mfma_f32_16x16x32_bf16 v[140:143], v[212:215], v[76:79], v[140:143]\n"
"ds_read_b64_tr_b16 v[212:213], v251 offset:42272\n"
"s_waitcnt lgkmcnt(5)\n"
"v_mfma_f32_16x16x32_bf16 a[0:3], v[216:219], v[80:83], a[0:3]\n"
"s_waitcnt lgkmcnt(4)\n"
"v_lshlrev_b32 v232, 16, v192\n"
"v_and_b32 v233, s21, v192\n"
"v_mfma_f32_16x16x32_bf16 a[4:7], v[216:219], v[84:87], a[4:7]\n"
"ds_read_b64_tr_b16 v[214:215], v251 offset:42528\n"
"v_fma_f32 v232, v232, v224, v176\n"
"v_mfma_f32_16x16x32_bf16 v[128:131], v[216:219], v[88:91], v[128:131]\n"
"v_fma_f32 v233, v233, v224, v177\n"
"v_cvt_pk_bf16_f32 v192, v232, v233\n"
"v_mfma_f32_16x16x32_bf16 v[132:135], v[216:219], v[92:95], v[132:135]\n"
"ds_read_b64_tr_b16 v[216:217], v251 offset:42752\n"
"v_mfma_f32_16x16x32_bf16 a[8:11], v[220:223], v[80:83], a[8:11]\n"
"v_lshlrev_b32 v236, 16, v193\n"
"v_and_b32 v237, s21, v193\n"
"v_mfma_f32_16x16x32_bf16 a[12:15], v[220:223], v[84:87], a[12:15]\n"
"ds_read_b64_tr_b16 v[218:219], v251 offset:43008\n"
"v_fma_f32 v236, v236, v224, v178\n"
"v_mfma_f32_16x16x32_bf16 v[136:139], v[220:223], v[88:91], v[136:139]\n"
"v_fma_f32 v237, v237, v224, v179\n"
"v_cvt_pk_bf16_f32 v193, v236, v237\n"
"v_mfma_f32_16x16x32_bf16 v[140:143], v[220:223], v[92:95], v[140:143]\n"
"ds_read_b64_tr_b16 v[220:221], v251 offset:42784\n"
"s_waitcnt lgkmcnt(3)\n"
"v_mfma_f32_16x16x32_bf16 a[0:3], v[208:211], v[96:99], a[0:3]\n"
"v_lshlrev_b32 v232, 16, v194\n"
"v_and_b32 v233, s21, v194\n"
"v_mfma_f32_16x16x32_bf16 a[4:7], v[208:211], v[100:103], a[4:7]\n"
"ds_read_b64_tr_b16 v[222:223], v251 offset:43040\n"
"v_fma_f32 v232, v232, v224, v184\n"
"v_mfma_f32_16x16x32_bf16 v[128:131], v[208:211], v[104:107], v[128:131]\n"
"v_fma_f32 v233, v233, v224, v185\n"
"v_cvt_pk_bf16_f32 v194, v232, v233\n"
"v_mfma_f32_16x16x32_bf16 v[132:135], v[208:211], v[108:111], v[132:135]\n"
"ds_read_b64_tr_b16 v[208:209], v251 offset:33856\n"
"v_mfma_f32_16x16x32_bf16 a[8:11], v[212:215], v[96:99], a[8:11]\n"
"v_lshlrev_b32 v236, 16, v195\n"
"v_and_b32 v237, s21, v195\n"
"v_mfma_f32_16x16x32_bf16 a[12:15], v[212:215], v[100:103], a[12:15]\n"
"ds_read_b64_tr_b16 v[210:211], v251 offset:34112\n"
"v_fma_f32 v236, v236, v224, v186\n"
"v_mfma_f32_16x16x32_bf16 v[136:139], v[212:215], v[104:107], v[136:139]\n"
"v_fma_f32 v237, v237, v224, v187\n"
"v_cvt_pk_bf16_f32 v195, v236, v237\n"
"v_mfma_f32_16x16x32_bf16 v[140:143], v[212:215], v[108:111], v[140:143]\n"
"ds_read_b64_tr_b16 v[212:213], v251 offset:33888\n"
"s_waitcnt lgkmcnt(3)\n"
"v_mfma_f32_16x16x32_bf16 a[0:3], v[216:219], v[112:115], a[0:3]\n"
"s_add_u32 s65, s66, 14336\n"
"buffer_store_dwordx4 v[192:195], v255, s[28:31], s66 offen offset:1024 sc1\n"
"v_mfma_f32_16x16x32_bf16 a[4:7], v[216:219], v[116:119], a[4:7]\n"
"ds_read_b64_tr_b16 v[214:215], v251 offset:34144\n"
"v_lshlrev_b32 v232, 16, v196\n"
"v_mfma_f32_16x16x32_bf16 v[128:131], v[216:219], v[120:123], v[128:131]\n"
"v_and_b32 v233, s21, v196\n"
"v_fma_f32 v232, v232, v225, v180\n"
"v_mfma_f32_16x16x32_bf16 v[132:135], v[216:219], v[124:127], v[132:135]\n"
"ds_read_b64_tr_b16 v[216:217], v251 offset:34368\n"
"v_mfma_f32_16x16x32_bf16 a[8:11], v[220:223], v[112:115], a[8:11]\n"
"v_fma_f32 v233, v233, v225, v181\n"
"v_cvt_pk_bf16_f32 v196, v232, v233\n"
"v_mfma_f32_16x16x32_bf16 a[12:15], v[220:223], v[116:119], a[12:15]\n"
"ds_read_b64_tr_b16 v[218:219], v251 offset:34624\n"
"v_lshlrev_b32 v236, 16, v197\n"
"v_mfma_f32_16x16x32_bf16 v[136:139], v[220:223], v[120:123], v[136:139]\n"
"v_and_b32 v237, s21, v197\n"
"v_fma_f32 v236, v236, v225, v182\n"
"global_load_dword v231, v244, s[74:75] sc1\n"
"v_mfma_f32_16x16x32_bf16 v[140:143], v[220:223], v[124:127], v[140:143]\n"
"ds_read_b64_tr_b16 v[220:221], v251 offset:34400\n"
"s_add_u32 s20, s71, 2\n"
"s_waitcnt lgkmcnt(3)\n"
"v_mfma_f32_16x16x32_bf16 a[16:19], v[208:211], v[64:67], a[16:19]\n"
"v_fma_f32 v237, v237, v225, v183\n"
"v_cvt_pk_bf16_f32 v197, v236, v237\n"
"s_sub_u32 s70, 7, 2\n"
"v_mfma_f32_16x16x32_bf16 a[20:23], v[208:211], v[68:71], a[20:23]\n"
"ds_read_b64_tr_b16 v[222:223], v251 offset:34656\n"
"v_lshlrev_b32 v232, 16, v198\n"
"v_mfma_f32_16x16x32_bf16 v[144:147], v[208:211], v[72:75], v[144:147]\n"
"v_and_b32 v233, s21, v198\n"
"v_fma_f32 v232, v232, v225, v188\n"
"v_mfma_f32_16x16x32_bf16 v[148:151], v[208:211], v[76:79], v[148:151]\n"
"ds_read_b64_tr_b16 v[208:209], v251 offset:42304\n"
"v_mfma_f32_16x16x32_bf16 a[24:27], v[212:215], v[64:67], a[24:27]\n"
"v_cvt_pk_bf16_f32 v200, v128, v129\n"
"v_cvt_pk_bf16_f32 v201, v130, v131\n"
"v_mfma_f32_16x16x32_bf16 a[28:31], v[212:215], v[68:71], a[28:31]\n"
"ds_read_b64_tr_b16 v[210:211], v251 offset:42560\n"
"v_cvt_pk_bf16_f32 v202, v136, v137\n"
"v_mfma_f32_16x16x32_bf16 v[152:155], v[212:215], v[72:75], v[152:155]\n"
"v_cvt_pk_bf16_f32 v203, v138, v139\n"
"v_cvt_pk_bf16_f32 v204, v132, v133\n"
"v_mfma_f32_16x16x32_bf16 v[156:159], v[212:215], v[76:79], v[156:159]\n"
"ds_read_b64_tr_b16 v[212:213], v251 offset:42336\n"
"s_waitcnt lgkmcnt(3)\n"
"v_mfma_f32_16x16x32_bf16 a[16:19], v[216:219], v[80:83], a[16:19]\n"
"v_cvt_pk_bf16_f32 v205, v134, v135\n"
"v_cvt_pk_bf16_f32 v206, v140, v141\n"
"v_mfma_f32_16x16x32_bf16 a[20:23], v[216:219], v[84:87], a[20:23]\n"
"ds_read_b64_tr_b16 v[214:215], v251 offset:42592\n"
"v_cvt_pk_bf16_f32 v207, v142, v143\n"
"v_mfma_f32_16x16x32_bf16 v[144:147], v[216:219], v[88:91], v[144:147]\n"
"v_fma_f32 v233, v233, v225, v189\n"
"v_cvt_pk_bf16_f32 v198, v232, v233\n"
"v_mfma_f32_16x16x32_bf16 v[148:151], v[216:219], v[92:95], v[148:151]\n"
"ds_read_b64_tr_b16 v[216:217], v251 offset:42816\n"
"v_mfma_f32_16x16x32_bf16 a[24:27], v[220:223], v[80:83], a[24:27]\n"
"v_lshlrev_b32 v236, 16, v199\n"
"v_and_b32 v237, s21, v199\n"
"v_mfma_f32_16x16x32_bf16 a[28:31], v[220:223], v[84:87], a[28:31]\n"
"ds_read_b64_tr_b16 v[218:219], v251 offset:43072\n"
"v_fma_f32 v236, v236, v225, v190\n"
"v_mfma_f32_16x16x32_bf16 v[152:155], v[220:223], v[88:91], v[152:155]\n"
"v_fma_f32 v237, v237, v225, v191\n"
"v_cvt_pk_bf16_f32 v199, v236, v237\n"
"v_mfma_f32_16x16x32_bf16 v[156:159], v[220:223], v[92:95], v[156:159]\n"
"ds_read_b64_tr_b16 v[220:221], v251 offset:42848\n"
"s_waitcnt lgkmcnt(5)\n"
"v_mfma_f32_16x16x32_bf16 a[16:19], v[208:211], v[96:99], a[16:19]\n"
"ds_write_b128 v254, v[200:203] offset:18432\n"
"buffer_store_dwordx4 v[196:199], v255, s[28:31], s65 offen offset:1024 sc1\n"
"v_mfma_f32_16x16x32_bf16 a[20:23], v[208:211], v[100:103], a[20:23]\n"
"ds_read_b64_tr_b16 v[222:223], v251 offset:43104\n"
"v_mfma_f32_16x16x32_bf16 v[144:147], v[208:211], v[104:107], v[144:147]\n"
"ds_write_b128 v254, v[204:207] offset:19456\n"
"v_mfma_f32_16x16x32_bf16 v[148:151], v[208:211], v[108:111], v[148:151]\n"
"s_waitcnt lgkmcnt(6)\n"
"v_mfma_f32_16x16x32_bf16 a[24:27], v[212:215], v[96:99], a[24:27]\n"
"v_mfma_f32_16x16x32_bf16 a[28:31], v[212:215], v[100:103], a[28:31]\n"
"v_mfma_f32_16x16x32_bf16 v[152:155], v[212:215], v[104:107], v[152:155]\n"
"s_waitcnt vmcnt(14) lgkmcnt(0)\n"
"s_barrier\n"
"ds_read_b64_tr_b16 v[208:209], v252 offset:0\n"
"v_mfma_f32_16x16x32_bf16 v[156:159], v[212:215], v[108:111], v[156:159]\n"
"ds_read_b64_tr_b16 v[210:211], v252 offset:256\n"
"v_mfma_f32_16x16x32_bf16 a[16:19], v[216:219], v[112:115], a[16:19]\n"
"ds_read_b64_tr_b16 v[212:213], v252 offset:32\n"
"v_mfma_f32_16x16x32_bf16 a[20:23], v[216:219], v[116:119], a[20:23]\n"
"ds_read_b64_tr_b16 v[214:215], v252 offset:288\n"
"v_mfma_f32_16x16x32_bf16 v[144:147], v[216:219], v[120:123], v[144:147]\n"
"v_mfma_f32_16x16x32_bf16 v[148:151], v[216:219], v[124:127], v[148:151]\n"
"ds_read_b64_tr_b16 v[216:217], v252 offset:512\n"
"v_mfma_f32_16x16x32_bf16 a[24:27], v[220:223], v[112:115], a[24:27]\n"
"ds_read_b64_tr_b16 v[218:219], v252 offset:768\n"
"v_mfma_f32_16x16x32_bf16 a[28:31], v[220:223], v[116:119], a[28:31]\n"
"v_mfma_f32_16x16x32_bf16 v[152:155], v[220:223], v[120:123], v[152:155]\n"
"v_mfma_f32_16x16x32_bf16 v[156:159], v[220:223], v[124:127], v[156:159]\n"
"ds_read_b64_tr_b16 v[220:221], v252 offset:544\n"
"ds_read_b64_tr_b16 v[222:223], v252 offset:800\n"
"s_waitcnt lgkmcnt(6)\n"
"v_mfma_f32_16x16x32_bf16 v[160:163], v[208:211], v[0:3], 0\n"
"s_add_u32 s66, s61, s60\n"
"v_mfma_f32_16x16x32_bf16 v[164:167], v[208:211], v[4:7], 0\n"
"ds_read_b128 v[192:195], v255 offset:18432\n"
"s_waitcnt vmcnt(1)\n"
"v_subrev_u32 v229, s20, v231\n"
"v_cmp_lt_u32 vcc, s70, v229\n"
"s_nop 1\n"
"s_cbranch_vccz LR_25_%=\n"
"s_getpc_b64 s[72:73]\n"
"s_branch LSP_%=\n"
"LR_25_%=:\n"
"v_mfma_f32_16x16x32_bf16 v[128:131], v[208:211], v[8:11], 0\n"
"ds_read_b128 v[196:199], v255 offset:19456\n"
"v_mfma_f32_16x16x32_bf16 v[132:135], v[208:211], v[12:15], 0\n"
"ds_read_b64_tr_b16 v[208:209], v252 offset:8448\n"
"s_add_u32 s22, s8, s9\n"
"buffer_load_dwordx2 a[64:65], v248, s[28:31], s22 offen sc1\n"
"s_waitcnt lgkmcnt(7)\n"
"v_mfma_f32_16x16x32_bf16 v[168:171], v[212:215], v[0:3], 0\n"
"v_cvt_pk_bf16_f32 v200, v144, v145\n"
"s_add_u32 s22, s22, 114688\n"
"buffer_load_dwordx2 a[66:67], v248, s[28:31], s22 offen sc1\n"
"v_mfma_f32_16x16x32_bf16 v[172:175], v[212:215], v[4:7], 0\n"
"ds_read_b64_tr_b16 v[210:211], v252 offset:8704\n"
"s_add_u32 s22, s22, 114688\n"
"buffer_load_dwordx2 a[68:69], v248, s[28:31], s22 offen sc1\n"
"v_mfma_f32_16x16x32_bf16 v[136:139], v[212:215], v[8:11], 0\n"
"v_cvt_pk_bf16_f32 v201, v146, v147\n"
"s_add_u32 s22, s22, 114688\n"
"buffer_load_dwordx2 a[70:71], v248, s[28:31], s22 offen sc1\n"
"v_mfma_f32_16x16x32_bf16 v[140:143], v[212:215], v[12:15], 0\n"
"ds_read_b64_tr_b16 v[212:213], v252 offset:8480\n"
"s_add_u32 s22, s22, 114688\n"
"buffer_load_dwordx2 a[72:73], v248, s[28:31], s22 offen sc1\n"
"s_waitcnt lgkmcnt(5)\n"
"v_mfma_f32_16x16x32_bf16 v[160:163], v[216:219], v[16:19], v[160:163]\n"
"v_cvt_pk_bf16_f32 v202, v152, v153\n"
"v_accvgpr_read_b32 v234, a0\n"
"s_add_u32 s22, s22, 114688\n"
"buffer_load_dwordx2 a[74:75], v248, s[28:31], s22 offen sc1\n"
"v_mfma_f32_16x16x32_bf16 v[164:167], v[216:219], v[20:23], v[164:167]\n"
"ds_read_b64_tr_b16 v[214:215], v252 offset:8736\n"
"v_accvgpr_read_b32 v235, a1\n"
"s_add_u32 s22, s22, 114688\n"
"buffer_load_dwordx2 a[76:77], v248, s[28:31], s22 offen sc1\n"
"v_mfma_f32_16x16x32_bf16 v[128:131], v[216:219], v[24:27], v[128:131]\n"
"v_cvt_pk_bf16_f32 v203, v154, v155\n"
"s_waitcnt lgkmcnt(5)\n"
"v_lshlrev_b32 v232, 16, v192\n"
"s_add_u32 s22, s22, 114688\n"
"buffer_load_dwordx2 a[78:79], v248, s[28:31], s22 offen sc1\n"
"v_mfma_f32_16x16x32_bf16 v[132:135], v[216:219], v[28:31], v[132:135]\n"
"ds_read_b64_tr_b16 v[216:217], v252 offset:8960\n"
"s_add_u32 s9, s9, 2048\n"
"s_cmp_eq_u32 s9, 14336\n"
"s_cselect_b32 s9, 0, s9\n"
"v_mfma_f32_16x16x32_bf16 v[168:171], v[220:223], v[16:19], v[168:171]\n"
"v_cvt_pk_bf16_f32 v204, v148, v149\n"
"v_and_b32 v233, s21, v192\n"
"s_add_u32 s22, s8, s9\n"
"buffer_load_dwordx2 a[80:81], v248, s[28:31], s22 offen sc1\n"
"v_mfma_f32_16x16x32_bf16 v[172:175], v[220:223], v[20:23], v[172:175]\n"
"ds_read_b64_tr_b16 v[218:219], v252 offset:9216\n"
"v_fma_f32 v232, v232, v224, v234\n"
"s_add_u32 s22, s22, 114688\n"
"buffer_load_dwordx2 a[82:83], v248, s[28:31], s22 offen sc1\n"
"v_mfma_f32_16x16x32_bf16 v[136:139], v[220:223], v[24:27], v[136:139]\n"
"v_cvt_pk_bf16_f32 v205, v150, v151\n"
"v_fma_f32 v233, v233, v224, v235\n"
"s_add_u32 s22, s22, 114688\n"
"buffer_load_dwordx2 a[84:85], v248, s[28:31], s22 offen sc1\n"
"v_mfma_f32_16x16x32_bf16 v[140:143], v[220:223], v[28:31], v[140:143]\n"
"ds_read_b64_tr_b16 v[220:221], v252 offset:8992\n"
"s_add_u32 s22, s22, 114688\n"
"buffer_load_dwordx2 a[86:87], v248, s[28:31], s22 offen sc1\n"
"s_waitcnt lgkmcnt(3)\n"
"v_mfma_f32_16x16x32_bf16 v[160:163], v[208:211], v[32:35], v[160:163]\n"
"v_cvt_pk_bf16_f32 v206, v156, v157\n"
"v_cvt_pk_bf16_f32 v192, v232, v233\n"
"s_add_u32 s22, s22, 114688\n"
"buffer_load_dwordx2 a[88:89], v248, s[28:31], s22 offen sc1\n"
"v_mfma_f32_16x16x32_bf16 v[164:167], v[208:211], v[36:39], v[164:167]\n"
"ds_read_b64_tr_b16 v[222:223], v252 offset:9248\n"
"v_accvgpr_read_b32 v238, a2\n"
"s_add_u32 s22, s22, 114688\n"
"buffer_load_dwordx2 a[90:91], v248, s[28:31], s22 offen sc1\n"
"v_mfma_f32_16x16x32_bf16 v[128:131], v[208:211], v[40:43], v[128:131]\n"
"v_cvt_pk_bf16_f32 v207, v158, v159\n"
"v_accvgpr_read_b32 v239, a3\n"
"s_add_u32 s22, s22, 114688\n"
"buffer_load_dwordx2 a[92:93], v248, s[28:31], s22 offen sc1\n"
"v_mfma_f32_16x16x32_bf16 v[132:135], v[208:211], v[44:47], v[132:135]\n"
"ds_read_b64_tr_b16 v[208:209], v252 offset:64\n"
"s_add_u32 s22, s22, 114688\n"
"buffer_load_dwordx2 a[94:95], v248, s[28:31], s22 offen sc1\n"
"v_mfma_f32_16x16x32_bf16 v[168:171], v[212:215], v[32:35], v[168:171]\n"
"v_lshlrev_b32 v236, 16, v193\n"
"v_and_b32 v237, s21, v193\n"
"s_add_u32 s9, s9, 2048\n"
"s_cmp_eq_u32 s9, 14336\n"
"s_cselect_b32 s9, 0, s9\n"
"v_mfma_f32_16x16x32_bf16 v[172:175], v[212:215], v[36:39], v[172:175]\n"
"ds_read_b64_tr_b16 v[210:211], v252 offset:320\n"
"v_fma_f32 v236, v236, v224, v238\n"
"v_mfma_f32_16x16x32_bf16 v[136:139], v[212:215], v[40:43], v[136:139]\n"
"v_fma_f32 v237, v237, v224, v239\n"
"v_cvt_pk_bf16_f32 v193, v236, v237\n"
"v_mfma_f32_16x16x32_bf16 v[140:143], v[212:215], v[44:47], v[140:143]\n"
"ds_read_b64_tr_b16 v[212:213], v252 offset:96\n"
"s_waitcnt lgkmcnt(3)\n"
"v_mfma_f32_16x16x32_bf16 v[160:163], v[216:219], v[48:51], v[160:163]\n"
"v_accvgpr_read_b32 v234, a8\n"
"v_accvgpr_read_b32 v235, a9\n"
"v_mfma_f32_16x16x32_bf16 v[164:167], v[216:219], v[52:55], v[164:167]\n"
"ds_read_b64_tr_b16 v[214:215], v252 offset:352\n"
"v_lshlrev_b32 v232, 16, v194\n"
"v_mfma_f32_16x16x32_bf16 v[128:131], v[216:219], v[56:59], v[128:131]\n"
"v_and_b32 v233, s21, v194\n"
"v_fma_f32 v232, v232, v224, v234\n"
"v_mfma_f32_16x16x32_bf16 v[132:135], v[216:219], v[60:63], v[132:135]\n"
"ds_read_b64_tr_b16 v[216:217], v252 offset:576\n"
"v_mfma_f32_16x16x32_bf16 v[168:171], v[220:223], v[48:51], v[168:171]\n"
"v_fma_f32 v233, v233, v224, v235\n"
"v_cvt_pk_bf16_f32 v194, v232, v233\n"
"v_mfma_f32_16x16x32_bf16 v[172:175], v[220:223], v[52:55], v[172:175]\n"
"ds_read_b64_tr_b16 v[218:219], v252 offset:832\n"
"v_accvgpr_read_b32 v238, a10\n"
"v_mfma_f32_16x16x32_bf16 v[136:139], v[220:223], v[56:59], v[136:139]\n"
"v_accvgpr_read_b32 v239, a11\n"
"v_lshlrev_b32 v236, 16, v195\n"
"v_mfma_f32_16x16x32_bf16 v[140:143], v[220:223], v[60:63], v[140:143]\n"
"ds_read_b64_tr_b16 v[220:221], v252 offset:608\n"
"s_waitcnt lgkmcnt(3)\n"
"v_mfma_f32_16x16x32_bf16 v[176:179], v[208:211], v[0:3], 0\n"
"v_and_b32 v237, s21, v195\n"
"v_fma_f32 v236, v236, v224, v238\n"
"v_mfma_f32_16x16x32_bf16 v[180:183], v[208:211], v[4:7], 0\n"
"ds_read_b64_tr_b16 v[222:223], v252 offset:864\n"
"v_fma_f32 v237, v237, v224, v239\n"
"v_mfma_f32_16x16x32_bf16 v[144:147], v[208:211], v[8:11], 0\n"
"v_cvt_pk_bf16_f32 v195, v236, v237\n"
"s_add_u32 s65, s66, 14336\n"
"v_mfma_f32_16x16x32_bf16 v[148:151], v[208:211], v[12:15], 0\n"
"ds_read_b64_tr_b16 v[208:209], v252 offset:8512\n"
"v_mfma_f32_16x16x32_bf16 v[184:187], v[212:215], v[0:3], 0\n"
"buffer_store_dwordx4 v[192:195], v255, s[28:31], s66 offen offset:0 sc1\n"
"v_accvgpr_read_b32 v234, a4\n"
"v_mfma_f32_16x16x32_bf16 v[188:191], v[212:215], v[4:7], 0\n"
"ds_read_b64_tr_b16 v[210:211], v252 offset:8768\n"
"v_accvgpr_read_b32 v235, a5\n"
"v_mfma_f32_16x16x32_bf16 v[152:155], v[212:215], v[8:11], 0\n"
"v_lshlrev_b32 v232, 16, v196\n"
"v_and_b32 v233, s21, v196\n"
"v_mfma_f32_16x16x32_bf16 v[156:159], v[212:215], v[12:15], 0\n"
"ds_read_b64_tr_b16 v[212:213], v252 offset:8544\n"
"s_waitcnt lgkmcnt(3)\n"
"v_mfma_f32_16x16x32_bf16 v[176:179], v[216:219], v[16:19], v[176:179]\n"
"v_fma_f32 v232, v232, v225, v234\n"
"v_fma_f32 v233, v233, v225, v235\n"
"v_mfma_f32_16x16x32_bf16 v[180:183], v[216:219], v[20:23], v[180:183]\n"
"ds_read_b64_tr_b16 v[214:215], v252 offset:8800\n"
"v_cvt_pk_bf16_f32 v196, v232, v233\n"
"v_mfma_f32_16x16x32_bf16 v[144:147], v[216:219], v[24:27], v[144:147]\n"
"v_accvgpr_read_b32 v238, a6\n"
"v_accvgpr_read_b32 v239, a7\n"
"v_mfma_f32_16x16x32_bf16 v[148:151], v[216:219], v[28:31], v[148:151]\n"
"ds_read_b64_tr_b16 v[216:217], v252 offset:9024\n"
"v_mfma_f32_16x16x32_bf16 v[184:187], v[220:223], v[16:19], v[184:187]\n"
"v_lshlrev_b32 v236, 16, v197\n"
"v_and_b32 v237, s21, v197\n"
"v_mfma_f32_16x16x32_bf16 v[188:191], v[220:223], v[20:23], v[188:191]\n"
"ds_read_b64_tr_b16 v[218:219], v252 offset:9280\n"
"v_fma_f32 v236, v236, v225, v238\n"
"v_mfma_f32_16x16x32_bf16 v[152:155], v[220:223], v[24:27], v[152:155]\n"
"v_fma_f32 v237, v237, v225, v239\n"
"v_cvt_pk_bf16_f32 v197, v236, v237\n"
"v_mfma_f32_16x16x32_bf16 v[156:159], v[220:223], v[28:31], v[156:159]\n"
"ds_read_b64_tr_b16 v[220:221], v252 offset:9056\n"
"s_waitcnt lgkmcnt(5)\n"
"v_mfma_f32_16x16x32_bf16 v[176:179], v[208:211], v[32:35], v[176:179]\n"
"ds_write_b128 v254, v[200:203] offset:16384\n"
"v_accvgpr_read_b32 v234, a12\n"
"v_mfma_f32_16x16x32_bf16 v[180:183], v[208:211], v[36:39], v[180:183]\n"
"ds_read_b64_tr_b16 v[222:223], v252 offset:9312\n"
"v_accvgpr_read_b32 v235, a13\n"
"v_mfma_f32_16x16x32_bf16 v[144:147], v[208:211], v[40:43], v[144:147]\n"
"ds_write_b128 v254, v[204:207] offset:17408\n"
"v_lshlrev_b32 v232, 16, v198\n"
"v_mfma_f32_16x16x32_bf16 v[148:151], v[208:211], v[44:47], v[148:151]\n"
"v_and_b32 v233, s21, v198\n"
"s_waitcnt lgkmcnt(6)\n"
"v_mfma_f32_16x16x32_bf16 v[184:187], v[212:215], v[32:35], v[184:187]\n"
"v_fma_f32 v232, v232, v225, v234\n"
"v_fma_f32 v233, v233, v225, v235\n"
"v_mfma_f32_16x16x32_bf16 v[188:191], v[212:215], v[36:39], v[188:191]\n"
"v_cvt_pk_bf16_f32 v198, v232, v233\n"
"v_accvgpr_read_b32 v238, a14\n"
"v_mfma_f32_16x16x32_bf16 v[152:155], v[212:215], v[40:43], v[152:155]\n"
"s_waitcnt lgkmcnt(0)\n"
"s_barrier\n"
"ds_read_b64_tr_b16 v[208:209], v252 offset:33792\n"
"v_mfma_f32_16x16x32_bf16 v[156:159], v[212:215], v[44:47], v[156:159]\n"
"ds_read_b64_tr_b16 v[210:211], v252 offset:34048\n"
"s_add_u32 s69, s69, 1\n"
"v_mfma_f32_16x16x32_bf16 v[176:179], v[216:219], v[48:51], v[176:179]\n"
"ds_read_b64_tr_b16 v[212:213], v252 offset:33824\n"
"v_accvgpr_read_b32 v239, a15\n"
"s_max_i32 s15, s69, 0\n"
"v_mfma_f32_16x16x32_bf16 v[180:183], v[216:219], v[52:55], v[180:183]\n"
"ds_read_b64_tr_b16 v[214:215], v252 offset:34080\n"
"v_lshlrev_b32 v236, 16, v199\n"
"v_or_b32 v228, s15, v230\n"
"v_mfma_f32_16x16x32_bf16 v[144:147], v[216:219], v[56:59], v[144:147]\n"
"v_and_b32 v237, s21, v199\n"
"v_fma_f32 v236, v236, v225, v238\n"
"v_mfma_f32_16x16x32_bf16 v[148:151], v[216:219], v[60:63], v[148:151]\n"
"ds_read_b64_tr_b16 v[216:217], v252 offset:34304\n"
"v_mfma_f32_16x16x32_bf16 v[184:187], v[220:223], v[48:51], v[184:187]\n"
"ds_read_b64_tr_b16 v[218:219], v252 offset:34560\n"
"v_fma_f32 v237, v237, v225, v239\n"
"v_mfma_f32_16x16x32_bf16 v[188:191], v[220:223], v[52:55], v[188:191]\n"
"v_cvt_pk_bf16_f32 v199, v236, v237\n"
"buffer_store_dwordx4 v[196:199], v255, s[28:31], s65 offen offset:0 sc1\n"
"v_mfma_f32_16x16x32_bf16 v[152:155], v[220:223], v[56:59], v[152:155]\n"
"s_mov_b64 exec, s[46:47]\n"
"global_store_dword v245, v228, s[74:75] sc1\n"
"s_mov_b64 exec, -1\n"
"v_mfma_f32_16x16x32_bf16 v[156:159], v[220:223], v[60:63], v[156:159]\n"
"ds_read_b64_tr_b16 v[220:221], v252 offset:34336\n"
"ds_read_b64_tr_b16 v[222:223], v252 offset:34592\n"
"s_waitcnt lgkmcnt(6)\n"
"v_mfma_f32_16x16x32_bf16 v[160:163], v[208:211], v[64:67], v[160:163]\n"
"s_add_u32 s66, s61, s60\n"
"v_mfma_f32_16x16x32_bf16 v[164:167], v[208:211], v[68:71], v[164:167]\n"
"s_add_u32 s60, s60, 2048\n"
"s_cmp_eq_u32 s60, 14336\n"
"s_cselect_b32 s60, 0, s60\n"
"ds_read_b128 v[192:195], v255 offset:16384\n"
"v_mfma_f32_16x16x32_bf16 v[128:131], v[208:211], v[72:75], v[128:131]\n"
"ds_read_b128 v[196:199], v255 offset:17408\n"
"v_mfma_f32_16x16x32_bf16 v[132:135], v[208:211], v[76:79], v[132:135]\n"
"ds_read_b64_tr_b16 v[208:209], v252 offset:42240\n"
"s_waitcnt lgkmcnt(7)\n"
"v_mfma_f32_16x16x32_bf16 v[168:171], v[212:215], v[64:67], v[168:171]\n"
"v_mfma_f32_16x16x32_bf16 v[172:175], v[212:215], v[68:71], v[172:175]\n"
"ds_read_b64_tr_b16 v[210:211], v252 offset:42496\n"
"v_mfma_f32_16x16x32_bf16 v[136:139], v[212:215], v[72:75], v[136:139]\n"
"v_mfma_f32_16x16x32_bf16 v[140:143], v[212:215], v[76:79], v[140:143]\n"
"ds_read_b64_tr_b16 v[212:213], v252 offset:42272\n"
"s_waitcnt lgkmcnt(5)\n"
"v_mfma_f32_16x16x32_bf16 v[160:163], v[216:219], v[80:83], v[160:163]\n"
"v_accvgpr_read_b32 v234, a16\n"
"v_accvgpr_read_b32 v235, a17\n"
"v_mfma_f32_16x16x32_bf16 v[164:167], v[216:219], v[84:87], v[164:167]\n"
"ds_read_b64_tr_b16 v[214:215], v252 offset:42528\n"
"s_waitcnt lgkmcnt(5)\n"
"v_lshlrev_b32 v232, 16, v192\n"
"v_mfma_f32_16x16x32_bf16 v[128:131], v[216:219], v[88:91], v[128:131]\n"
"v_and_b32 v233, s21, v192\n"
"v_fma_f32 v232, v232, v224, v234\n"
"v_mfma_f32_16x16x32_bf16 v[132:135], v[216:219], v[92:95], v[132:135]\n"
"ds_read_b64_tr_b16 v[216:217], v252 offset:42752\n"
"v_mfma_f32_16x16x32_bf16 v[168:171], v[220:223], v[80:83], v[168:171]\n"
"v_fma_f32 v233, v233, v224, v235\n"
"v_cvt_pk_bf16_f32 v192, v232, v233\n"
"v_mfma_f32_16x16x32_bf16 v[172:175], v[220:223], v[84:87], v[172:175]\n"
"ds_read_b64_tr_b16 v[218:219], v252 offset:43008\n"
"v_accvgpr_read_b32 v238, a18\n"
"v_mfma_f32_16x16x32_bf16 v[136:139], v[220:223], v[88:91], v[136:139]\n"
"v_accvgpr_read_b32 v239, a19\n"
"v_lshlrev_b32 v236, 16, v193\n"
"v_mfma_f32_16x16x32_bf16 v[140:143], v[220:223], v[92:95], v[140:143]\n"
"ds_read_b64_tr_b16 v[220:221], v252 offset:42784\n"
"s_waitcnt lgkmcnt(3)\n"
"v_mfma_f32_16x16x32_bf16 v[160:163], v[208:211], v[96:99], v[160:163]\n"
"v_and_b32 v237, s21, v193\n"
"v_fma_f32 v236, v236, v224, v238\n"
"v_mfma_f32_16x16x32_bf16 v[164:167], v[208:211], v[100:103], v[164:167]\n"
"ds_read_b64_tr_b16 v[222:223], v252 offset:43040\n"
"v_fma_f32 v237, v237, v224, v239\n"
"v_mfma_f32_16x16x32_bf16 v[128:131], v[208:211], v[104:107], v[128:131]\n"
"v_cvt_pk_bf16_f32 v193, v236, v237\n"
"v_accvgpr_read_b32 v234, a24\n"
"v_mfma_f32_16x16x32_bf16 v[132:135], v[208:211], v[108:111], v[132:135]\n"
"ds_read_b64_tr_b16 v[208:209], v252 offset:33856\n"
"v_mfma_f32_16x16x32_bf16 v[168:171], v[212:215], v[96:99], v[168:171]\n"
"v_accvgpr_read_b32 v235, a25\n"
"v_lshlrev_b32 v232, 16, v194\n"
"v_mfma_f32_16x16x32_bf16 v[172:175], v[212:215], v[100:103], v[172:175]\n"
"ds_read_b64_tr_b16 v[210:211], v252 offset:34112\n"
"v_and_b32 v233, s21, v194\n"
"v_mfma_f32_16x16x32_bf16 v[136:139], v[212:215], v[104:107], v[136:139]\n"
"v_fma_f32 v232, v232, v224, v234\n"
"v_fma_f32 v233, v233, v224, v235\n"
"v_mfma_f32_16x16x32_bf16 v[140:143], v[212:215], v[108:111], v[140:143]\n"
"ds_read_b64_tr_b16 v[212:213], v252 offset:33888\n"
"s_waitcnt lgkmcnt(3)\n"
"v_mfma_f32_16x16x32_bf16 v[160:163], v[216:219], v[112:115], v[160:163]\n"
"v_cvt_pk_bf16_f32 v194, v232, v233\n"
"v_accvgpr_read_b32 v238, a26\n"
"v_mfma_f32_16x16x32_bf16 v[164:167], v[216:219], v[116:119], v[164:167]\n"
"ds_read_b64_tr_b16 v[214:215], v252 offset:34144\n"
"v_accvgpr_read_b32 v239, a27\n"
"v_mfma_f32_16x16x32_bf16 v[128:131], v[216:219], v[120:123], v[128:131]\n"
"v_lshlrev_b32 v236, 16, v195\n"
"v_and_b32 v237, s21, v195\n"
"v_mfma_f32_16x16x32_bf16 v[132:135], v[216:219], v[124:127], v[132:135]\n"
"ds_read_b64_tr_b16 v[216:217], v252 offset:34368\n"
"v_mfma_f32_16x16x32_bf16 v[168:171], v[220:223], v[112:115], v[168:171]\n"
"v_fma_f32 v236, v236, v224, v238\n"
"v_fma_f32 v237, v237, v224, v239\n"
"v_mfma_f32_16x16x32_bf16 v[172:175], v[220:223], v[116:119], v[172:175]\n"
"ds_read_b64_tr_b16 v[218:219], v252 offset:34624\n"
"v_cvt_pk_bf16_f32 v195, v236, v237\n"
"v_mfma_f32_16x16x32_bf16 v[136:139], v[220:223], v[120:123], v[136:139]\n"
"s_add_u32 s65, s66, 14336\n"
"buffer_store_dwordx4 v[192:195], v255, s[28:31], s66 offen offset:1024 sc1\n"
"v_mfma_f32_16x16x32_bf16 v[140:143], v[220:223], v[124:127], v[140:143]\n"
"ds_read_b64_tr_b16 v[220:221], v252 offset:34400\n"
"s_waitcnt lgkmcnt(3)\n"
"v_mfma_f32_16x16x32_bf16 v[176:179], v[208:211], v[64:67], v[176:179]\n"
"v_accvgpr_read_b32 v234, a20\n"
"v_accvgpr_read_b32 v235, a21\n"
"v_mfma_f32_16x16x32_bf16 v[180:183], v[208:211], v[68:71], v[180:183]\n"
"ds_read_b64_tr_b16 v[222:223], v252 offset:34656\n"
"v_lshlrev_b32 v232, 16, v196\n"
"v_mfma_f32_16x16x32_bf16 v[144:147], v[208:211], v[72:75], v[144:147]\n"
"v_and_b32 v233, s21, v196\n"
"v_fma_f32 v232, v232, v225, v234\n"
"v_mfma_f32_16x16x32_bf16 v[148:151], v[208:211], v[76:79], v[148:151]\n"
"ds_read_b64_tr_b16 v[208:209], v252 offset:42304\n"
"v_mfma_f32_16x16x32_bf16 v[184:187], v[212:215], v[64:67], v[184:187]\n"
"v_cvt_pk_bf16_f32 v200, v128, v129\n"
"v_cvt_pk_bf16_f32 v201, v130, v131\n"
"v_mfma_f32_16x16x32_bf16 v[188:191], v[212:215], v[68:71], v[188:191]\n"
"ds_read_b64_tr_b16 v[210:211], v252 offset:42560\n"
"v_cvt_pk_bf16_f32 v202, v136, v137\n"
"v_mfma_f32_16x16x32_bf16 v[152:155], v[212:215], v[72:75], v[152:155]\n"
"v_cvt_pk_bf16_f32 v203, v138, v139\n"
"v_cvt_pk_bf16_f32 v204, v132, v133\n"
"v_mfma_f32_16x16x32_bf16 v[156:159], v[212:215], v[76:79], v[156:159]\n"
"ds_read_b64_tr_b16 v[212:213], v252 offset:42336\n"
"s_waitcnt lgkmcnt(3)\n"
"v_mfma_f32_16x16x32_bf16 v[176:179], v[216:219], v[80:83], v[176:179]\n"
"v_cvt_pk_bf16_f32 v205, v134, v135\n"
"v_cvt_pk_bf16_f32 v206, v140, v141\n"
"v_mfma_f32_16x16x32_bf16 v[180:183], v[216:219], v[84:87], v[180:183]\n"
"ds_read_b64_tr_b16 v[214:215], v252 offset:42592\n"
"v_cvt_pk_bf16_f32 v207, v142, v143\n"
"v_mfma_f32_16x16x32_bf16 v[144:147], v[216:219], v[88:91], v[144:147]\n"
"v_fma_f32 v233, v233, v225, v235\n"
"v_cvt_pk_bf16_f32 v196, v232, v233\n"
"v_mfma_f32_16x16x32_bf16 v[148:151], v[216:219], v[92:95], v[148:151]\n"
"ds_read_b64_tr_b16 v[216:217], v252 offset:42816\n"
"v_mfma_f32_16x16x32_bf16 v[184:187], v[220:223], v[80:83], v[184:187]\n"
"v_accvgpr_read_b32 v238, a22\n"
"v_accvgpr_read_b32 v239, a23\n"
"v_mfma_f32_16x16x32_bf16 v[188:191], v[220:223], v[84:87], v[188:191]\n"
"ds_read_b64_tr_b16 v[218:219], v252 offset:43072\n"
"v_lshlrev_b32 v236, 16, v197\n"
"v_mfma_f32_16x16x32_bf16 v[152:155], v[220:223], v[88:91], v[152:155]\n"
"v_and_b32 v237, s21, v197\n"
"v_fma_f32 v236, v236, v225, v238\n"
"v_mfma_f32_16x16x32_bf16 v[156:159], v[220:223], v[92:95], v[156:159]\n"
"ds_read_b64_tr_b16 v[220:221], v252 offset:42848\n"
"s_waitcnt lgkmcnt(5)\n"
"v_mfma_f32_16x16x32_bf16 v[176:179], v[208:211], v[96:99], v[176:179]\n"
"ds_write_b128 v254, v[200:203] offset:18432\n"
"v_fma_f32 v237, v237, v225, v239\n"
"v_mfma_f32_16x16x32_bf16 v[180:183], v[208:211], v[100:103], v[180:183]\n"
"ds_read_b64_tr_b16 v[222:223], v252 offset:43104\n"
"v_cvt_pk_bf16_f32 v197, v236, v237\n"
"v_mfma_f32_16x16x32_bf16 v[144:147], v[208:211], v[104:107], v[144:147]\n"
"ds_write_b128 v254, v[204:207] offset:19456\n"
"v_accvgpr_read_b32 v234, a28\n"
"v_mfma_f32_16x16x32_bf16 v[148:151], v[208:211], v[108:111], v[148:151]\n"
"v_accvgpr_read_b32 v235, a29\n"
"s_waitcnt lgkmcnt(6)\n"
"v_mfma_f32_16x16x32_bf16 v[184:187], v[212:215], v[96:99], v[184:187]\n"
"v_lshlrev_b32 v232, 16, v198\n"
"v_and_b32 v233, s21, v198\n"
"v_mfma_f32_16x16x32_bf16 v[188:191], v[212:215], v[100:103], v[188:191]\n"
"v_fma_f32 v232, v232, v225, v234\n"
"v_fma_f32 v233, v233, v225, v235\n"
"v_mfma_f32_16x16x32_bf16 v[152:155], v[212:215], v[104:107], v[152:155]\n"
"v_cvt_pk_bf16_f32 v198, v232, v233\n"
"v_accvgpr_read_b32 v238, a30\n"
"v_mfma_f32_16x16x32_bf16 v[156:159], v[212:215], v[108:111], v[156:159]\n"
"v_accvgpr_read_b32 v239, a31\n"
"s_waitcnt lgkmcnt(4)\n"
"v_mfma_f32_16x16x32_bf16 v[176:179], v[216:219], v[112:115], v[176:179]\n"
"v_lshlrev_b32 v236, 16, v199\n"
"v_and_b32 v237, s21, v199\n"
"v_mfma_f32_16x16x32_bf16 v[180:183], v[216:219], v[116:119], v[180:183]\n"
"v_fma_f32 v236, v236, v225, v238\n"
"v_fma_f32 v237, v237, v225, v239\n"
"v_mfma_f32_16x16x32_bf16 v[144:147], v[216:219], v[120:123], v[144:147]\n"
"v_cvt_pk_bf16_f32 v199, v236, v237\n"
"buffer_store_dwordx4 v[196:199], v255, s[28:31], s65 offen offset:1024 sc1\n"
"v_mfma_f32_16x16x32_bf16 v[148:151], v[216:219], v[124:127], v[148:151]\n"
"global_load_dword v231, v244, s[74:75] sc1\n"
"s_waitcnt lgkmcnt(1)\n"
"v_mfma_f32_16x16x32_bf16 v[184:187], v[220:223], v[112:115], v[184:187]\n"
"s_add_u32 s20, s71, 3\n"
"s_sub_u32 s70, 7, 3\n"
"v_mfma_f32_16x16x32_bf16 v[188:191], v[220:223], v[116:119], v[188:191]\n"
"v_mfma_f32_16x16x32_bf16 v[152:155], v[220:223], v[120:123], v[152:155]\n"
"v_mfma_f32_16x16x32_bf16 v[156:159], v[220:223], v[124:127], v[156:159]\n"
"s_add_u32 s66, s61, s60\n"
"s_waitcnt vmcnt(22) lgkmcnt(0)\n"
"s_barrier\n"
"s_add_u32 s69, s69, 1\n"
"s_max_i32 s15, s69, 0\n"
"v_or_b32 v228, s15, v230\n"
"s_mov_b64 exec, s[46:47]\n"
"global_store_dword v245, v228, s[74:75] sc1\n"
"s_mov_b64 exec, -1\n"
"v_bfe_u32 v120, v253, 1, 4\n"
"s_lshl_b32 s96, s43, 12\n"
"s_lshl_b32 s97, s40, 6\n"
"s_add_u32 s96, s96, s97\n"
"v_lshlrev_b32 v120, 2, v120\n"
"s_add_u32 s96, s96, 65536\n"
"buffer_load_dword v112, v120, s[28:31], s96 offen offset:0 sc1\n"
"buffer_load_dword v113, v120, s[28:31], s96 offen offset:512 sc1\n"
"buffer_load_dword v114, v120, s[28:31], s96 offen offset:1024 sc1\n"
"buffer_load_dword v115, v120, s[28:31], s96 offen offset:1536 sc1\n"
"buffer_load_dword v116, v120, s[28:31], s96 offen offset:2048 sc1\n"
"buffer_load_dword v117, v120, s[28:31], s96 offen offset:2560 sc1\n"
"buffer_load_dword v118, v120, s[28:31], s96 offen offset:3072 sc1\n"
"buffer_load_dword v119, v120, s[28:31], s96 offen offset:3584 sc1\n"
"ds_read_b128 v[192:195], v255 offset:18432\n"
"ds_read_b128 v[196:199], v255 offset:19456\n"
"v_cvt_pk_bf16_f32 v200, v144, v145\n"
"v_cvt_pk_bf16_f32 v201, v146, v147\n"
"v_cvt_pk_bf16_f32 v202, v152, v153\n"
"v_cvt_pk_bf16_f32 v203, v154, v155\n"
"ds_write_b128 v254, v[200:203] offset:16384\n"
"v_cvt_pk_bf16_f32 v204, v148, v149\n"
"v_cvt_pk_bf16_f32 v205, v150, v151\n"
"v_cvt_pk_bf16_f32 v206, v156, v157\n"
"v_cvt_pk_bf16_f32 v207, v158, v159\n"
"ds_write_b128 v254, v[204:207] offset:17408\n"
"s_waitcnt lgkmcnt(3)\n"
"v_lshlrev_b32 v232, 16, v192\n"
"v_and_b32 v233, s21, v192\n"
"v_fma_f32 v232, v232, v224, v160\n"
"v_fma_f32 v233, v233, v224, v161\n"
"v_cvt_pk_bf16_f32 v192, v232, v233\n"
"v_lshlrev_b32 v236, 16, v193\n"
"v_and_b32 v237, s21, v193\n"
"v_fma_f32 v236, v236, v224, v162\n"
"v_fma_f32 v237, v237, v224, v163\n"
"v_cvt_pk_bf16_f32 v193, v236, v237\n"
"v_lshlrev_b32 v232, 16, v194\n"
"v_and_b32 v233, s21, v194\n"
"v_fma_f32 v232, v232, v224, v168\n"
"v_fma_f32 v233, v233, v224, v169\n"
"v_cvt_pk_bf16_f32 v194, v232, v233\n"
"v_lshlrev_b32 v236, 16, v195\n"
"v_and_b32 v237, s21, v195\n"
"v_fma_f32 v236, v236, v224, v170\n"
"v_fma_f32 v237, v237, v224, v171\n"
"v_cvt_pk_bf16_f32 v195, v236, v237\n"
"s_waitcnt lgkmcnt(0)\n"
"s_barrier\n"
"ds_read_b128 v[208:211], v255 offset:16384\n"
"ds_read_b128 v[212:215], v255 offset:17408\n"
"s_add_u32 s65, s66, 14336\n"
"buffer_store_dwordx4 v[192:195], v255, s[28:31], s66 offen offset:0 sc1\n"
"v_lshlrev_b32 v232, 16, v196\n"
"v_and_b32 v233, s21, v196\n"
"v_fma_f32 v232, v232, v225, v164\n"
"v_fma_f32 v233, v233, v225, v165\n"
"v_cvt_pk_bf16_f32 v196, v232, v233\n"
"v_lshlrev_b32 v236, 16, v197\n"
"v_and_b32 v237, s21, v197\n"
"v_fma_f32 v236, v236, v225, v166\n"
"v_fma_f32 v237, v237, v225, v167\n"
"v_cvt_pk_bf16_f32 v197, v236, v237\n"
"v_lshlrev_b32 v232, 16, v198\n"
"v_and_b32 v233, s21, v198\n"
"v_fma_f32 v232, v232, v225, v172\n"
"v_fma_f32 v233, v233, v225, v173\n"
"v_cvt_pk_bf16_f32 v198, v232, v233\n"
"v_lshlrev_b32 v236, 16, v199\n"
"v_and_b32 v237, s21, v199\n"
"v_fma_f32 v236, v236, v225, v174\n"
"v_fma_f32 v237, v237, v225, v175\n"
"v_cvt_pk_bf16_f32 v199, v236, v237\n"
"buffer_store_dwordx4 v[196:199], v255, s[28:31], s65 offen offset:0 sc1\n"
"s_waitcnt lgkmcnt(1)\n"
"v_lshlrev_b32 v232, 16, v208\n"
"v_and_b32 v233, s21, v208\n"
"v_fma_f32 v232, v232, v224, v176\n"
"v_fma_f32 v233, v233, v224, v177\n"
"v_cvt_pk_bf16_f32 v208, v232, v233\n"
"v_lshlrev_b32 v236, 16, v209\n"
"v_and_b32 v237, s21, v209\n"
"v_fma_f32 v236, v236, v224, v178\n"
"v_fma_f32 v237, v237, v224, v179\n"
"v_cvt_pk_bf16_f32 v209, v236, v237\n"
"v_lshlrev_b32 v232, 16, v210\n"
"v_and_b32 v233, s21, v210\n"
"v_fma_f32 v232, v232, v224, v184\n"
"v_fma_f32 v233, v233, v224, v185\n"
"v_cvt_pk_bf16_f32 v210, v232, v233\n"
"v_lshlrev_b32 v236, 16, v211\n"
"v_and_b32 v237, s21, v211\n"
"v_fma_f32 v236, v236, v224, v186\n"
"v_fma_f32 v237, v237, v224, v187\n"
"v_cvt_pk_bf16_f32 v211, v236, v237\n"
"s_add_u32 s65, s66, 14336\n"
"buffer_store_dwordx4 v[208:211], v255, s[28:31], s66 offen offset:1024 sc1\n"
"s_waitcnt lgkmcnt(0)\n"
"v_lshlrev_b32 v232, 16, v212\n"
"v_and_b32 v233, s21, v212\n"
"v_fma_f32 v232, v232, v225, v180\n"
"v_fma_f32 v233, v233, v225, v181\n"
"v_cvt_pk_bf16_f32 v212, v232, v233\n"
"v_lshlrev_b32 v236, 16, v213\n"
"v_and_b32 v237, s21, v213\n"
"v_fma_f32 v236, v236, v225, v182\n"
"v_fma_f32 v237, v237, v225, v183\n"
"v_cvt_pk_bf16_f32 v213, v236, v237\n"
"v_lshlrev_b32 v232, 16, v214\n"
"v_and_b32 v233, s21, v214\n"
"v_fma_f32 v232, v232, v225, v188\n"
"v_fma_f32 v233, v233, v225, v189\n"
"v_cvt_pk_bf16_f32 v214, v232, v233\n"
"v_lshlrev_b32 v236, 16, v215\n"
"v_and_b32 v237, s21, v215\n"
"v_fma_f32 v236, v236, v225, v190\n"
"v_fma_f32 v237, v237, v225, v191\n"
"v_cvt_pk_bf16_f32 v215, v236, v237\n"
"buffer_store_dwordx4 v[212:215], v255, s[28:31], s65 offen offset:1024 sc1\n"
"s_waitcnt vmcnt(13)\n"
"v_subrev_u32 v229, s20, v231\n"
"v_cmp_lt_u32 vcc, s70, v229\n"
"s_nop 1\n"
"s_cbranch_vccnz LTX_%=\n"
"s_add_u32 s22, s8, s9\n"
"buffer_load_dwordx2 v[32:33], v248, s[28:31], s22 offen sc1\n"
"s_add_u32 s22, s22, 114688\n"
"buffer_load_dwordx2 v[34:35], v248, s[28:31], s22 offen sc1\n"
"s_add_u32 s22, s22, 114688\n"
"buffer_load_dwordx2 v[36:37], v248, s[28:31], s22 offen sc1\n"
"s_add_u32 s22, s22, 114688\n"
"buffer_load_dwordx2 v[38:39], v248, s[28:31], s22 offen sc1\n"
"s_add_u32 s22, s22, 114688\n"
"buffer_load_dwordx2 v[40:41], v248, s[28:31], s22 offen sc1\n"
"s_add_u32 s22, s22, 114688\n"
"buffer_load_dwordx2 v[42:43], v248, s[28:31], s22 offen sc1\n"
"s_add_u32 s22, s22, 114688\n"
"buffer_load_dwordx2 v[44:45], v248, s[28:31], s22 offen sc1\n"
"s_add_u32 s22, s22, 114688\n"
"buffer_load_dwordx2 v[46:47], v248, s[28:31], s22 offen sc1\n"
"s_add_u32 s9, s9, 2048\n"
"s_cmp_eq_u32 s9, 14336\n"
"s_cselect_b32 s9, 0, s9\n"
"v_accvgpr_read_b32 v0, a64\n"
"v_accvgpr_read_b32 v1, a65\n"
"v_accvgpr_read_b32 v2, a66\n"
"v_accvgpr_read_b32 v3, a67\n"
"v_accvgpr_read_b32 v4, a68\n"
"v_accvgpr_read_b32 v5, a69\n"
"v_accvgpr_read_b32 v6, a70\n"
"v_accvgpr_read_b32 v7, a71\n"
"v_accvgpr_read_b32 v8, a72\n"
"v_accvgpr_read_b32 v9, a73\n"
"v_accvgpr_read_b32 v10, a74\n"
"v_accvgpr_read_b32 v11, a75\n"
"v_accvgpr_read_b32 v12, a76\n"
"v_accvgpr_read_b32 v13, a77\n"
"v_accvgpr_read_b32 v14, a78\n"
"v_accvgpr_read_b32 v15, a79\n"
"v_accvgpr_read_b32 v16, a80\n"
"v_accvgpr_read_b32 v17, a81\n"
"v_accvgpr_read_b32 v18, a82\n"
"v_accvgpr_read_b32 v19, a83\n"
"v_accvgpr_read_b32 v20, a84\n"
"v_accvgpr_read_b32 v21, a85\n"
"v_accvgpr_read_b32 v22, a86\n"
"v_accvgpr_read_b32 v23, a87\n"
"v_accvgpr_read_b32 v24, a88\n"
"v_accvgpr_read_b32 v25, a89\n"
"v_accvgpr_read_b32 v26, a90\n"
"v_accvgpr_read_b32 v27, a91\n"
"v_accvgpr_read_b32 v28, a92\n"
"v_accvgpr_read_b32 v29, a93\n"
"v_accvgpr_read_b32 v30, a94\n"
"v_accvgpr_read_b32 v31, a95\n"
"s_waitcnt vmcnt(8)\n"
"s_barrier\n"
"s_add_u32 s69, s69, 2\n"
"s_max_i32 s15, s69, 0\n"
"v_or_b32 v228, s15, v230\n"
"s_mov_b64 exec, s[46:47]\n"
"global_store_dword v245, v228, s[74:75] sc1\n"
"s_mov_b64 exec, -1\n"
"s_branch LTY_%=\n"
"LTX_%=:\n"
"s_waitcnt vmcnt(0)\n"
"v_accvgpr_read_b32 v0, a64\n"
"v_accvgpr_read_b32 v1, a65\n"
"v_accvgpr_read_b32 v2, a66\n"
"v_accvgpr_read_b32 v3, a67\n"
"v_accvgpr_read_b32 v4, a68\n"
"v_accvgpr_read_b32 v5, a69\n"
"v_accvgpr_read_b32 v6, a70\n"
"v_accvgpr_read_b32 v7, a71\n"
"v_accvgpr_read_b32 v8, a72\n"
"v_accvgpr_read_b32 v9, a73\n"
"v_accvgpr_read_b32 v10, a74\n"
"v_accvgpr_read_b32 v11, a75\n"
"v_accvgpr_read_b32 v12, a76\n"
"v_accvgpr_read_b32 v13, a77\n"
"v_accvgpr_read_b32 v14, a78\n"
"v_accvgpr_read_b32 v15, a79\n"
"v_accvgpr_read_b32 v16, a80\n"
"v_accvgpr_read_b32 v17, a81\n"
"v_accvgpr_read_b32 v18, a82\n"
"v_accvgpr_read_b32 v19, a83\n"
"v_accvgpr_read_b32 v20, a84\n"
"v_accvgpr_read_b32 v21, a85\n"
"v_accvgpr_read_b32 v22, a86\n"
"v_accvgpr_read_b32 v23, a87\n"
"v_accvgpr_read_b32 v24, a88\n"
"v_accvgpr_read_b32 v25, a89\n"
"v_accvgpr_read_b32 v26, a90\n"
"v_accvgpr_read_b32 v27, a91\n"
"v_accvgpr_read_b32 v28, a92\n"
"v_accvgpr_read_b32 v29, a93\n"
"v_accvgpr_read_b32 v30, a94\n"
"v_accvgpr_read_b32 v31, a95\n"
"s_barrier\n"
"s_add_u32 s69, s69, 2\n"
"s_max_i32 s15, s69, 0\n"
"v_or_b32 v228, s15, v230\n"
"s_mov_b64 exec, s[46:47]\n"
"global_store_dword v245, v228, s[74:75] sc1\n"
"s_mov_b64 exec, -1\n"
"s_getpc_b64 s[72:73]\n"
"s_branch LSP_%=\n"
"s_add_u32 s22, s8, s9\n"
"buffer_load_dwordx2 v[32:33], v248, s[28:31], s22 offen sc1\n"
"s_add_u32 s22, s22, 114688\n"
"buffer_load_dwordx2 v[34:35], v248, s[28:31], s22 offen sc1\n"
"s_add_u32 s22, s22, 114688\n"
"buffer_load_dwordx2 v[36:37], v248, s[28:31], s22 offen sc1\n"
"s_add_u32 s22, s22, 114688\n"
"buffer_load_dwordx2 v[38:39], v248, s[28:31], s22 offen sc1\n"
"s_add_u32 s22, s22, 114688\n"
"buffer_load_dwordx2 v[40:41], v248, s[28:31], s22 offen sc1\n"
"s_add_u32 s22, s22, 114688\n"
"buffer_load_dwordx2 v[42:43], v248, s[28:31], s22 offen sc1\n"
"s_add_u32 s22, s22, 114688\n"
"buffer_load_dwordx2 v[44:45], v248, s[28:31], s22 offen sc1\n"
"s_add_u32 s22, s22, 114688\n"
"buffer_load_dwordx2 v[46:47], v248, s[28:31], s22 offen sc1\n"
"s_add_u32 s9, s9, 2048\n"
"s_cmp_eq_u32 s9, 14336\n"
"s_cselect_b32 s9, 0, s9\n"
"s_add_u32 s69, s69, 0\n"
"s_max_i32 s15, s69, 0\n"
"v_or_b32 v228, s15, v230\n"
"s_mov_b64 exec, s[46:47]\n"
"global_store_dword v245, v228, s[74:75] sc1\n"
"s_mov_b64 exec, -1\n"
"LTY_%=:\n"
"global_load_dword v231, v244, s[74:75] sc1\n"
"s_add_u32 s20, s71, 5\n"
"s_sub_u32 s70, 7, 5\n"
"v_lshlrev_b32 v246, 16, v0\n"
"v_and_b32 v0, s21, v0\n"
"v_mul_f32 v246, v246, v112\n"
"v_mul_f32 v0, v0, v112\n"
"v_lshlrev_b32 v247, 16, v1\n"
"v_and_b32 v1, s21, v1\n"
"v_mul_f32 v247, v247, v112\n"
"v_mul_f32 v1, v1, v112\n"
"v_lshlrev_b32 v253, 16, v2\n"
"v_and_b32 v2, s21, v2\n"
"v_fmac_f32 v246, v253, v113\n"
"v_fmac_f32 v0, v2, v113\n"
"v_lshlrev_b32 v253, 16, v3\n"
"v_and_b32 v3, s21, v3\n"
"v_fmac_f32 v247, v253, v113\n"
"v_fmac_f32 v1, v3, v113\n"
"v_lshlrev_b32 v253, 16, v4\n"
"v_and_b32 v4, s21, v4\n"
"v_fmac_f32 v246, v253, v114\n"
"v_fmac_f32 v0, v4, v114\n"
"v_lshlrev_b32 v253, 16, v5\n"
"v_and_b32 v5, s21, v5\n"
"v_fmac_f32 v247, v253, v114\n"
"v_fmac_f32 v1, v5, v114\n"
"v_lshlrev_b32 v253, 16, v6\n"
"v_and_b32 v6, s21, v6\n"
"v_fmac_f32 v246, v253, v115\n"
"v_fmac_f32 v0, v6, v115\n"
"v_lshlrev_b32 v253, 16, v7\n"
"v_and_b32 v7, s21, v7\n"
"v_fmac_f32 v247, v253, v115\n"
"v_fmac_f32 v1, v7, v115\n"
"v_lshlrev_b32 v253, 16, v8\n"
"v_and_b32 v8, s21, v8\n"
"v_fmac_f32 v246, v253, v116\n"
"v_fmac_f32 v0, v8, v116\n"
"v_lshlrev_b32 v253, 16, v9\n"
"v_and_b32 v9, s21, v9\n"
"v_fmac_f32 v247, v253, v116\n"
"v_fmac_f32 v1, v9, v116\n"
"v_lshlrev_b32 v253, 16, v10\n"
"v_and_b32 v10, s21, v10\n"
"v_fmac_f32 v246, v253, v117\n"
"v_fmac_f32 v0, v10, v117\n"
"v_lshlrev_b32 v253, 16, v11\n"
"v_and_b32 v11, s21, v11\n"
"v_fmac_f32 v247, v253, v117\n"
"v_fmac_f32 v1, v11, v117\n"
"v_lshlrev_b32 v253, 16, v12\n"
"v_and_b32 v12, s21, v12\n"
"v_fmac_f32 v246, v253, v118\n"
"v_fmac_f32 v0, v12, v118\n"
"v_lshlrev_b32 v253, 16, v13\n"
"v_and_b32 v13, s21, v13\n"
"v_fmac_f32 v247, v253, v118\n"
"v_fmac_f32 v1, v13, v118\n"
"v_lshlrev_b32 v253, 16, v14\n"
"v_and_b32 v14, s21, v14\n"
"v_fmac_f32 v246, v253, v119\n"
"v_fmac_f32 v0, v14, v119\n"
"v_lshlrev_b32 v253, 16, v15\n"
"v_and_b32 v15, s21, v15\n"
"v_fmac_f32 v247, v253, v119\n"
"v_fmac_f32 v1, v15, v119\n"
"v_cvt_pk_bf16_f32 v2, v246, v0\n"
"v_cvt_pk_bf16_f32 v3, v247, v1\n"
"s_add_u32 s23, s10, s11\n"
"buffer_store_dwordx2 v[2:3], v249, s[32:35], s23 offen\n"
"s_add_u32 s11, s11, 128\n"
"s_cmp_eq_u32 s11, 896\n"
"s_cselect_b32 s11, 0, s11\n"
"v_lshlrev_b32 v246, 16, v16\n"
"v_and_b32 v16, s21, v16\n"
"v_mul_f32 v246, v246, v112\n"
"v_mul_f32 v16, v16, v112\n"
"v_lshlrev_b32 v247, 16, v17\n"
"v_and_b32 v17, s21, v17\n"
"v_mul_f32 v247, v247, v112\n"
"v_mul_f32 v17, v17, v112\n"
"v_lshlrev_b32 v253, 16, v18\n"
"v_and_b32 v18, s21, v18\n"
"v_fmac_f32 v246, v253, v113\n"
"v_fmac_f32 v16, v18, v113\n"
"v_lshlrev_b32 v253, 16, v19\n"
"v_and_b32 v19, s21, v19\n"
"v_fmac_f32 v247, v253, v113\n"
"v_fmac_f32 v17, v19, v113\n"
"v_lshlrev_b32 v253, 16, v20\n"
"v_and_b32 v20, s21, v20\n"
"v_fmac_f32 v246, v253, v114\n"
"v_fmac_f32 v16, v20, v114\n"
"v_lshlrev_b32 v253, 16, v21\n"
"v_and_b32 v21, s21, v21\n"
"v_fmac_f32 v247, v253, v114\n"
"v_fmac_f32 v17, v21, v114\n"
"v_lshlrev_b32 v253, 16, v22\n"
"v_and_b32 v22, s21, v22\n"
"v_fmac_f32 v246, v253, v115\n"
"v_fmac_f32 v16, v22, v115\n"
"v_lshlrev_b32 v253, 16, v23\n"
"v_and_b32 v23, s21, v23\n"
"v_fmac_f32 v247, v253, v115\n"
"v_fmac_f32 v17, v23, v115\n"
"v_lshlrev_b32 v253, 16, v24\n"
"v_and_b32 v24, s21, v24\n"
"v_fmac_f32 v246, v253, v116\n"
"v_fmac_f32 v16, v24, v116\n"
"v_lshlrev_b32 v253, 16, v25\n"
"v_and_b32 v25, s21, v25\n"
"v_fmac_f32 v247, v253, v116\n"
"v_fmac_f32 v17, v25, v116\n"
"v_lshlrev_b32 v253, 16, v26\n"
"v_and_b32 v26, s21, v26\n"
"v_fmac_f32 v246, v253, v117\n"
"v_fmac_f32 v16, v26, v117\n"
"v_lshlrev_b32 v253, 16, v27\n"
"v_and_b32 v27, s21, v27\n"
"v_fmac_f32 v247, v253, v117\n"
"v_fmac_f32 v17, v27, v117\n"
"v_lshlrev_b32 v253, 16, v28\n"
"v_and_b32 v28, s21, v28\n"
"v_fmac_f32 v246, v253, v118\n"
"v_fmac_f32 v16, v28, v118\n"
"v_lshlrev_b32 v253, 16, v29\n"
"v_and_b32 v29, s21, v29\n"
"v_fmac_f32 v247, v253, v118\n"
"v_fmac_f32 v17, v29, v118\n"
"v_lshlrev_b32 v253, 16, v30\n"
"v_and_b32 v30, s21, v30\n"
"v_fmac_f32 v246, v253, v119\n"
"v_fmac_f32 v16, v30, v119\n"
"v_lshlrev_b32 v253, 16, v31\n"
"v_and_b32 v31, s21, v31\n"
"v_fmac_f32 v247, v253, v119\n"
"v_fmac_f32 v17, v31, v119\n"
"v_cvt_pk_bf16_f32 v18, v246, v16\n"
"v_cvt_pk_bf16_f32 v19, v247, v17\n"
"s_add_u32 s23, s10, s11\n"
"buffer_store_dwordx2 v[18:19], v249, s[32:35], s23 offen\n"
"s_add_u32 s11, s11, 128\n"
"s_cmp_eq_u32 s11, 896\n"
"s_cselect_b32 s11, 0, s11\n"
"s_waitcnt vmcnt(2)\n"
"v_subrev_u32 v229, s20, v231\n"
"v_cmp_lt_u32 vcc, s70, v229\n"
"s_nop 1\n"
"s_cbranch_vccz LR_32_%=\n"
"s_getpc_b64 s[72:73]\n"
"s_branch LSP_%=\n"
"LR_32_%=:\n"
"s_add_u32 s22, s8, s9\n"
"buffer_load_dwordx2 v[48:49], v248, s[28:31], s22 offen sc1\n"
"s_add_u32 s22, s22, 114688\n"
"buffer_load_dwordx2 v[50:51], v248, s[28:31], s22 offen sc1\n"
"s_add_u32 s22, s22, 114688\n"
"buffer_load_dwordx2 v[52:53], v248, s[28:31], s22 offen sc1\n"
"s_add_u32 s22, s22, 114688\n"
"buffer_load_dwordx2 v[54:55], v248, s[28:31], s22 offen sc1\n"
"s_add_u32 s22, s22, 114688\n"
"buffer_load_dwordx2 v[56:57], v248, s[28:31], s22 offen sc1\n"
"s_add_u32 s22, s22, 114688\n"
"buffer_load_dwordx2 v[58:59], v248, s[28:31], s22 offen sc1\n"
"s_add_u32 s22, s22, 114688\n"
"buffer_load_dwordx2 v[60:61], v248, s[28:31], s22 offen sc1\n"
"s_add_u32 s22, s22, 114688\n"
"buffer_load_dwordx2 v[62:63], v248, s[28:31], s22 offen sc1\n"
"s_add_u32 s9, s9, 2048\n"
"s_cmp_eq_u32 s9, 14336\n"
"s_cselect_b32 s9, 0, s9\n"
"s_add_u32 s22, s8, s9\n"
"buffer_load_dwordx2 v[64:65], v248, s[28:31], s22 offen sc1\n"
"s_add_u32 s22, s22, 114688\n"
"buffer_load_dwordx2 v[66:67], v248, s[28:31], s22 offen sc1\n"
"s_add_u32 s22, s22, 114688\n"
"buffer_load_dwordx2 v[68:69], v248, s[28:31], s22 offen sc1\n"
"s_add_u32 s22, s22, 114688\n"
"buffer_load_dwordx2 v[70:71], v248, s[28:31], s22 offen sc1\n"
"s_add_u32 s22, s22, 114688\n"
"buffer_load_dwordx2 v[72:73], v248, s[28:31], s22 offen sc1\n"
"s_add_u32 s22, s22, 114688\n"
"buffer_load_dwordx2 v[74:75], v248, s[28:31], s22 offen sc1\n"
"s_add_u32 s22, s22, 114688\n"
"buffer_load_dwordx2 v[76:77], v248, s[28:31], s22 offen sc1\n"
"s_add_u32 s22, s22, 114688\n"
"buffer_load_dwordx2 v[78:79], v248, s[28:31], s22 offen sc1\n"
"s_add_u32 s9, s9, 2048\n"
"s_cmp_eq_u32 s9, 14336\n"
"s_cselect_b32 s9, 0, s9\n"
"s_add_u32 s20, s71, 7\n"
"s_sub_u32 s70, 7, 7\n"
"v_subrev_u32 v229, s20, v231\n"
"v_cmp_lt_u32 vcc, s70, v229\n"
"s_nop 1\n"
"s_cbranch_vccz LR_33_%=\n"
"s_getpc_b64 s[72:73]\n"
"s_branch LSP_%=\n"
"LR_33_%=:\n"
"s_add_u32 s22, s8, s9\n"
"buffer_load_dwordx2 v[80:81], v248, s[28:31], s22 offen sc1\n"
"s_add_u32 s22, s22, 114688\n"
"buffer_load_dwordx2 v[82:83], v248, s[28:31], s22 offen sc1\n"
"s_add_u32 s22, s22, 114688\n"
"buffer_load_dwordx2 v[84:85], v248, s[28:31], s22 offen sc1\n"
"s_add_u32 s22, s22, 114688\n"
"buffer_load_dwordx2 v[86:87], v248, s[28:31], s22 offen sc1\n"
"s_add_u32 s22, s22, 114688\n"
"buffer_load_dwordx2 v[88:89], v248, s[28:31], s22 offen sc1\n"
"s_add_u32 s22, s22, 114688\n"
"buffer_load_dwordx2 v[90:91], v248, s[28:31], s22 offen sc1\n"
"s_add_u32 s22, s22, 114688\n"
"buffer_load_dwordx2 v[92:93], v248, s[28:31], s22 offen sc1\n"
"s_add_u32 s22, s22, 114688\n"
"buffer_load_dwordx2 v[94:95], v248, s[28:31], s22 offen sc1\n"
"s_add_u32 s9, s9, 2048\n"
"s_cmp_eq_u32 s9, 14336\n"
"s_cselect_b32 s9, 0, s9\n"
"s_add_u32 s22, s8, s9\n"
"buffer_load_dwordx2 v[96:97], v248, s[28:31], s22 offen sc1\n"
"s_add_u32 s22, s22, 114688\n"
"buffer_load_dwordx2 v[98:99], v248, s[28:31], s22 offen sc1\n"
"s_add_u32 s22, s22, 114688\n"
"buffer_load_dwordx2 v[100:101], v248, s[28:31], s22 offen sc1\n"
"s_add_u32 s22, s22, 114688\n"
"buffer_load_dwordx2 v[102:103], v248, s[28:31], s22 offen sc1\n"
"s_add_u32 s22, s22, 114688\n"
"buffer_load_dwordx2 v[104:105], v248, s[28:31], s22 offen sc1\n"
"s_add_u32 s22, s22, 114688\n"
"buffer_load_dwordx2 v[106:107], v248, s[28:31], s22 offen sc1\n"
"s_add_u32 s22, s22, 114688\n"
"buffer_load_dwordx2 v[108:109], v248, s[28:31], s22 offen sc1\n"
"s_add_u32 s22, s22, 114688\n"
"buffer_load_dwordx2 v[110:111], v248, s[28:31], s22 offen sc1\n"
"s_add_u32 s9, s9, 2048\n"
"s_cmp_eq_u32 s9, 14336\n"
"s_cselect_b32 s9, 0, s9\n"
"global_load_dword v230, v245, s[12:13] sc1\n"
"v_lshlrev_b32 v246, 16, v32\n"
"v_and_b32 v32, s21, v32\n"
"v_mul_f32 v246, v246, v112\n"
"v_mul_f32 v32, v32, v112\n"
"v_lshlrev_b32 v247, 16, v33\n"
"v_and_b32 v33, s21, v33\n"
"v_mul_f32 v247, v247, v112\n"
"v_mul_f32 v33, v33, v112\n"
"v_lshlrev_b32 v253, 16, v34\n"
"v_and_b32 v34, s21, v34\n"
"v_fmac_f32 v246, v253, v113\n"
"v_fmac_f32 v32, v34, v113\n"
"v_lshlrev_b32 v253, 16, v35\n"
"v_and_b32 v35, s21, v35\n"
"v_fmac_f32 v247, v253, v113\n"
"v_fmac_f32 v33, v35, v113\n"
"v_lshlrev_b32 v253, 16, v36\n"
"v_and_b32 v36, s21, v36\n"
"v_fmac_f32 v246, v253, v114\n"
"v_fmac_f32 v32, v36, v114\n"
"v_lshlrev_b32 v253, 16, v37\n"
"v_and_b32 v37, s21, v37\n"
"v_fmac_f32 v247, v253, v114\n"
"v_fmac_f32 v33, v37, v114\n"
"v_lshlrev_b32 v253, 16, v38\n"
"v_and_b32 v38, s21, v38\n"
"v_fmac_f32 v246, v253, v115\n"
"v_fmac_f32 v32, v38, v115\n"
"v_lshlrev_b32 v253, 16, v39\n"
"v_and_b32 v39, s21, v39\n"
"v_fmac_f32 v247, v253, v115\n"
"v_fmac_f32 v33, v39, v115\n"
"v_lshlrev_b32 v253, 16, v40\n"
"v_and_b32 v40, s21, v40\n"
"v_fmac_f32 v246, v253, v116\n"
"v_fmac_f32 v32, v40, v116\n"
"v_lshlrev_b32 v253, 16, v41\n"
"v_and_b32 v41, s21, v41\n"
"v_fmac_f32 v247, v253, v116\n"
"v_fmac_f32 v33, v41, v116\n"
"v_lshlrev_b32 v253, 16, v42\n"
"v_and_b32 v42, s21, v42\n"
"v_fmac_f32 v246, v253, v117\n"
"v_fmac_f32 v32, v42, v117\n"
"v_lshlrev_b32 v253, 16, v43\n"
"v_and_b32 v43, s21, v43\n"
"v_fmac_f32 v247, v253, v117\n"
"v_fmac_f32 v33, v43, v117\n"
"v_lshlrev_b32 v253, 16, v44\n"
"v_and_b32 v44, s21, v44\n"
"v_fmac_f32 v246, v253, v118\n"
"v_fmac_f32 v32, v44, v118\n"
"v_lshlrev_b32 v253, 16, v45\n"
"v_and_b32 v45, s21, v45\n"
"v_fmac_f32 v247, v253, v118\n"
"v_fmac_f32 v33, v45, v118\n"
"v_lshlrev_b32 v253, 16, v46\n"
"v_and_b32 v46, s21, v46\n"
"v_fmac_f32 v246, v253, v119\n"
"v_fmac_f32 v32, v46, v119\n"
"v_lshlrev_b32 v253, 16, v47\n"
"v_and_b32 v47, s21, v47\n"
"v_fmac_f32 v247, v253, v119\n"
"v_fmac_f32 v33, v47, v119\n"
"v_cvt_pk_bf16_f32 v34, v246, v32\n"
"v_cvt_pk_bf16_f32 v35, v247, v33\n"
"s_add_u32 s23, s10, s11\n"
"buffer_store_dwordx2 v[34:35], v249, s[32:35], s23 offen\n"
"s_add_u32 s11, s11, 128\n"
"s_cmp_eq_u32 s11, 896\n"
"s_cselect_b32 s11, 0, s11\n"
"s_waitcnt vmcnt(33)\n"
"v_lshlrev_b32 v246, 16, v48\n"
"v_and_b32 v48, s21, v48\n"
"v_mul_f32 v246, v246, v112\n"
"v_mul_f32 v48, v48, v112\n"
"v_lshlrev_b32 v247, 16, v49\n"
"v_and_b32 v49, s21, v49\n"
"v_mul_f32 v247, v247, v112\n"
"v_mul_f32 v49, v49, v112\n"
"s_waitcnt vmcnt(32)\n"
"v_lshlrev_b32 v253, 16, v50\n"
"v_and_b32 v50, s21, v50\n"
"v_fmac_f32 v246, v253, v113\n"
"v_fmac_f32 v48, v50, v113\n"
"v_lshlrev_b32 v253, 16, v51\n"
"v_and_b32 v51, s21, v51\n"
"v_fmac_f32 v247, v253, v113\n"
"v_fmac_f32 v49, v51, v113\n"
"s_waitcnt vmcnt(31)\n"
"v_lshlrev_b32 v253, 16, v52\n"
"v_and_b32 v52, s21, v52\n"
"v_fmac_f32 v246, v253, v114\n"
"v_fmac_f32 v48, v52, v114\n"
"v_lshlrev_b32 v253, 16, v53\n"
"v_and_b32 v53, s21, v53\n"
"v_fmac_f32 v247, v253, v114\n"
"v_fmac_f32 v49, v53, v114\n"
"s_waitcnt vmcnt(30)\n"
"v_lshlrev_b32 v253, 16, v54\n"
"v_and_b32 v54, s21, v54\n"
"v_fmac_f32 v246, v253, v115\n"
"v_fmac_f32 v48, v54, v115\n"
"v_lshlrev_b32 v253, 16, v55\n"
"v_and_b32 v55, s21, v55\n"
"v_fmac_f32 v247, v253, v115\n"
"v_fmac_f32 v49, v55, v115\n"
"s_waitcnt vmcnt(29)\n"
"v_lshlrev_b32 v253, 16, v56\n"
"v_and_b32 v56, s21, v56\n"
"v_fmac_f32 v246, v253, v116\n"
"v_fmac_f32 v48, v56, v116\n"
"v_lshlrev_b32 v253, 16, v57\n"
"v_and_b32 v57, s21, v57\n"
"v_fmac_f32 v247, v253, v116\n"
"v_fmac_f32 v49, v57, v116\n"
"s_waitcnt vmcnt(28)\n"
"v_lshlrev_b32 v253, 16, v58\n"
"v_and_b32 v58, s21, v58\n"
"v_fmac_f32 v246, v253, v117\n"
"v_fmac_f32 v48, v58, v117\n"
"v_lshlrev_b32 v253, 16, v59\n"
"v_and_b32 v59, s21, v59\n"
"v_fmac_f32 v247, v253, v117\n"
"v_fmac_f32 v49, v59, v117\n"
"s_waitcnt vmcnt(27)\n"
"v_lshlrev_b32 v253, 16, v60\n"
"v_and_b32 v60, s21, v60\n"
"v_fmac_f32 v246, v253, v118\n"
"v_fmac_f32 v48, v60, v118\n"
"v_lshlrev_b32 v253, 16, v61\n"
"v_and_b32 v61, s21, v61\n"
"v_fmac_f32 v247, v253, v118\n"
"v_fmac_f32 v49, v61, v118\n"
"s_waitcnt vmcnt(26)\n"
"v_lshlrev_b32 v253, 16, v62\n"
"v_and_b32 v62, s21, v62\n"
"v_fmac_f32 v246, v253, v119\n"
"v_fmac_f32 v48, v62, v119\n"
"v_lshlrev_b32 v253, 16, v63\n"
"v_and_b32 v63, s21, v63\n"
"v_fmac_f32 v247, v253, v119\n"
"v_fmac_f32 v49, v63, v119\n"
"v_cvt_pk_bf16_f32 v50, v246, v48\n"
"v_cvt_pk_bf16_f32 v51, v247, v49\n"
"s_add_u32 s23, s10, s11\n"
"buffer_store_dwordx2 v[50:51], v249, s[32:35], s23 offen\n"
"s_add_u32 s11, s11, 128\n"
"s_cmp_eq_u32 s11, 896\n"
"s_cselect_b32 s11, 0, s11\n"
"s_waitcnt vmcnt(26)\n"
"v_lshlrev_b32 v246, 16, v64\n"
"v_and_b32 v64, s21, v64\n"
"v_mul_f32 v246, v246, v112\n"
"v_mul_f32 v64, v64, v112\n"
"v_lshlrev_b32 v247, 16, v65\n"
"v_and_b32 v65, s21, v65\n"
"v_mul_f32 v247, v247, v112\n"
"v_mul_f32 v65, v65, v112\n"
"s_waitcnt vmcnt(25)\n"
"v_lshlrev_b32 v253, 16, v66\n"
"v_and_b32 v66, s21, v66\n"
"v_fmac_f32 v246, v253, v113\n"
"v_fmac_f32 v64, v66, v113\n"
"v_lshlrev_b32 v253, 16, v67\n"
"v_and_b32 v67, s21, v67\n"
"v_fmac_f32 v247, v253, v113\n"
"v_fmac_f32 v65, v67, v113\n"
"s_waitcnt vmcnt(24)\n"
"v_lshlrev_b32 v253, 16, v68\n"
"v_and_b32 v68, s21, v68\n"
"v_fmac_f32 v246, v253, v114\n"
"v_fmac_f32 v64, v68, v114\n"
"v_lshlrev_b32 v253, 16, v69\n"
"v_and_b32 v69, s21, v69\n"
"v_fmac_f32 v247, v253, v114\n"
"v_fmac_f32 v65, v69, v114\n"
"s_waitcnt vmcnt(23)\n"
"v_lshlrev_b32 v253, 16, v70\n"
"v_and_b32 v70, s21, v70\n"
"v_fmac_f32 v246, v253, v115\n"
"v_fmac_f32 v64, v70, v115\n"
"v_lshlrev_b32 v253, 16, v71\n"
"v_and_b32 v71, s21, v71\n"
"v_fmac_f32 v247, v253, v115\n"
"v_fmac_f32 v65, v71, v115\n"
"s_waitcnt vmcnt(22)\n"
"v_lshlrev_b32 v253, 16, v72\n"
"v_and_b32 v72, s21, v72\n"
"v_fmac_f32 v246, v253, v116\n"
"v_fmac_f32 v64, v72, v116\n"
"v_lshlrev_b32 v253, 16, v73\n"
"v_and_b32 v73, s21, v73\n"
"v_fmac_f32 v247, v253, v116\n"
"v_fmac_f32 v65, v73, v116\n"
"s_waitcnt vmcnt(21)\n"
"v_lshlrev_b32 v253, 16, v74\n"
"v_and_b32 v74, s21, v74\n"
"v_fmac_f32 v246, v253, v117\n"
"v_fmac_f32 v64, v74, v117\n"
"v_lshlrev_b32 v253, 16, v75\n"
"v_and_b32 v75, s21, v75\n"
"v_fmac_f32 v247, v253, v117\n"
"v_fmac_f32 v65, v75, v117\n"
"s_waitcnt vmcnt(20)\n"
"v_lshlrev_b32 v253, 16, v76\n"
"v_and_b32 v76, s21, v76\n"
"v_fmac_f32 v246, v253, v118\n"
"v_fmac_f32 v64, v76, v118\n"
"v_lshlrev_b32 v253, 16, v77\n"
"v_and_b32 v77, s21, v77\n"
"v_fmac_f32 v247, v253, v118\n"
"v_fmac_f32 v65, v77, v118\n"
"s_waitcnt vmcnt(19)\n"
"v_lshlrev_b32 v253, 16, v78\n"
"v_and_b32 v78, s21, v78\n"
"v_fmac_f32 v246, v253, v119\n"
"v_fmac_f32 v64, v78, v119\n"
"v_lshlrev_b32 v253, 16, v79\n"
"v_and_b32 v79, s21, v79\n"
"v_fmac_f32 v247, v253, v119\n"
"v_fmac_f32 v65, v79, v119\n"
"v_cvt_pk_bf16_f32 v66, v246, v64\n"
"v_cvt_pk_bf16_f32 v67, v247, v65\n"
"s_add_u32 s23, s10, s11\n"
"buffer_store_dwordx2 v[66:67], v249, s[32:35], s23 offen\n"
"s_add_u32 s11, s11, 128\n"
"s_cmp_eq_u32 s11, 896\n"
"s_cselect_b32 s11, 0, s11\n"
"s_waitcnt vmcnt(19)\n"
"v_lshlrev_b32 v246, 16, v80\n"
"v_and_b32 v80, s21, v80\n"
"v_mul_f32 v246, v246, v112\n"
"v_mul_f32 v80, v80, v112\n"
"v_lshlrev_b32 v247, 16, v81\n"
"v_and_b32 v81, s21, v81\n"
"v_mul_f32 v247, v247, v112\n"
"v_mul_f32 v81, v81, v112\n"
"s_waitcnt vmcnt(18)\n"
"v_lshlrev_b32 v253, 16, v82\n"
"v_and_b32 v82, s21, v82\n"
"v_fmac_f32 v246, v253, v113\n"
"v_fmac_f32 v80, v82, v113\n"
"v_lshlrev_b32 v253, 16, v83\n"
"v_and_b32 v83, s21, v83\n"
"v_fmac_f32 v247, v253, v113\n"
"v_fmac_f32 v81, v83, v113\n"
"s_waitcnt vmcnt(17)\n"
"v_lshlrev_b32 v253, 16, v84\n"
"v_and_b32 v84, s21, v84\n"
"v_fmac_f32 v246, v253, v114\n"
"v_fmac_f32 v80, v84, v114\n"
"v_lshlrev_b32 v253, 16, v85\n"
"v_and_b32 v85, s21, v85\n"
"v_fmac_f32 v247, v253, v114\n"
"v_fmac_f32 v81, v85, v114\n"
"s_waitcnt vmcnt(16)\n"
"v_lshlrev_b32 v253, 16, v86\n"
"v_and_b32 v86, s21, v86\n"
"v_fmac_f32 v246, v253, v115\n"
"v_fmac_f32 v80, v86, v115\n"
"v_lshlrev_b32 v253, 16, v87\n"
"v_and_b32 v87, s21, v87\n"
"v_fmac_f32 v247, v253, v115\n"
"v_fmac_f32 v81, v87, v115\n"
"s_waitcnt vmcnt(15)\n"
"v_lshlrev_b32 v253, 16, v88\n"
"v_and_b32 v88, s21, v88\n"
"v_fmac_f32 v246, v253, v116\n"
"v_fmac_f32 v80, v88, v116\n"
"v_lshlrev_b32 v253, 16, v89\n"
"v_and_b32 v89, s21, v89\n"
"v_fmac_f32 v247, v253, v116\n"
"v_fmac_f32 v81, v89, v116\n"
"s_waitcnt vmcnt(14)\n"
"v_lshlrev_b32 v253, 16, v90\n"
"v_and_b32 v90, s21, v90\n"
"v_fmac_f32 v246, v253, v117\n"
"v_fmac_f32 v80, v90, v117\n"
"v_lshlrev_b32 v253, 16, v91\n"
"v_and_b32 v91, s21, v91\n"
"v_fmac_f32 v247, v253, v117\n"
"v_fmac_f32 v81, v91, v117\n"
"s_waitcnt vmcnt(13)\n"
"v_lshlrev_b32 v253, 16, v92\n"
"v_and_b32 v92, s21, v92\n"
"v_fmac_f32 v246, v253, v118\n"
"v_fmac_f32 v80, v92, v118\n"
"v_lshlrev_b32 v253, 16, v93\n"
"v_and_b32 v93, s21, v93\n"
"v_fmac_f32 v247, v253, v118\n"
"v_fmac_f32 v81, v93, v118\n"
"s_waitcnt vmcnt(12)\n"
"v_lshlrev_b32 v253, 16, v94\n"
"v_and_b32 v94, s21, v94\n"
"v_fmac_f32 v246, v253, v119\n"
"v_fmac_f32 v80, v94, v119\n"
"v_lshlrev_b32 v253, 16, v95\n"
"v_and_b32 v95, s21, v95\n"
"v_fmac_f32 v247, v253, v119\n"
"v_fmac_f32 v81, v95, v119\n"
"v_cvt_pk_bf16_f32 v82, v246, v80\n"
"v_cvt_pk_bf16_f32 v83, v247, v81\n"
"s_add_u32 s23, s10, s11\n"
"buffer_store_dwordx2 v[82:83], v249, s[32:35], s23 offen\n"
"s_add_u32 s11, s11, 128\n"
"s_cmp_eq_u32 s11, 896\n"
"s_cselect_b32 s11, 0, s11\n"
"s_waitcnt vmcnt(12)\n"
"v_lshlrev_b32 v246, 16, v96\n"
"v_and_b32 v96, s21, v96\n"
"v_mul_f32 v246, v246, v112\n"
"v_mul_f32 v96, v96, v112\n"
"v_lshlrev_b32 v247, 16, v97\n"
"v_and_b32 v97, s21, v97\n"
"v_mul_f32 v247, v247, v112\n"
"v_mul_f32 v97, v97, v112\n"
"s_waitcnt vmcnt(11)\n"
"v_lshlrev_b32 v253, 16, v98\n"
"v_and_b32 v98, s21, v98\n"
"v_fmac_f32 v246, v253, v113\n"
"v_fmac_f32 v96, v98, v113\n"
"v_lshlrev_b32 v253, 16, v99\n"
"v_and_b32 v99, s21, v99\n"
"v_fmac_f32 v247, v253, v113\n"
"v_fmac_f32 v97, v99, v113\n"
"s_waitcnt vmcnt(10)\n"
"v_lshlrev_b32 v253, 16, v100\n"
"v_and_b32 v100, s21, v100\n"
"v_fmac_f32 v246, v253, v114\n"
"v_fmac_f32 v96, v100, v114\n"
"v_lshlrev_b32 v253, 16, v101\n"
"v_and_b32 v101, s21, v101\n"
"v_fmac_f32 v247, v253, v114\n"
"v_fmac_f32 v97, v101, v114\n"
"s_waitcnt vmcnt(9)\n"
"v_lshlrev_b32 v253, 16, v102\n"
"v_and_b32 v102, s21, v102\n"
"v_fmac_f32 v246, v253, v115\n"
"v_fmac_f32 v96, v102, v115\n"
"v_lshlrev_b32 v253, 16, v103\n"
"v_and_b32 v103, s21, v103\n"
"v_fmac_f32 v247, v253, v115\n"
"v_fmac_f32 v97, v103, v115\n"
"s_waitcnt vmcnt(8)\n"
"v_lshlrev_b32 v253, 16, v104\n"
"v_and_b32 v104, s21, v104\n"
"v_fmac_f32 v246, v253, v116\n"
"v_fmac_f32 v96, v104, v116\n"
"v_lshlrev_b32 v253, 16, v105\n"
"v_and_b32 v105, s21, v105\n"
"v_fmac_f32 v247, v253, v116\n"
"v_fmac_f32 v97, v105, v116\n"
"s_waitcnt vmcnt(7)\n"
"v_lshlrev_b32 v253, 16, v106\n"
"v_and_b32 v106, s21, v106\n"
"v_fmac_f32 v246, v253, v117\n"
"v_fmac_f32 v96, v106, v117\n"
"v_lshlrev_b32 v253, 16, v107\n"
"v_and_b32 v107, s21, v107\n"
"v_fmac_f32 v247, v253, v117\n"
"v_fmac_f32 v97, v107, v117\n"
"s_waitcnt vmcnt(6)\n"
"v_lshlrev_b32 v253, 16, v108\n"
"v_and_b32 v108, s21, v108\n"
"v_fmac_f32 v246, v253, v118\n"
"v_fmac_f32 v96, v108, v118\n"
"v_lshlrev_b32 v253, 16, v109\n"
"v_and_b32 v109, s21, v109\n"
"v_fmac_f32 v247, v253, v118\n"
"v_fmac_f32 v97, v109, v118\n"
"s_waitcnt vmcnt(5)\n"
"v_lshlrev_b32 v253, 16, v110\n"
"v_and_b32 v110, s21, v110\n"
"v_fmac_f32 v246, v253, v119\n"
"v_fmac_f32 v96, v110, v119\n"
"v_lshlrev_b32 v253, 16, v111\n"
"v_and_b32 v111, s21, v111\n"
"v_fmac_f32 v247, v253, v119\n"
"v_fmac_f32 v97, v111, v119\n"
"v_cvt_pk_bf16_f32 v98, v246, v96\n"
"v_cvt_pk_bf16_f32 v99, v247, v97\n"
"s_add_u32 s23, s10, s11\n"
"buffer_store_dwordx2 v[98:99], v249, s[32:35], s23 offen\n"
"s_add_u32 s11, s11, 128\n"
"s_cmp_eq_u32 s11, 896\n"
"s_cselect_b32 s11, 0, s11\n"
"s_add_u32 s69, s69, 0\n"
"s_max_i32 s15, s69, 0\n"
"s_waitcnt vmcnt(5)\n"
"v_or_b32 v228, s15, v230\n"
"s_mov_b64 exec, s[46:47]\n"
"global_store_dword v245, v228, s[74:75] sc1\n"
"s_mov_b64 exec, -1\n"
"s_branch LEND_%=\n"
"LP10_%=:\n"
"s_nop 15\n"
"s_lshl_b32 s65, s37, 13\n"
"s_sub_u32 s65, s55, s65\n"
"s_sub_u32 s65, s65, s68\n"
"s_add_u32 s96, s65, 8192\n"
"v_and_b32 v192, 15, v253\n"
"v_lshrrev_b32 v193, 4, v253\n"
"v_lshlrev_b32 v192, 9, v192\n"
"v_lshl_add_u32 v192, v193, 4, v192\n"
"buffer_load_dwordx4 v[192:195], v192, s[12:15], s65 offen offset:0\n"
"v_and_b32 v196, 15, v253\n"
"v_lshrrev_b32 v197, 4, v253\n"
"v_lshlrev_b32 v196, 9, v196\n"
"v_lshl_add_u32 v196, v197, 4, v196\n"
"buffer_load_dwordx4 v[196:199], v196, s[12:15], s96 offen offset:0\n"
"v_and_b32 v200, 15, v253\n"
"v_lshrrev_b32 v201, 4, v253\n"
"v_lshlrev_b32 v200, 9, v200\n"
"v_lshl_add_u32 v200, v201, 4, v200\n"
"buffer_load_dwordx4 v[200:203], v200, s[12:15], s65 offen offset:64\n"
"v_and_b32 v204, 15, v253\n"
"v_lshrrev_b32 v205, 4, v253\n"
"v_lshlrev_b32 v204, 9, v204\n"
"v_lshl_add_u32 v204, v205, 4, v204\n"
"buffer_load_dwordx4 v[204:207], v204, s[12:15], s96 offen offset:64\n"
"s_waitcnt vmcnt(0)\n"
"v_mfma_f32_16x16x32_bf16 v[128:131], v[192:195], v[0:3], v[224:227]\n"
"v_mfma_f32_16x16x32_bf16 v[136:139], v[192:195], v[4:7], v[228:231]\n"
"v_mfma_f32_16x16x32_bf16 v[144:147], v[192:195], v[8:11], v[232:235]\n"
"v_mfma_f32_16x16x32_bf16 v[152:155], v[192:195], v[12:15], v[236:239]\n"
"v_mfma_f32_16x16x32_bf16 v[132:135], v[196:199], v[0:3], v[224:227]\n"
"v_mfma_f32_16x16x32_bf16 v[140:143], v[196:199], v[4:7], v[228:231]\n"
"v_mfma_f32_16x16x32_bf16 v[148:151], v[196:199], v[8:11], v[232:235]\n"
"v_mfma_f32_16x16x32_bf16 v[156:159], v[196:199], v[12:15], v[236:239]\n"
"v_mfma_f32_16x16x32_bf16 v[128:131], v[200:203], v[16:19], v[128:131]\n"
"v_mfma_f32_16x16x32_bf16 v[136:139], v[200:203], v[20:23], v[136:139]\n"
"v_mfma_f32_16x16x32_bf16 v[144:147], v[200:203], v[24:27], v[144:147]\n"
"v_mfma_f32_16x16x32_bf16 v[152:155], v[200:203], v[28:31], v[152:155]\n"
"v_mfma_f32_16x16x32_bf16 v[132:135], v[204:207], v[16:19], v[132:135]\n"
"v_mfma_f32_16x16x32_bf16 v[140:143], v[204:207], v[20:23], v[140:143]\n"
"v_mfma_f32_16x16x32_bf16 v[148:151], v[204:207], v[24:27], v[148:151]\n"
"v_mfma_f32_16x16x32_bf16 v[156:159], v[204:207], v[28:31], v[156:159]\n"
"s_nop 15\n"
"v_and_b32 v192, 15, v253\n"
"v_lshrrev_b32 v193, 4, v253\n"
"v_lshlrev_b32 v192, 9, v192\n"
"v_lshl_add_u32 v192, v193, 4, v192\n"
"buffer_load_dwordx4 v[192:195], v192, s[12:15], s65 offen offset:128\n"
"v_and_b32 v196, 15, v253\n"
"v_lshrrev_b32 v197, 4, v253\n"
"v_lshlrev_b32 v196, 9, v196\n"
"v_lshl_add_u32 v196, v197, 4, v196\n"
"buffer_load_dwordx4 v[196:199], v196, s[12:15], s96 offen offset:128\n"
"v_and_b32 v200, 15, v253\n"
"v_lshrrev_b32 v201, 4, v253\n"
"v_lshlrev_b32 v200, 9, v200\n"
"v_lshl_add_u32 v200, v201, 4, v200\n"
"buffer_load_dwordx4 v[200:203], v200, s[12:15], s65 offen offset:192\n"
"v_and_b32 v204, 15, v253\n"
"v_lshrrev_b32 v205, 4, v253\n"
"v_lshlrev_b32 v204, 9, v204\n"
"v_lshl_add_u32 v204, v205, 4, v204\n"
"buffer_load_dwordx4 v[204:207], v204, s[12:15], s96 offen offset:192\n"
"s_waitcnt vmcnt(0)\n"
"v_mfma_f32_16x16x32_bf16 v[128:131], v[192:195], v[32:35], v[128:131]\n"
"v_mfma_f32_16x16x32_bf16 v[136:139], v[192:195], v[36:39], v[136:139]\n"
"v_mfma_f32_16x16x32_bf16 v[144:147], v[192:195], v[40:43], v[144:147]\n"
"v_mfma_f32_16x16x32_bf16 v[152:155], v[192:195], v[44:47], v[152:155]\n"
"v_mfma_f32_16x16x32_bf16 v[132:135], v[196:199], v[32:35], v[132:135]\n"
"v_mfma_f32_16x16x32_bf16 v[140:143], v[196:199], v[36:39], v[140:143]\n"
"v_mfma_f32_16x16x32_bf16 v[148:151], v[196:199], v[40:43], v[148:151]\n"
"v_mfma_f32_16x16x32_bf16 v[156:159], v[196:199], v[44:47], v[156:159]\n"
"v_mfma_f32_16x16x32_bf16 v[128:131], v[200:203], v[48:51], v[128:131]\n"
"v_mfma_f32_16x16x32_bf16 v[136:139], v[200:203], v[52:55], v[136:139]\n"
"v_mfma_f32_16x16x32_bf16 v[144:147], v[200:203], v[56:59], v[144:147]\n"
"v_mfma_f32_16x16x32_bf16 v[152:155], v[200:203], v[60:63], v[152:155]\n"
"v_mfma_f32_16x16x32_bf16 v[132:135], v[204:207], v[48:51], v[132:135]\n"
"v_mfma_f32_16x16x32_bf16 v[140:143], v[204:207], v[52:55], v[140:143]\n"
"v_mfma_f32_16x16x32_bf16 v[148:151], v[204:207], v[56:59], v[148:151]\n"
"v_mfma_f32_16x16x32_bf16 v[156:159], v[204:207], v[60:63], v[156:159]\n"
"s_nop 15\n"
"v_and_b32 v192, 15, v253\n"
"v_lshrrev_b32 v193, 4, v253\n"
"v_lshlrev_b32 v192, 9, v192\n"
"v_lshl_add_u32 v192, v193, 4, v192\n"
"buffer_load_dwordx4 v[192:195], v192, s[12:15], s65 offen offset:256\n"
"v_and_b32 v196, 15, v253\n"
"v_lshrrev_b32 v197, 4, v253\n"
"v_lshlrev_b32 v196, 9, v196\n"
"v_lshl_add_u32 v196, v197, 4, v196\n"
"buffer_load_dwordx4 v[196:199], v196, s[12:15], s96 offen offset:256\n"
"v_and_b32 v200, 15, v253\n"
"v_lshrrev_b32 v201, 4, v253\n"
"v_lshlrev_b32 v200, 9, v200\n"
"v_lshl_add_u32 v200, v201, 4, v200\n"
"buffer_load_dwordx4 v[200:203], v200, s[12:15], s65 offen offset:320\n"
"v_and_b32 v204, 15, v253\n"
"v_lshrrev_b32 v205, 4, v253\n"
"v_lshlrev_b32 v204, 9, v204\n"
"v_lshl_add_u32 v204, v205, 4, v204\n"
"buffer_load_dwordx4 v[204:207], v204, s[12:15], s96 offen offset:320\n"
"s_waitcnt vmcnt(0)\n"
"v_mfma_f32_16x16x32_bf16 v[128:131], v[192:195], v[64:67], v[128:131]\n"
"v_mfma_f32_16x16x32_bf16 v[136:139], v[192:195], v[68:71], v[136:139]\n"
"v_mfma_f32_16x16x32_bf16 v[144:147], v[192:195], v[72:75], v[144:147]\n"
"v_mfma_f32_16x16x32_bf16 v[152:155], v[192:195], v[76:79], v[152:155]\n"
"v_mfma_f32_16x16x32_bf16 v[132:135], v[196:199], v[64:67], v[132:135]\n"
"v_mfma_f32_16x16x32_bf16 v[140:143], v[196:199], v[68:71], v[140:143]\n"
"v_mfma_f32_16x16x32_bf16 v[148:151], v[196:199], v[72:75], v[148:151]\n"
"v_mfma_f32_16x16x32_bf16 v[156:159], v[196:199], v[76:79], v[156:159]\n"
"v_mfma_f32_16x16x32_bf16 v[128:131], v[200:203], v[80:83], v[128:131]\n"
"v_mfma_f32_16x16x32_bf16 v[136:139], v[200:203], v[84:87], v[136:139]\n"
"v_mfma_f32_16x16x32_bf16 v[144:147], v[200:203], v[88:91], v[144:147]\n"
"v_mfma_f32_16x16x32_bf16 v[152:155], v[200:203], v[92:95], v[152:155]\n"
"v_mfma_f32_16x16x32_bf16 v[132:135], v[204:207], v[80:83], v[132:135]\n"
"v_mfma_f32_16x16x32_bf16 v[140:143], v[204:207], v[84:87], v[140:143]\n"
"v_mfma_f32_16x16x32_bf16 v[148:151], v[204:207], v[88:91], v[148:151]\n"
"v_mfma_f32_16x16x32_bf16 v[156:159], v[204:207], v[92:95], v[156:159]\n"
"s_nop 15\n"
"v_and_b32 v192, 15, v253\n"
"v_lshrrev_b32 v193, 4, v253\n"
"v_lshlrev_b32 v192, 9, v192\n"
"v_lshl_add_u32 v192, v193, 4, v192\n"
"buffer_load_dwordx4 v[192:195], v192, s[12:15], s65 offen offset:384\n"
"v_and_b32 v196, 15, v253\n"
"v_lshrrev_b32 v197, 4, v253\n"
"v_lshlrev_b32 v196, 9, v196\n"
"v_lshl_add_u32 v196, v197, 4, v196\n"
"buffer_load_dwordx4 v[196:199], v196, s[12:15], s96 offen offset:384\n"
"v_and_b32 v200, 15, v253\n"
"v_lshrrev_b32 v201, 4, v253\n"
"v_lshlrev_b32 v200, 9, v200\n"
"v_lshl_add_u32 v200, v201, 4, v200\n"
"buffer_load_dwordx4 v[200:203], v200, s[12:15], s65 offen offset:448\n"
"v_and_b32 v204, 15, v253\n"
"v_lshrrev_b32 v205, 4, v253\n"
"v_lshlrev_b32 v204, 9, v204\n"
"v_lshl_add_u32 v204, v205, 4, v204\n"
"buffer_load_dwordx4 v[204:207], v204, s[12:15], s96 offen offset:448\n"
"s_waitcnt vmcnt(0)\n"
"v_mfma_f32_16x16x32_bf16 v[128:131], v[192:195], v[96:99], v[128:131]\n"
"v_mfma_f32_16x16x32_bf16 v[136:139], v[192:195], v[100:103], v[136:139]\n"
"v_mfma_f32_16x16x32_bf16 v[144:147], v[192:195], v[104:107], v[144:147]\n"
"v_mfma_f32_16x16x32_bf16 v[152:155], v[192:195], v[108:111], v[152:155]\n"
"v_mfma_f32_16x16x32_bf16 v[132:135], v[196:199], v[96:99], v[132:135]\n"
"v_mfma_f32_16x16x32_bf16 v[140:143], v[196:199], v[100:103], v[140:143]\n"
"v_mfma_f32_16x16x32_bf16 v[148:151], v[196:199], v[104:107], v[148:151]\n"
"v_mfma_f32_16x16x32_bf16 v[156:159], v[196:199], v[108:111], v[156:159]\n"
"v_mfma_f32_16x16x32_bf16 v[128:131], v[200:203], v[112:115], v[128:131]\n"
"v_mfma_f32_16x16x32_bf16 v[136:139], v[200:203], v[116:119], v[136:139]\n"
"v_mfma_f32_16x16x32_bf16 v[144:147], v[200:203], v[120:123], v[144:147]\n"
"v_mfma_f32_16x16x32_bf16 v[152:155], v[200:203], v[124:127], v[152:155]\n"
"v_mfma_f32_16x16x32_bf16 v[132:135], v[204:207], v[112:115], v[132:135]\n"
"v_mfma_f32_16x16x32_bf16 v[140:143], v[204:207], v[116:119], v[140:143]\n"
"v_mfma_f32_16x16x32_bf16 v[148:151], v[204:207], v[120:123], v[148:151]\n"
"v_mfma_f32_16x16x32_bf16 v[156:159], v[204:207], v[124:127], v[156:159]\n"
"s_nop 15\n"
"v_max3_f32 v192, v128, v129, v130\n"
"v_max3_f32 v193, v136, v137, v138\n"
"v_max3_f32 v194, v144, v145, v146\n"
"v_max3_f32 v195, v152, v153, v154\n"
"v_max3_f32 v192, v192, v131, v132\n"
"v_max3_f32 v193, v193, v139, v140\n"
"v_max3_f32 v194, v194, v147, v148\n"
"v_max3_f32 v195, v195, v155, v156\n"
"v_max3_f32 v192, v192, v133, v134\n"
"v_max3_f32 v193, v193, v141, v142\n"
"v_max3_f32 v194, v194, v149, v150\n"
"v_max3_f32 v195, v195, v157, v158\n"
"v_max_f32 v192, v192, v135\n"
"v_max_f32 v193, v193, v143\n"
"v_max_f32 v194, v194, v151\n"
"v_max_f32 v195, v195, v159\n"
"v_mov_b32 v196, v192\n"
"v_mov_b32 v197, v193\n"
"v_mov_b32 v198, v194\n"
"v_mov_b32 v199, v195\n"
"v_permlane32_swap_b32 v192, v196\n"
"v_permlane32_swap_b32 v193, v197\n"
"v_permlane32_swap_b32 v194, v198\n"
"v_permlane32_swap_b32 v195, v199\n"
"v_max_f32 v192, v192, v196\n"
"v_max_f32 v193, v193, v197\n"
"v_max_f32 v194, v194, v198\n"
"v_max_f32 v195, v195, v199\n"
"v_mov_b32 v196, v192\n"
"v_mov_b32 v197, v193\n"
"v_mov_b32 v198, v194\n"
"v_mov_b32 v199, v195\n"
"v_permlane16_swap_b32 v192, v196\n"
"v_permlane16_swap_b32 v193, v197\n"
"v_permlane16_swap_b32 v194, v198\n"
"v_permlane16_swap_b32 v195, v199\n"
"v_max_f32 v192, v192, v196\n"
"v_max_f32 v193, v193, v197\n"
"v_max_f32 v194, v194, v198\n"
"v_max_f32 v195, v195, v199\n"
"v_max_f32 v192, 0, v192\n"
"v_sub_f32 v196, 0, v192\n"
"v_exp_f32 v196, v196\n"
"v_sub_f32 v224, v224, v192\n"
"v_sub_f32 v225, v225, v192\n"
"v_sub_f32 v226, v226, v192\n"
"v_sub_f32 v227, v227, v192\n"
"v_sub_f32 v128, v128, v192\n"
"v_sub_f32 v129, v129, v192\n"
"v_sub_f32 v130, v130, v192\n"
"v_sub_f32 v131, v131, v192\n"
"v_sub_f32 v132, v132, v192\n"
"v_sub_f32 v133, v133, v192\n"
"v_sub_f32 v134, v134, v192\n"
"v_sub_f32 v135, v135, v192\n"
"v_sub_f32 v160, v160, v192\n"
"v_sub_f32 v161, v161, v192\n"
"v_sub_f32 v162, v162, v192\n"
"v_sub_f32 v163, v163, v192\n"
"v_sub_f32 v164, v164, v192\n"
"v_sub_f32 v165, v165, v192\n"
"v_sub_f32 v166, v166, v192\n"
"v_sub_f32 v167, v167, v192\n"
"v_mul_f32 v240, v240, v196\n"
"v_max_f32 v193, 0, v193\n"
"v_sub_f32 v197, 0, v193\n"
"v_exp_f32 v197, v197\n"
"v_sub_f32 v228, v228, v193\n"
"v_sub_f32 v229, v229, v193\n"
"v_sub_f32 v230, v230, v193\n"
"v_sub_f32 v231, v231, v193\n"
"v_sub_f32 v136, v136, v193\n"
"v_sub_f32 v137, v137, v193\n"
"v_sub_f32 v138, v138, v193\n"
"v_sub_f32 v139, v139, v193\n"
"v_sub_f32 v140, v140, v193\n"
"v_sub_f32 v141, v141, v193\n"
"v_sub_f32 v142, v142, v193\n"
"v_sub_f32 v143, v143, v193\n"
"v_sub_f32 v168, v168, v193\n"
"v_sub_f32 v169, v169, v193\n"
"v_sub_f32 v170, v170, v193\n"
"v_sub_f32 v171, v171, v193\n"
"v_sub_f32 v172, v172, v193\n"
"v_sub_f32 v173, v173, v193\n"
"v_sub_f32 v174, v174, v193\n"
"v_sub_f32 v175, v175, v193\n"
"v_mul_f32 v241, v241, v197\n"
"v_max_f32 v194, 0, v194\n"
"v_sub_f32 v198, 0, v194\n"
"v_exp_f32 v198, v198\n"
"v_sub_f32 v232, v232, v194\n"
"v_sub_f32 v233, v233, v194\n"
"v_sub_f32 v234, v234, v194\n"
"v_sub_f32 v235, v235, v194\n"
"v_sub_f32 v144, v144, v194\n"
"v_sub_f32 v145, v145, v194\n"
"v_sub_f32 v146, v146, v194\n"
"v_sub_f32 v147, v147, v194\n"
"v_sub_f32 v148, v148, v194\n"
"v_sub_f32 v149, v149, v194\n"
"v_sub_f32 v150, v150, v194\n"
"v_sub_f32 v151, v151, v194\n"
"v_sub_f32 v176, v176, v194\n"
"v_sub_f32 v177, v177, v194\n"
"v_sub_f32 v178, v178, v194\n"
"v_sub_f32 v179, v179, v194\n"
"v_sub_f32 v180, v180, v194\n"
"v_sub_f32 v181, v181, v194\n"
"v_sub_f32 v182, v182, v194\n"
"v_sub_f32 v183, v183, v194\n"
"v_mul_f32 v242, v242, v198\n"
"v_max_f32 v195, 0, v195\n"
"v_sub_f32 v199, 0, v195\n"
"v_exp_f32 v199, v199\n"
"v_sub_f32 v236, v236, v195\n"
"v_sub_f32 v237, v237, v195\n"
"v_sub_f32 v238, v238, v195\n"
"v_sub_f32 v239, v239, v195\n"
"v_sub_f32 v152, v152, v195\n"
"v_sub_f32 v153, v153, v195\n"
"v_sub_f32 v154, v154, v195\n"
"v_sub_f32 v155, v155, v195\n"
"v_sub_f32 v156, v156, v195\n"
"v_sub_f32 v157, v157, v195\n"
"v_sub_f32 v158, v158, v195\n"
"v_sub_f32 v159, v159, v195\n"
"v_sub_f32 v184, v184, v195\n"
"v_sub_f32 v185, v185, v195\n"
"v_sub_f32 v186, v186, v195\n"
"v_sub_f32 v187, v187, v195\n"
"v_sub_f32 v188, v188, v195\n"
"v_sub_f32 v189, v189, v195\n"
"v_sub_f32 v190, v190, v195\n"
"v_sub_f32 v191, v191, v195\n"
"v_mul_f32 v243, v243, v199\n"
"s_getpc_b64 s[90:91]\n"
"s_branch LP2_%=\n"
"v_exp_f32 v128, v128\n"
"v_exp_f32 v129, v129\n"
"v_exp_f32 v130, v130\n"
"v_exp_f32 v131, v131\n"
"v_exp_f32 v132, v132\n"
"v_exp_f32 v133, v133\n"
"v_exp_f32 v134, v134\n"
"v_exp_f32 v135, v135\n"
"v_exp_f32 v136, v136\n"
"v_cvt_pk_bf16_f32 v192, v128, v129\n"
"v_add_f32 v128, v128, v129\n"
"v_exp_f32 v137, v137\n"
"v_cvt_pk_bf16_f32 v193, v130, v131\n"
"v_exp_f32 v138, v138\n"
"v_add_f32 v130, v130, v131\n"
"v_cvt_pk_bf16_f32 v194, v132, v133\n"
"v_exp_f32 v139, v139\n"
"v_add_f32 v132, v132, v133\n"
"v_exp_f32 v140, v140\n"
"v_add_f32 v128, v128, v130\n"
"v_exp_f32 v141, v141\n"
"v_cvt_pk_bf16_f32 v195, v134, v135\n"
"v_add_f32 v134, v134, v135\n"
"v_exp_f32 v142, v142\n"
"v_add_f32 v128, v128, v132\n"
"v_exp_f32 v143, v143\n"
"v_add_f32 v128, v128, v134\n"
"v_exp_f32 v144, v144\n"
"v_cvt_pk_bf16_f32 v196, v136, v137\n"
"v_add_f32 v136, v136, v137\n"
"v_exp_f32 v145, v145\n"
"v_cvt_pk_bf16_f32 v197, v138, v139\n"
"v_exp_f32 v146, v146\n"
"v_add_f32 v138, v138, v139\n"
"v_cvt_pk_bf16_f32 v198, v140, v141\n"
"v_exp_f32 v147, v147\n"
"v_add_f32 v140, v140, v141\n"
"v_exp_f32 v148, v148\n"
"v_add_f32 v136, v136, v138\n"
"v_exp_f32 v149, v149\n"
"v_cvt_pk_bf16_f32 v199, v142, v143\n"
"v_add_f32 v142, v142, v143\n"
"v_exp_f32 v150, v150\n"
"v_add_f32 v136, v136, v140\n"
"v_exp_f32 v151, v151\n"
"v_add_f32 v136, v136, v142\n"
"v_exp_f32 v152, v152\n"
"v_cvt_pk_bf16_f32 v200, v144, v145\n"
"v_add_f32 v144, v144, v145\n"
"v_exp_f32 v153, v153\n"
"v_cvt_pk_bf16_f32 v201, v146, v147\n"
"v_exp_f32 v154, v154\n"
"v_add_f32 v146, v146, v147\n"
"v_cvt_pk_bf16_f32 v202, v148, v149\n"
"v_exp_f32 v155, v155\n"
"v_add_f32 v148, v148, v149\n"
"v_exp_f32 v156, v156\n"
"v_add_f32 v144, v144, v146\n"
"v_exp_f32 v157, v157\n"
"v_cvt_pk_bf16_f32 v203, v150, v151\n"
"v_add_f32 v150, v150, v151\n"
"v_exp_f32 v158, v158\n"
"v_add_f32 v144, v144, v148\n"
"v_exp_f32 v159, v159\n"
"v_add_f32 v144, v144, v150\n"
"v_cvt_pk_bf16_f32 v204, v152, v153\n"
"v_add_f32 v152, v152, v153\n"
"v_cvt_pk_bf16_f32 v205, v154, v155\n"
"v_add_f32 v154, v154, v155\n"
"v_cvt_pk_bf16_f32 v206, v156, v157\n"
"v_add_f32 v156, v156, v157\n"
"v_add_f32 v152, v152, v154\n"
"v_cvt_pk_bf16_f32 v207, v158, v159\n"
"v_add_f32 v158, v158, v159\n"
"v_add_f32 v152, v152, v156\n"
"v_add_f32 v152, v152, v158\n"
"s_add_u32 s72, s72, 4\n"
"s_addc_u32 s73, s73, 0\n"
"s_setpc_b64 s[72:73]\n"
"LP11_%=:\n"
"s_nop 15\n"
"s_lshl_b32 s65, s37, 13\n"
"s_sub_u32 s65, s55, s65\n"
"s_sub_u32 s65, s65, s68\n"
"s_add_u32 s96, s65, 8192\n"
"v_and_b32 v192, 15, v253\n"
"v_lshrrev_b32 v193, 4, v253\n"
"v_lshlrev_b32 v192, 9, v192\n"
"v_lshl_add_u32 v192, v193, 4, v192\n"
"buffer_load_dwordx4 v[192:195], v192, s[12:15], s65 offen offset:0\n"
"v_and_b32 v196, 15, v253\n"
"v_lshrrev_b32 v197, 4, v253\n"
"v_lshlrev_b32 v196, 9, v196\n"
"v_lshl_add_u32 v196, v197, 4, v196\n"
"buffer_load_dwordx4 v[196:199], v196, s[12:15], s96 offen offset:0\n"
"v_and_b32 v200, 15, v253\n"
"v_lshrrev_b32 v201, 4, v253\n"
"v_lshlrev_b32 v200, 9, v200\n"
"v_lshl_add_u32 v200, v201, 4, v200\n"
"buffer_load_dwordx4 v[200:203], v200, s[12:15], s65 offen offset:64\n"
"v_and_b32 v204, 15, v253\n"
"v_lshrrev_b32 v205, 4, v253\n"
"v_lshlrev_b32 v204, 9, v204\n"
"v_lshl_add_u32 v204, v205, 4, v204\n"
"buffer_load_dwordx4 v[204:207], v204, s[12:15], s96 offen offset:64\n"
"s_waitcnt vmcnt(0)\n"
"v_mfma_f32_16x16x32_bf16 v[160:163], v[192:195], v[0:3], v[224:227]\n"
"v_mfma_f32_16x16x32_bf16 v[168:171], v[192:195], v[4:7], v[228:231]\n"
"v_mfma_f32_16x16x32_bf16 v[176:179], v[192:195], v[8:11], v[232:235]\n"
"v_mfma_f32_16x16x32_bf16 v[184:187], v[192:195], v[12:15], v[236:239]\n"
"v_mfma_f32_16x16x32_bf16 v[164:167], v[196:199], v[0:3], v[224:227]\n"
"v_mfma_f32_16x16x32_bf16 v[172:175], v[196:199], v[4:7], v[228:231]\n"
"v_mfma_f32_16x16x32_bf16 v[180:183], v[196:199], v[8:11], v[232:235]\n"
"v_mfma_f32_16x16x32_bf16 v[188:191], v[196:199], v[12:15], v[236:239]\n"
"v_mfma_f32_16x16x32_bf16 v[160:163], v[200:203], v[16:19], v[160:163]\n"
"v_mfma_f32_16x16x32_bf16 v[168:171], v[200:203], v[20:23], v[168:171]\n"
"v_mfma_f32_16x16x32_bf16 v[176:179], v[200:203], v[24:27], v[176:179]\n"
"v_mfma_f32_16x16x32_bf16 v[184:187], v[200:203], v[28:31], v[184:187]\n"
"v_mfma_f32_16x16x32_bf16 v[164:167], v[204:207], v[16:19], v[164:167]\n"
"v_mfma_f32_16x16x32_bf16 v[172:175], v[204:207], v[20:23], v[172:175]\n"
"v_mfma_f32_16x16x32_bf16 v[180:183], v[204:207], v[24:27], v[180:183]\n"
"v_mfma_f32_16x16x32_bf16 v[188:191], v[204:207], v[28:31], v[188:191]\n"
"s_nop 15\n"
"v_and_b32 v192, 15, v253\n"
"v_lshrrev_b32 v193, 4, v253\n"
"v_lshlrev_b32 v192, 9, v192\n"
"v_lshl_add_u32 v192, v193, 4, v192\n"
"buffer_load_dwordx4 v[192:195], v192, s[12:15], s65 offen offset:128\n"
"v_and_b32 v196, 15, v253\n"
"v_lshrrev_b32 v197, 4, v253\n"
"v_lshlrev_b32 v196, 9, v196\n"
"v_lshl_add_u32 v196, v197, 4, v196\n"
"buffer_load_dwordx4 v[196:199], v196, s[12:15], s96 offen offset:128\n"
"v_and_b32 v200, 15, v253\n"
"v_lshrrev_b32 v201, 4, v253\n"
"v_lshlrev_b32 v200, 9, v200\n"
"v_lshl_add_u32 v200, v201, 4, v200\n"
"buffer_load_dwordx4 v[200:203], v200, s[12:15], s65 offen offset:192\n"
"v_and_b32 v204, 15, v253\n"
"v_lshrrev_b32 v205, 4, v253\n"
"v_lshlrev_b32 v204, 9, v204\n"
"v_lshl_add_u32 v204, v205, 4, v204\n"
"buffer_load_dwordx4 v[204:207], v204, s[12:15], s96 offen offset:192\n"
"s_waitcnt vmcnt(0)\n"
"v_mfma_f32_16x16x32_bf16 v[160:163], v[192:195], v[32:35], v[160:163]\n"
"v_mfma_f32_16x16x32_bf16 v[168:171], v[192:195], v[36:39], v[168:171]\n"
"v_mfma_f32_16x16x32_bf16 v[176:179], v[192:195], v[40:43], v[176:179]\n"
"v_mfma_f32_16x16x32_bf16 v[184:187], v[192:195], v[44:47], v[184:187]\n"
"v_mfma_f32_16x16x32_bf16 v[164:167], v[196:199], v[32:35], v[164:167]\n"
"v_mfma_f32_16x16x32_bf16 v[172:175], v[196:199], v[36:39], v[172:175]\n"
"v_mfma_f32_16x16x32_bf16 v[180:183], v[196:199], v[40:43], v[180:183]\n"
"v_mfma_f32_16x16x32_bf16 v[188:191], v[196:199], v[44:47], v[188:191]\n"
"v_mfma_f32_16x16x32_bf16 v[160:163], v[200:203], v[48:51], v[160:163]\n"
"v_mfma_f32_16x16x32_bf16 v[168:171], v[200:203], v[52:55], v[168:171]\n"
"v_mfma_f32_16x16x32_bf16 v[176:179], v[200:203], v[56:59], v[176:179]\n"
"v_mfma_f32_16x16x32_bf16 v[184:187], v[200:203], v[60:63], v[184:187]\n"
"v_mfma_f32_16x16x32_bf16 v[164:167], v[204:207], v[48:51], v[164:167]\n"
"v_mfma_f32_16x16x32_bf16 v[172:175], v[204:207], v[52:55], v[172:175]\n"
"v_mfma_f32_16x16x32_bf16 v[180:183], v[204:207], v[56:59], v[180:183]\n"
"v_mfma_f32_16x16x32_bf16 v[188:191], v[204:207], v[60:63], v[188:191]\n"
"s_nop 15\n"
"v_and_b32 v192, 15, v253\n"
"v_lshrrev_b32 v193, 4, v253\n"
"v_lshlrev_b32 v192, 9, v192\n"
"v_lshl_add_u32 v192, v193, 4, v192\n"
"buffer_load_dwordx4 v[192:195], v192, s[12:15], s65 offen offset:256\n"
"v_and_b32 v196, 15, v253\n"
"v_lshrrev_b32 v197, 4, v253\n"
"v_lshlrev_b32 v196, 9, v196\n"
"v_lshl_add_u32 v196, v197, 4, v196\n"
"buffer_load_dwordx4 v[196:199], v196, s[12:15], s96 offen offset:256\n"
"v_and_b32 v200, 15, v253\n"
"v_lshrrev_b32 v201, 4, v253\n"
"v_lshlrev_b32 v200, 9, v200\n"
"v_lshl_add_u32 v200, v201, 4, v200\n"
"buffer_load_dwordx4 v[200:203], v200, s[12:15], s65 offen offset:320\n"
"v_and_b32 v204, 15, v253\n"
"v_lshrrev_b32 v205, 4, v253\n"
"v_lshlrev_b32 v204, 9, v204\n"
"v_lshl_add_u32 v204, v205, 4, v204\n"
"buffer_load_dwordx4 v[204:207], v204, s[12:15], s96 offen offset:320\n"
"s_waitcnt vmcnt(0)\n"
"v_mfma_f32_16x16x32_bf16 v[160:163], v[192:195], v[64:67], v[160:163]\n"
"v_mfma_f32_16x16x32_bf16 v[168:171], v[192:195], v[68:71], v[168:171]\n"
"v_mfma_f32_16x16x32_bf16 v[176:179], v[192:195], v[72:75], v[176:179]\n"
"v_mfma_f32_16x16x32_bf16 v[184:187], v[192:195], v[76:79], v[184:187]\n"
"v_mfma_f32_16x16x32_bf16 v[164:167], v[196:199], v[64:67], v[164:167]\n"
"v_mfma_f32_16x16x32_bf16 v[172:175], v[196:199], v[68:71], v[172:175]\n"
"v_mfma_f32_16x16x32_bf16 v[180:183], v[196:199], v[72:75], v[180:183]\n"
"v_mfma_f32_16x16x32_bf16 v[188:191], v[196:199], v[76:79], v[188:191]\n"
"v_mfma_f32_16x16x32_bf16 v[160:163], v[200:203], v[80:83], v[160:163]\n"
"v_mfma_f32_16x16x32_bf16 v[168:171], v[200:203], v[84:87], v[168:171]\n"
"v_mfma_f32_16x16x32_bf16 v[176:179], v[200:203], v[88:91], v[176:179]\n"
"v_mfma_f32_16x16x32_bf16 v[184:187], v[200:203], v[92:95], v[184:187]\n"
"v_mfma_f32_16x16x32_bf16 v[164:167], v[204:207], v[80:83], v[164:167]\n"
"v_mfma_f32_16x16x32_bf16 v[172:175], v[204:207], v[84:87], v[172:175]\n"
"v_mfma_f32_16x16x32_bf16 v[180:183], v[204:207], v[88:91], v[180:183]\n"
"v_mfma_f32_16x16x32_bf16 v[188:191], v[204:207], v[92:95], v[188:191]\n"
"s_nop 15\n"
"v_and_b32 v192, 15, v253\n"
"v_lshrrev_b32 v193, 4, v253\n"
"v_lshlrev_b32 v192, 9, v192\n"
"v_lshl_add_u32 v192, v193, 4, v192\n"
"buffer_load_dwordx4 v[192:195], v192, s[12:15], s65 offen offset:384\n"
"v_and_b32 v196, 15, v253\n"
"v_lshrrev_b32 v197, 4, v253\n"
"v_lshlrev_b32 v196, 9, v196\n"
"v_lshl_add_u32 v196, v197, 4, v196\n"
"buffer_load_dwordx4 v[196:199], v196, s[12:15], s96 offen offset:384\n"
"v_and_b32 v200, 15, v253\n"
"v_lshrrev_b32 v201, 4, v253\n"
"v_lshlrev_b32 v200, 9, v200\n"
"v_lshl_add_u32 v200, v201, 4, v200\n"
"buffer_load_dwordx4 v[200:203], v200, s[12:15], s65 offen offset:448\n"
"v_and_b32 v204, 15, v253\n"
"v_lshrrev_b32 v205, 4, v253\n"
"v_lshlrev_b32 v204, 9, v204\n"
"v_lshl_add_u32 v204, v205, 4, v204\n"
"buffer_load_dwordx4 v[204:207], v204, s[12:15], s96 offen offset:448\n"
"s_waitcnt vmcnt(0)\n"
"v_mfma_f32_16x16x32_bf16 v[160:163], v[192:195], v[96:99], v[160:163]\n"
"v_mfma_f32_16x16x32_bf16 v[168:171], v[192:195], v[100:103], v[168:171]\n"
"v_mfma_f32_16x16x32_bf16 v[176:179], v[192:195], v[104:107], v[176:179]\n"
"v_mfma_f32_16x16x32_bf16 v[184:187], v[192:195], v[108:111], v[184:187]\n"
"v_mfma_f32_16x16x32_bf16 v[164:167], v[196:199], v[96:99], v[164:167]\n"
"v_mfma_f32_16x16x32_bf16 v[172:175], v[196:199], v[100:103], v[172:175]\n"
"v_mfma_f32_16x16x32_bf16 v[180:183], v[196:199], v[104:107], v[180:183]\n"
"v_mfma_f32_16x16x32_bf16 v[188:191], v[196:199], v[108:111], v[188:191]\n"
"v_mfma_f32_16x16x32_bf16 v[160:163], v[200:203], v[112:115], v[160:163]\n"
"v_mfma_f32_16x16x32_bf16 v[168:171], v[200:203], v[116:119], v[168:171]\n"
"v_mfma_f32_16x16x32_bf16 v[176:179], v[200:203], v[120:123], v[176:179]\n"
"v_mfma_f32_16x16x32_bf16 v[184:187], v[200:203], v[124:127], v[184:187]\n"
"v_mfma_f32_16x16x32_bf16 v[164:167], v[204:207], v[112:115], v[164:167]\n"
"v_mfma_f32_16x16x32_bf16 v[172:175], v[204:207], v[116:119], v[172:175]\n"
"v_mfma_f32_16x16x32_bf16 v[180:183], v[204:207], v[120:123], v[180:183]\n"
"v_mfma_f32_16x16x32_bf16 v[188:191], v[204:207], v[124:127], v[188:191]\n"
"s_nop 15\n"
"v_max3_f32 v192, v160, v161, v162\n"
"v_max3_f32 v193, v168, v169, v170\n"
"v_max3_f32 v194, v176, v177, v178\n"
"v_max3_f32 v195, v184, v185, v186\n"
"v_max3_f32 v192, v192, v163, v164\n"
"v_max3_f32 v193, v193, v171, v172\n"
"v_max3_f32 v194, v194, v179, v180\n"
"v_max3_f32 v195, v195, v187, v188\n"
"v_max3_f32 v192, v192, v165, v166\n"
"v_max3_f32 v193, v193, v173, v174\n"
"v_max3_f32 v194, v194, v181, v182\n"
"v_max3_f32 v195, v195, v189, v190\n"
"v_max_f32 v192, v192, v167\n"
"v_max_f32 v193, v193, v175\n"
"v_max_f32 v194, v194, v183\n"
"v_max_f32 v195, v195, v191\n"
"v_mov_b32 v196, v192\n"
"v_mov_b32 v197, v193\n"
"v_mov_b32 v198, v194\n"
"v_mov_b32 v199, v195\n"
"v_permlane32_swap_b32 v192, v196\n"
"v_permlane32_swap_b32 v193, v197\n"
"v_permlane32_swap_b32 v194, v198\n"
"v_permlane32_swap_b32 v195, v199\n"
"v_max_f32 v192, v192, v196\n"
"v_max_f32 v193, v193, v197\n"
"v_max_f32 v194, v194, v198\n"
"v_max_f32 v195, v195, v199\n"
"v_mov_b32 v196, v192\n"
"v_mov_b32 v197, v193\n"
"v_mov_b32 v198, v194\n"
"v_mov_b32 v199, v195\n"
"v_permlane16_swap_b32 v192, v196\n"
"v_permlane16_swap_b32 v193, v197\n"
"v_permlane16_swap_b32 v194, v198\n"
"v_permlane16_swap_b32 v195, v199\n"
"v_max_f32 v192, v192, v196\n"
"v_max_f32 v193, v193, v197\n"
"v_max_f32 v194, v194, v198\n"
"v_max_f32 v195, v195, v199\n"
"v_max_f32 v192, 0, v192\n"
"v_sub_f32 v196, 0, v192\n"
"v_exp_f32 v196, v196\n"
"v_sub_f32 v224, v224, v192\n"
"v_sub_f32 v225, v225, v192\n"
"v_sub_f32 v226, v226, v192\n"
"v_sub_f32 v227, v227, v192\n"
"v_sub_f32 v160, v160, v192\n"
"v_sub_f32 v161, v161, v192\n"
"v_sub_f32 v162, v162, v192\n"
"v_sub_f32 v163, v163, v192\n"
"v_sub_f32 v164, v164, v192\n"
"v_sub_f32 v165, v165, v192\n"
"v_sub_f32 v166, v166, v192\n"
"v_sub_f32 v167, v167, v192\n"
"v_sub_f32 v128, v128, v192\n"
"v_sub_f32 v129, v129, v192\n"
"v_sub_f32 v130, v130, v192\n"
"v_sub_f32 v131, v131, v192\n"
"v_sub_f32 v132, v132, v192\n"
"v_sub_f32 v133, v133, v192\n"
"v_sub_f32 v134, v134, v192\n"
"v_sub_f32 v135, v135, v192\n"
"v_mul_f32 v240, v240, v196\n"
"v_max_f32 v193, 0, v193\n"
"v_sub_f32 v197, 0, v193\n"
"v_exp_f32 v197, v197\n"
"v_sub_f32 v228, v228, v193\n"
"v_sub_f32 v229, v229, v193\n"
"v_sub_f32 v230, v230, v193\n"
"v_sub_f32 v231, v231, v193\n"
"v_sub_f32 v168, v168, v193\n"
"v_sub_f32 v169, v169, v193\n"
"v_sub_f32 v170, v170, v193\n"
"v_sub_f32 v171, v171, v193\n"
"v_sub_f32 v172, v172, v193\n"
"v_sub_f32 v173, v173, v193\n"
"v_sub_f32 v174, v174, v193\n"
"v_sub_f32 v175, v175, v193\n"
"v_sub_f32 v136, v136, v193\n"
"v_sub_f32 v137, v137, v193\n"
"v_sub_f32 v138, v138, v193\n"
"v_sub_f32 v139, v139, v193\n"
"v_sub_f32 v140, v140, v193\n"
"v_sub_f32 v141, v141, v193\n"
"v_sub_f32 v142, v142, v193\n"
"v_sub_f32 v143, v143, v193\n"
"v_mul_f32 v241, v241, v197\n"
"v_max_f32 v194, 0, v194\n"
"v_sub_f32 v198, 0, v194\n"
"v_exp_f32 v198, v198\n"
"v_sub_f32 v232, v232, v194\n"
"v_sub_f32 v233, v233, v194\n"
"v_sub_f32 v234, v234, v194\n"
"v_sub_f32 v235, v235, v194\n"
"v_sub_f32 v176, v176, v194\n"
"v_sub_f32 v177, v177, v194\n"
"v_sub_f32 v178, v178, v194\n"
"v_sub_f32 v179, v179, v194\n"
"v_sub_f32 v180, v180, v194\n"
"v_sub_f32 v181, v181, v194\n"
"v_sub_f32 v182, v182, v194\n"
"v_sub_f32 v183, v183, v194\n"
"v_sub_f32 v144, v144, v194\n"
"v_sub_f32 v145, v145, v194\n"
"v_sub_f32 v146, v146, v194\n"
"v_sub_f32 v147, v147, v194\n"
"v_sub_f32 v148, v148, v194\n"
"v_sub_f32 v149, v149, v194\n"
"v_sub_f32 v150, v150, v194\n"
"v_sub_f32 v151, v151, v194\n"
"v_mul_f32 v242, v242, v198\n"
"v_max_f32 v195, 0, v195\n"
"v_sub_f32 v199, 0, v195\n"
"v_exp_f32 v199, v199\n"
"v_sub_f32 v236, v236, v195\n"
"v_sub_f32 v237, v237, v195\n"
"v_sub_f32 v238, v238, v195\n"
"v_sub_f32 v239, v239, v195\n"
"v_sub_f32 v184, v184, v195\n"
"v_sub_f32 v185, v185, v195\n"
"v_sub_f32 v186, v186, v195\n"
"v_sub_f32 v187, v187, v195\n"
"v_sub_f32 v188, v188, v195\n"
"v_sub_f32 v189, v189, v195\n"
"v_sub_f32 v190, v190, v195\n"
"v_sub_f32 v191, v191, v195\n"
"v_sub_f32 v152, v152, v195\n"
"v_sub_f32 v153, v153, v195\n"
"v_sub_f32 v154, v154, v195\n"
"v_sub_f32 v155, v155, v195\n"
"v_sub_f32 v156, v156, v195\n"
"v_sub_f32 v157, v157, v195\n"
"v_sub_f32 v158, v158, v195\n"
"v_sub_f32 v159, v159, v195\n"
"v_mul_f32 v243, v243, v199\n"
"s_getpc_b64 s[90:91]\n"
"s_branch LP2_%=\n"
"v_exp_f32 v160, v160\n"
"v_exp_f32 v161, v161\n"
"v_exp_f32 v162, v162\n"
"v_exp_f32 v163, v163\n"
"v_exp_f32 v164, v164\n"
"v_exp_f32 v165, v165\n"
"v_exp_f32 v166, v166\n"
"v_exp_f32 v167, v167\n"
"v_exp_f32 v168, v168\n"
"v_cvt_pk_bf16_f32 v192, v160, v161\n"
"v_add_f32 v160, v160, v161\n"
"v_exp_f32 v169, v169\n"
"v_cvt_pk_bf16_f32 v193, v162, v163\n"
"v_exp_f32 v170, v170\n"
"v_add_f32 v162, v162, v163\n"
"v_cvt_pk_bf16_f32 v194, v164, v165\n"
"v_exp_f32 v171, v171\n"
"v_add_f32 v164, v164, v165\n"
"v_exp_f32 v172, v172\n"
"v_add_f32 v160, v160, v162\n"
"v_exp_f32 v173, v173\n"
"v_cvt_pk_bf16_f32 v195, v166, v167\n"
"v_add_f32 v166, v166, v167\n"
"v_exp_f32 v174, v174\n"
"v_add_f32 v160, v160, v164\n"
"v_exp_f32 v175, v175\n"
"v_add_f32 v160, v160, v166\n"
"v_exp_f32 v176, v176\n"
"v_cvt_pk_bf16_f32 v196, v168, v169\n"
"v_add_f32 v168, v168, v169\n"
"v_exp_f32 v177, v177\n"
"v_cvt_pk_bf16_f32 v197, v170, v171\n"
"v_exp_f32 v178, v178\n"
"v_add_f32 v170, v170, v171\n"
"v_cvt_pk_bf16_f32 v198, v172, v173\n"
"v_exp_f32 v179, v179\n"
"v_add_f32 v172, v172, v173\n"
"v_exp_f32 v180, v180\n"
"v_add_f32 v168, v168, v170\n"
"v_exp_f32 v181, v181\n"
"v_cvt_pk_bf16_f32 v199, v174, v175\n"
"v_add_f32 v174, v174, v175\n"
"v_exp_f32 v182, v182\n"
"v_add_f32 v168, v168, v172\n"
"v_exp_f32 v183, v183\n"
"v_add_f32 v168, v168, v174\n"
"v_exp_f32 v184, v184\n"
"v_cvt_pk_bf16_f32 v200, v176, v177\n"
"v_add_f32 v176, v176, v177\n"
"v_exp_f32 v185, v185\n"
"v_cvt_pk_bf16_f32 v201, v178, v179\n"
"v_exp_f32 v186, v186\n"
"v_add_f32 v178, v178, v179\n"
"v_cvt_pk_bf16_f32 v202, v180, v181\n"
"v_exp_f32 v187, v187\n"
"v_add_f32 v180, v180, v181\n"
"v_exp_f32 v188, v188\n"
"v_add_f32 v176, v176, v178\n"
"v_exp_f32 v189, v189\n"
"v_cvt_pk_bf16_f32 v203, v182, v183\n"
"v_add_f32 v182, v182, v183\n"
"v_exp_f32 v190, v190\n"
"v_add_f32 v176, v176, v180\n"
"v_exp_f32 v191, v191\n"
"v_add_f32 v176, v176, v182\n"
"v_cvt_pk_bf16_f32 v204, v184, v185\n"
"v_add_f32 v184, v184, v185\n"
"v_cvt_pk_bf16_f32 v205, v186, v187\n"
"v_add_f32 v186, v186, v187\n"
"v_cvt_pk_bf16_f32 v206, v188, v189\n"
"v_add_f32 v188, v188, v189\n"
"v_add_f32 v184, v184, v186\n"
"v_cvt_pk_bf16_f32 v207, v190, v191\n"
"v_add_f32 v190, v190, v191\n"
"v_add_f32 v184, v184, v188\n"
"v_add_f32 v184, v184, v190\n"
"s_add_u32 s72, s72, 4\n"
"s_addc_u32 s73, s73, 0\n"
"s_setpc_b64 s[72:73]\n"
"LP12_%=:\n"
"s_nop 15\n"
"s_lshl_b32 s65, s37, 13\n"
"s_sub_u32 s65, s55, s65\n"
"s_sub_u32 s65, s65, s68\n"
"s_add_u32 s96, s65, 8192\n"
"v_and_b32 v128, 15, v253\n"
"v_lshrrev_b32 v129, 4, v253\n"
"v_lshlrev_b32 v128, 9, v128\n"
"v_lshl_add_u32 v128, v129, 4, v128\n"
"buffer_load_dwordx4 v[128:131], v128, s[12:15], s65 offen offset:0\n"
"v_and_b32 v132, 15, v253\n"
"v_lshrrev_b32 v133, 4, v253\n"
"v_lshlrev_b32 v132, 9, v132\n"
"v_lshl_add_u32 v132, v133, 4, v132\n"
"buffer_load_dwordx4 v[132:135], v132, s[12:15], s96 offen offset:0\n"
"v_and_b32 v136, 15, v253\n"
"v_lshrrev_b32 v137, 4, v253\n"
"v_lshlrev_b32 v136, 9, v136\n"
"v_lshl_add_u32 v136, v137, 4, v136\n"
"buffer_load_dwordx4 v[136:139], v136, s[12:15], s65 offen offset:64\n"
"v_and_b32 v140, 15, v253\n"
"v_lshrrev_b32 v141, 4, v253\n"
"v_lshlrev_b32 v140, 9, v140\n"
"v_lshl_add_u32 v140, v141, 4, v140\n"
"buffer_load_dwordx4 v[140:143], v140, s[12:15], s96 offen offset:64\n"
"s_waitcnt vmcnt(0)\n"
"v_mfma_f32_16x16x32_bf16 v[160:163], v[128:131], v[0:3], v[224:227]\n"
"v_mfma_f32_16x16x32_bf16 v[168:171], v[128:131], v[4:7], v[228:231]\n"
"v_mfma_f32_16x16x32_bf16 v[176:179], v[128:131], v[8:11], v[232:235]\n"
"v_mfma_f32_16x16x32_bf16 v[184:187], v[128:131], v[12:15], v[236:239]\n"
"v_mfma_f32_16x16x32_bf16 v[164:167], v[132:135], v[0:3], v[224:227]\n"
"v_mfma_f32_16x16x32_bf16 v[172:175], v[132:135], v[4:7], v[228:231]\n"
"v_mfma_f32_16x16x32_bf16 v[180:183], v[132:135], v[8:11], v[232:235]\n"
"v_mfma_f32_16x16x32_bf16 v[188:191], v[132:135], v[12:15], v[236:239]\n"
"v_mfma_f32_16x16x32_bf16 v[160:163], v[136:139], v[16:19], v[160:163]\n"
"v_mfma_f32_16x16x32_bf16 v[168:171], v[136:139], v[20:23], v[168:171]\n"
"v_mfma_f32_16x16x32_bf16 v[176:179], v[136:139], v[24:27], v[176:179]\n"
"v_mfma_f32_16x16x32_bf16 v[184:187], v[136:139], v[28:31], v[184:187]\n"
"v_mfma_f32_16x16x32_bf16 v[164:167], v[140:143], v[16:19], v[164:167]\n"
"v_mfma_f32_16x16x32_bf16 v[172:175], v[140:143], v[20:23], v[172:175]\n"
"v_mfma_f32_16x16x32_bf16 v[180:183], v[140:143], v[24:27], v[180:183]\n"
"v_mfma_f32_16x16x32_bf16 v[188:191], v[140:143], v[28:31], v[188:191]\n"
"s_nop 15\n"
"v_and_b32 v128, 15, v253\n"
"v_lshrrev_b32 v129, 4, v253\n"
"v_lshlrev_b32 v128, 9, v128\n"
"v_lshl_add_u32 v128, v129, 4, v128\n"
"buffer_load_dwordx4 v[128:131], v128, s[12:15], s65 offen offset:128\n"
"v_and_b32 v132, 15, v253\n"
"v_lshrrev_b32 v133, 4, v253\n"
"v_lshlrev_b32 v132, 9, v132\n"
"v_lshl_add_u32 v132, v133, 4, v132\n"
"buffer_load_dwordx4 v[132:135], v132, s[12:15], s96 offen offset:128\n"
"v_and_b32 v136, 15, v253\n"
"v_lshrrev_b32 v137, 4, v253\n"
"v_lshlrev_b32 v136, 9, v136\n"
"v_lshl_add_u32 v136, v137, 4, v136\n"
"buffer_load_dwordx4 v[136:139], v136, s[12:15], s65 offen offset:192\n"
"v_and_b32 v140, 15, v253\n"
"v_lshrrev_b32 v141, 4, v253\n"
"v_lshlrev_b32 v140, 9, v140\n"
"v_lshl_add_u32 v140, v141, 4, v140\n"
"buffer_load_dwordx4 v[140:143], v140, s[12:15], s96 offen offset:192\n"
"s_waitcnt vmcnt(0)\n"
"v_mfma_f32_16x16x32_bf16 v[160:163], v[128:131], v[32:35], v[160:163]\n"
"v_mfma_f32_16x16x32_bf16 v[168:171], v[128:131], v[36:39], v[168:171]\n"
"v_mfma_f32_16x16x32_bf16 v[176:179], v[128:131], v[40:43], v[176:179]\n"
"v_mfma_f32_16x16x32_bf16 v[184:187], v[128:131], v[44:47], v[184:187]\n"
"v_mfma_f32_16x16x32_bf16 v[164:167], v[132:135], v[32:35], v[164:167]\n"
"v_mfma_f32_16x16x32_bf16 v[172:175], v[132:135], v[36:39], v[172:175]\n"
"v_mfma_f32_16x16x32_bf16 v[180:183], v[132:135], v[40:43], v[180:183]\n"
"v_mfma_f32_16x16x32_bf16 v[188:191], v[132:135], v[44:47], v[188:191]\n"
"v_mfma_f32_16x16x32_bf16 v[160:163], v[136:139], v[48:51], v[160:163]\n"
"v_mfma_f32_16x16x32_bf16 v[168:171], v[136:139], v[52:55], v[168:171]\n"
"v_mfma_f32_16x16x32_bf16 v[176:179], v[136:139], v[56:59], v[176:179]\n"
"v_mfma_f32_16x16x32_bf16 v[184:187], v[136:139], v[60:63], v[184:187]\n"
"v_mfma_f32_16x16x32_bf16 v[164:167], v[140:143], v[48:51], v[164:167]\n"
"v_mfma_f32_16x16x32_bf16 v[172:175], v[140:143], v[52:55], v[172:175]\n"
"v_mfma_f32_16x16x32_bf16 v[180:183], v[140:143], v[56:59], v[180:183]\n"
"v_mfma_f32_16x16x32_bf16 v[188:191], v[140:143], v[60:63], v[188:191]\n"
"s_nop 15\n"
"v_and_b32 v128, 15, v253\n"
"v_lshrrev_b32 v129, 4, v253\n"
"v_lshlrev_b32 v128, 9, v128\n"
"v_lshl_add_u32 v128, v129, 4, v128\n"
"buffer_load_dwordx4 v[128:131], v128, s[12:15], s65 offen offset:256\n"
"v_and_b32 v132, 15, v253\n"
"v_lshrrev_b32 v133, 4, v253\n"
"v_lshlrev_b32 v132, 9, v132\n"
"v_lshl_add_u32 v132, v133, 4, v132\n"
"buffer_load_dwordx4 v[132:135], v132, s[12:15], s96 offen offset:256\n"
"v_and_b32 v136, 15, v253\n"
"v_lshrrev_b32 v137, 4, v253\n"
"v_lshlrev_b32 v136, 9, v136\n"
"v_lshl_add_u32 v136, v137, 4, v136\n"
"buffer_load_dwordx4 v[136:139], v136, s[12:15], s65 offen offset:320\n"
"v_and_b32 v140, 15, v253\n"
"v_lshrrev_b32 v141, 4, v253\n"
"v_lshlrev_b32 v140, 9, v140\n"
"v_lshl_add_u32 v140, v141, 4, v140\n"
"buffer_load_dwordx4 v[140:143], v140, s[12:15], s96 offen offset:320\n"
"s_waitcnt vmcnt(0)\n"
"v_mfma_f32_16x16x32_bf16 v[160:163], v[128:131], v[64:67], v[160:163]\n"
"v_mfma_f32_16x16x32_bf16 v[168:171], v[128:131], v[68:71], v[168:171]\n"
"v_mfma_f32_16x16x32_bf16 v[176:179], v[128:131], v[72:75], v[176:179]\n"
"v_mfma_f32_16x16x32_bf16 v[184:187], v[128:131], v[76:79], v[184:187]\n"
"v_mfma_f32_16x16x32_bf16 v[164:167], v[132:135], v[64:67], v[164:167]\n"
"v_mfma_f32_16x16x32_bf16 v[172:175], v[132:135], v[68:71], v[172:175]\n"
"v_mfma_f32_16x16x32_bf16 v[180:183], v[132:135], v[72:75], v[180:183]\n"
"v_mfma_f32_16x16x32_bf16 v[188:191], v[132:135], v[76:79], v[188:191]\n"
"v_mfma_f32_16x16x32_bf16 v[160:163], v[136:139], v[80:83], v[160:163]\n"
"v_mfma_f32_16x16x32_bf16 v[168:171], v[136:139], v[84:87], v[168:171]\n"
"v_mfma_f32_16x16x32_bf16 v[176:179], v[136:139], v[88:91], v[176:179]\n"
"v_mfma_f32_16x16x32_bf16 v[184:187], v[136:139], v[92:95], v[184:187]\n"
"v_mfma_f32_16x16x32_bf16 v[164:167], v[140:143], v[80:83], v[164:167]\n"
"v_mfma_f32_16x16x32_bf16 v[172:175], v[140:143], v[84:87], v[172:175]\n"
"v_mfma_f32_16x16x32_bf16 v[180:183], v[140:143], v[88:91], v[180:183]\n"
"v_mfma_f32_16x16x32_bf16 v[188:191], v[140:143], v[92:95], v[188:191]\n"
"s_nop 15\n"
"v_and_b32 v128, 15, v253\n"
"v_lshrrev_b32 v129, 4, v253\n"
"v_lshlrev_b32 v128, 9, v128\n"
"v_lshl_add_u32 v128, v129, 4, v128\n"
"buffer_load_dwordx4 v[128:131], v128, s[12:15], s65 offen offset:384\n"
"v_and_b32 v132, 15, v253\n"
"v_lshrrev_b32 v133, 4, v253\n"
"v_lshlrev_b32 v132, 9, v132\n"
"v_lshl_add_u32 v132, v133, 4, v132\n"
"buffer_load_dwordx4 v[132:135], v132, s[12:15], s96 offen offset:384\n"
"v_and_b32 v136, 15, v253\n"
"v_lshrrev_b32 v137, 4, v253\n"
"v_lshlrev_b32 v136, 9, v136\n"
"v_lshl_add_u32 v136, v137, 4, v136\n"
"buffer_load_dwordx4 v[136:139], v136, s[12:15], s65 offen offset:448\n"
"v_and_b32 v140, 15, v253\n"
"v_lshrrev_b32 v141, 4, v253\n"
"v_lshlrev_b32 v140, 9, v140\n"
"v_lshl_add_u32 v140, v141, 4, v140\n"
"buffer_load_dwordx4 v[140:143], v140, s[12:15], s96 offen offset:448\n"
"s_waitcnt vmcnt(0)\n"
"v_mfma_f32_16x16x32_bf16 v[160:163], v[128:131], v[96:99], v[160:163]\n"
"v_mfma_f32_16x16x32_bf16 v[168:171], v[128:131], v[100:103], v[168:171]\n"
"v_mfma_f32_16x16x32_bf16 v[176:179], v[128:131], v[104:107], v[176:179]\n"
"v_mfma_f32_16x16x32_bf16 v[184:187], v[128:131], v[108:111], v[184:187]\n"
"v_mfma_f32_16x16x32_bf16 v[164:167], v[132:135], v[96:99], v[164:167]\n"
"v_mfma_f32_16x16x32_bf16 v[172:175], v[132:135], v[100:103], v[172:175]\n"
"v_mfma_f32_16x16x32_bf16 v[180:183], v[132:135], v[104:107], v[180:183]\n"
"v_mfma_f32_16x16x32_bf16 v[188:191], v[132:135], v[108:111], v[188:191]\n"
"v_mfma_f32_16x16x32_bf16 v[160:163], v[136:139], v[112:115], v[160:163]\n"
"v_mfma_f32_16x16x32_bf16 v[168:171], v[136:139], v[116:119], v[168:171]\n"
"v_mfma_f32_16x16x32_bf16 v[176:179], v[136:139], v[120:123], v[176:179]\n"
"v_mfma_f32_16x16x32_bf16 v[184:187], v[136:139], v[124:127], v[184:187]\n"
"v_mfma_f32_16x16x32_bf16 v[164:167], v[140:143], v[112:115], v[164:167]\n"
"v_mfma_f32_16x16x32_bf16 v[172:175], v[140:143], v[116:119], v[172:175]\n"
"v_mfma_f32_16x16x32_bf16 v[180:183], v[140:143], v[120:123], v[180:183]\n"
"v_mfma_f32_16x16x32_bf16 v[188:191], v[140:143], v[124:127], v[188:191]\n"
"s_nop 15\n"
"v_max3_f32 v128, v160, v161, v162\n"
"v_max3_f32 v129, v168, v169, v170\n"
"v_max3_f32 v130, v176, v177, v178\n"
"v_max3_f32 v131, v184, v185, v186\n"
"v_max3_f32 v128, v128, v163, v164\n"
"v_max3_f32 v129, v129, v171, v172\n"
"v_max3_f32 v130, v130, v179, v180\n"
"v_max3_f32 v131, v131, v187, v188\n"
"v_max3_f32 v128, v128, v165, v166\n"
"v_max3_f32 v129, v129, v173, v174\n"
"v_max3_f32 v130, v130, v181, v182\n"
"v_max3_f32 v131, v131, v189, v190\n"
"v_max_f32 v128, v128, v167\n"
"v_max_f32 v129, v129, v175\n"
"v_max_f32 v130, v130, v183\n"
"v_max_f32 v131, v131, v191\n"
"v_mov_b32 v132, v128\n"
"v_mov_b32 v133, v129\n"
"v_mov_b32 v134, v130\n"
"v_mov_b32 v135, v131\n"
"v_permlane32_swap_b32 v128, v132\n"
"v_permlane32_swap_b32 v129, v133\n"
"v_permlane32_swap_b32 v130, v134\n"
"v_permlane32_swap_b32 v131, v135\n"
"v_max_f32 v128, v128, v132\n"
"v_max_f32 v129, v129, v133\n"
"v_max_f32 v130, v130, v134\n"
"v_max_f32 v131, v131, v135\n"
"v_mov_b32 v132, v128\n"
"v_mov_b32 v133, v129\n"
"v_mov_b32 v134, v130\n"
"v_mov_b32 v135, v131\n"
"v_permlane16_swap_b32 v128, v132\n"
"v_permlane16_swap_b32 v129, v133\n"
"v_permlane16_swap_b32 v130, v134\n"
"v_permlane16_swap_b32 v131, v135\n"
"v_max_f32 v128, v128, v132\n"
"v_max_f32 v129, v129, v133\n"
"v_max_f32 v130, v130, v134\n"
"v_max_f32 v131, v131, v135\n"
"v_max_f32 v128, 0, v128\n"
"v_sub_f32 v132, 0, v128\n"
"v_exp_f32 v132, v132\n"
"v_sub_f32 v224, v224, v128\n"
"v_sub_f32 v225, v225, v128\n"
"v_sub_f32 v226, v226, v128\n"
"v_sub_f32 v227, v227, v128\n"
"v_sub_f32 v160, v160, v128\n"
"v_sub_f32 v161, v161, v128\n"
"v_sub_f32 v162, v162, v128\n"
"v_sub_f32 v163, v163, v128\n"
"v_sub_f32 v164, v164, v128\n"
"v_sub_f32 v165, v165, v128\n"
"v_sub_f32 v166, v166, v128\n"
"v_sub_f32 v167, v167, v128\n"
"v_mul_f32 v240, v240, v132\n"
"v_max_f32 v129, 0, v129\n"
"v_sub_f32 v133, 0, v129\n"
"v_exp_f32 v133, v133\n"
"v_sub_f32 v228, v228, v129\n"
"v_sub_f32 v229, v229, v129\n"
"v_sub_f32 v230, v230, v129\n"
"v_sub_f32 v231, v231, v129\n"
"v_sub_f32 v168, v168, v129\n"
"v_sub_f32 v169, v169, v129\n"
"v_sub_f32 v170, v170, v129\n"
"v_sub_f32 v171, v171, v129\n"
"v_sub_f32 v172, v172, v129\n"
"v_sub_f32 v173, v173, v129\n"
"v_sub_f32 v174, v174, v129\n"
"v_sub_f32 v175, v175, v129\n"
"v_mul_f32 v241, v241, v133\n"
"v_max_f32 v130, 0, v130\n"
"v_sub_f32 v134, 0, v130\n"
"v_exp_f32 v134, v134\n"
"v_sub_f32 v232, v232, v130\n"
"v_sub_f32 v233, v233, v130\n"
"v_sub_f32 v234, v234, v130\n"
"v_sub_f32 v235, v235, v130\n"
"v_sub_f32 v176, v176, v130\n"
"v_sub_f32 v177, v177, v130\n"
"v_sub_f32 v178, v178, v130\n"
"v_sub_f32 v179, v179, v130\n"
"v_sub_f32 v180, v180, v130\n"
"v_sub_f32 v181, v181, v130\n"
"v_sub_f32 v182, v182, v130\n"
"v_sub_f32 v183, v183, v130\n"
"v_mul_f32 v242, v242, v134\n"
"v_max_f32 v131, 0, v131\n"
"v_sub_f32 v135, 0, v131\n"
"v_exp_f32 v135, v135\n"
"v_sub_f32 v236, v236, v131\n"
"v_sub_f32 v237, v237, v131\n"
"v_sub_f32 v238, v238, v131\n"
"v_sub_f32 v239, v239, v131\n"
"v_sub_f32 v184, v184, v131\n"
"v_sub_f32 v185, v185, v131\n"
"v_sub_f32 v186, v186, v131\n"
"v_sub_f32 v187, v187, v131\n"
"v_sub_f32 v188, v188, v131\n"
"v_sub_f32 v189, v189, v131\n"
"v_sub_f32 v190, v190, v131\n"
"v_sub_f32 v191, v191, v131\n"
"v_mul_f32 v243, v243, v135\n"
"v_mov_b32 v196, v132\n"
"v_mov_b32 v197, v133\n"
"v_mov_b32 v198, v134\n"
"v_mov_b32 v199, v135\n"
"s_getpc_b64 s[90:91]\n"
"s_branch LP2_%=\n"
"v_exp_f32 v160, v160\n"
"v_exp_f32 v161, v161\n"
"v_exp_f32 v162, v162\n"
"v_exp_f32 v163, v163\n"
"v_exp_f32 v164, v164\n"
"v_exp_f32 v165, v165\n"
"v_exp_f32 v166, v166\n"
"v_exp_f32 v167, v167\n"
"v_exp_f32 v168, v168\n"
"v_cvt_pk_bf16_f32 v128, v160, v161\n"
"v_add_f32 v160, v160, v161\n"
"v_exp_f32 v169, v169\n"
"v_cvt_pk_bf16_f32 v129, v162, v163\n"
"v_exp_f32 v170, v170\n"
"v_add_f32 v162, v162, v163\n"
"v_cvt_pk_bf16_f32 v130, v164, v165\n"
"v_exp_f32 v171, v171\n"
"v_add_f32 v164, v164, v165\n"
"v_exp_f32 v172, v172\n"
"v_add_f32 v160, v160, v162\n"
"v_exp_f32 v173, v173\n"
"v_cvt_pk_bf16_f32 v131, v166, v167\n"
"v_add_f32 v166, v166, v167\n"
"v_exp_f32 v174, v174\n"
"v_add_f32 v160, v160, v164\n"
"v_exp_f32 v175, v175\n"
"v_add_f32 v160, v160, v166\n"
"v_exp_f32 v176, v176\n"
"v_cvt_pk_bf16_f32 v132, v168, v169\n"
"v_add_f32 v168, v168, v169\n"
"v_exp_f32 v177, v177\n"
"v_cvt_pk_bf16_f32 v133, v170, v171\n"
"v_exp_f32 v178, v178\n"
"v_add_f32 v170, v170, v171\n"
"v_cvt_pk_bf16_f32 v134, v172, v173\n"
"v_exp_f32 v179, v179\n"
"v_add_f32 v172, v172, v173\n"
"v_exp_f32 v180, v180\n"
"v_add_f32 v168, v168, v170\n"
"v_exp_f32 v181, v181\n"
"v_cvt_pk_bf16_f32 v135, v174, v175\n"
"v_add_f32 v174, v174, v175\n"
"v_exp_f32 v182, v182\n"
"v_add_f32 v168, v168, v172\n"
"v_exp_f32 v183, v183\n"
"v_add_f32 v168, v168, v174\n"
"v_exp_f32 v184, v184\n"
"v_cvt_pk_bf16_f32 v136, v176, v177\n"
"v_add_f32 v176, v176, v177\n"
"v_exp_f32 v185, v185\n"
"v_cvt_pk_bf16_f32 v137, v178, v179\n"
"v_exp_f32 v186, v186\n"
"v_add_f32 v178, v178, v179\n"
"v_cvt_pk_bf16_f32 v138, v180, v181\n"
"v_exp_f32 v187, v187\n"
"v_add_f32 v180, v180, v181\n"
"v_exp_f32 v188, v188\n"
"v_add_f32 v176, v176, v178\n"
"v_exp_f32 v189, v189\n"
"v_cvt_pk_bf16_f32 v139, v182, v183\n"
"v_add_f32 v182, v182, v183\n"
"v_exp_f32 v190, v190\n"
"v_add_f32 v176, v176, v180\n"
"v_exp_f32 v191, v191\n"
"v_add_f32 v176, v176, v182\n"
"v_cvt_pk_bf16_f32 v140, v184, v185\n"
"v_add_f32 v184, v184, v185\n"
"v_cvt_pk_bf16_f32 v141, v186, v187\n"
"v_add_f32 v186, v186, v187\n"
"v_cvt_pk_bf16_f32 v142, v188, v189\n"
"v_add_f32 v188, v188, v189\n"
"v_add_f32 v184, v184, v186\n"
"v_cvt_pk_bf16_f32 v143, v190, v191\n"
"v_add_f32 v190, v190, v191\n"
"v_add_f32 v184, v184, v188\n"
"v_add_f32 v184, v184, v190\n"
"s_add_u32 s72, s72, 4\n"
"s_addc_u32 s73, s73, 0\n"
"s_setpc_b64 s[72:73]\n"
"LP13_%=:\n"
"s_nop 15\n"
"s_lshl_b32 s65, s37, 13\n"
"s_sub_u32 s65, s55, s65\n"
"s_sub_u32 s65, s65, s68\n"
"s_add_u32 s96, s65, 8192\n"
"v_and_b32 v192, 15, v253\n"
"v_lshrrev_b32 v193, 4, v253\n"
"v_lshlrev_b32 v192, 9, v192\n"
"v_lshl_add_u32 v192, v193, 4, v192\n"
"buffer_load_dwordx4 v[192:195], v192, s[12:15], s65 offen offset:0\n"
"v_and_b32 v196, 15, v253\n"
"v_lshrrev_b32 v197, 4, v253\n"
"v_lshlrev_b32 v196, 9, v196\n"
"v_lshl_add_u32 v196, v197, 4, v196\n"
"buffer_load_dwordx4 v[196:199], v196, s[12:15], s96 offen offset:0\n"
"v_and_b32 v200, 15, v253\n"
"v_lshrrev_b32 v201, 4, v253\n"
"v_lshlrev_b32 v200, 9, v200\n"
"v_lshl_add_u32 v200, v201, 4, v200\n"
"buffer_load_dwordx4 v[200:203], v200, s[12:15], s65 offen offset:64\n"
"v_and_b32 v204, 15, v253\n"
"v_lshrrev_b32 v205, 4, v253\n"
"v_lshlrev_b32 v204, 9, v204\n"
"v_lshl_add_u32 v204, v205, 4, v204\n"
"buffer_load_dwordx4 v[204:207], v204, s[12:15], s96 offen offset:64\n"
"s_waitcnt vmcnt(0)\n"
"v_mfma_f32_16x16x32_bf16 v[128:131], v[192:195], v[0:3], v[224:227]\n"
"v_mfma_f32_16x16x32_bf16 v[136:139], v[192:195], v[4:7], v[228:231]\n"
"v_mfma_f32_16x16x32_bf16 v[144:147], v[192:195], v[8:11], v[232:235]\n"
"v_mfma_f32_16x16x32_bf16 v[152:155], v[192:195], v[12:15], v[236:239]\n"
"v_mfma_f32_16x16x32_bf16 v[132:135], v[196:199], v[0:3], v[224:227]\n"
"v_mfma_f32_16x16x32_bf16 v[140:143], v[196:199], v[4:7], v[228:231]\n"
"v_mfma_f32_16x16x32_bf16 v[148:151], v[196:199], v[8:11], v[232:235]\n"
"v_mfma_f32_16x16x32_bf16 v[156:159], v[196:199], v[12:15], v[236:239]\n"
"v_mfma_f32_16x16x32_bf16 v[128:131], v[200:203], v[16:19], v[128:131]\n"
"v_mfma_f32_16x16x32_bf16 v[136:139], v[200:203], v[20:23], v[136:139]\n"
"v_mfma_f32_16x16x32_bf16 v[144:147], v[200:203], v[24:27], v[144:147]\n"
"v_mfma_f32_16x16x32_bf16 v[152:155], v[200:203], v[28:31], v[152:155]\n"
"v_mfma_f32_16x16x32_bf16 v[132:135], v[204:207], v[16:19], v[132:135]\n"
"v_mfma_f32_16x16x32_bf16 v[140:143], v[204:207], v[20:23], v[140:143]\n"
"v_mfma_f32_16x16x32_bf16 v[148:151], v[204:207], v[24:27], v[148:151]\n"
"v_mfma_f32_16x16x32_bf16 v[156:159], v[204:207], v[28:31], v[156:159]\n"
"s_nop 15\n"
"v_and_b32 v192, 15, v253\n"
"v_lshrrev_b32 v193, 4, v253\n"
"v_lshlrev_b32 v192, 9, v192\n"
"v_lshl_add_u32 v192, v193, 4, v192\n"
"buffer_load_dwordx4 v[192:195], v192, s[12:15], s65 offen offset:128\n"
"v_and_b32 v196, 15, v253\n"
"v_lshrrev_b32 v197, 4, v253\n"
"v_lshlrev_b32 v196, 9, v196\n"
"v_lshl_add_u32 v196, v197, 4, v196\n"
"buffer_load_dwordx4 v[196:199], v196, s[12:15], s96 offen offset:128\n"
"v_and_b32 v200, 15, v253\n"
"v_lshrrev_b32 v201, 4, v253\n"
"v_lshlrev_b32 v200, 9, v200\n"
"v_lshl_add_u32 v200, v201, 4, v200\n"
"buffer_load_dwordx4 v[200:203], v200, s[12:15], s65 offen offset:192\n"
"v_and_b32 v204, 15, v253\n"
"v_lshrrev_b32 v205, 4, v253\n"
"v_lshlrev_b32 v204, 9, v204\n"
"v_lshl_add_u32 v204, v205, 4, v204\n"
"buffer_load_dwordx4 v[204:207], v204, s[12:15], s96 offen offset:192\n"
"s_waitcnt vmcnt(0)\n"
"v_mfma_f32_16x16x32_bf16 v[128:131], v[192:195], v[32:35], v[128:131]\n"
"v_mfma_f32_16x16x32_bf16 v[136:139], v[192:195], v[36:39], v[136:139]\n"
"v_mfma_f32_16x16x32_bf16 v[144:147], v[192:195], v[40:43], v[144:147]\n"
"v_mfma_f32_16x16x32_bf16 v[152:155], v[192:195], v[44:47], v[152:155]\n"
"v_mfma_f32_16x16x32_bf16 v[132:135], v[196:199], v[32:35], v[132:135]\n"
"v_mfma_f32_16x16x32_bf16 v[140:143], v[196:199], v[36:39], v[140:143]\n"
"v_mfma_f32_16x16x32_bf16 v[148:151], v[196:199], v[40:43], v[148:151]\n"
"v_mfma_f32_16x16x32_bf16 v[156:159], v[196:199], v[44:47], v[156:159]\n"
"v_mfma_f32_16x16x32_bf16 v[128:131], v[200:203], v[48:51], v[128:131]\n"
"v_mfma_f32_16x16x32_bf16 v[136:139], v[200:203], v[52:55], v[136:139]\n"
"v_mfma_f32_16x16x32_bf16 v[144:147], v[200:203], v[56:59], v[144:147]\n"
"v_mfma_f32_16x16x32_bf16 v[152:155], v[200:203], v[60:63], v[152:155]\n"
"v_mfma_f32_16x16x32_bf16 v[132:135], v[204:207], v[48:51], v[132:135]\n"
"v_mfma_f32_16x16x32_bf16 v[140:143], v[204:207], v[52:55], v[140:143]\n"
"v_mfma_f32_16x16x32_bf16 v[148:151], v[204:207], v[56:59], v[148:151]\n"
"v_mfma_f32_16x16x32_bf16 v[156:159], v[204:207], v[60:63], v[156:159]\n"
"s_nop 15\n"
"v_and_b32 v192, 15, v253\n"
"v_lshrrev_b32 v193, 4, v253\n"
"v_lshlrev_b32 v192, 9, v192\n"
"v_lshl_add_u32 v192, v193, 4, v192\n"
"buffer_load_dwordx4 v[192:195], v192, s[12:15], s65 offen offset:256\n"
"v_and_b32 v196, 15, v253\n"
"v_lshrrev_b32 v197, 4, v253\n"
"v_lshlrev_b32 v196, 9, v196\n"
"v_lshl_add_u32 v196, v197, 4, v196\n"
"buffer_load_dwordx4 v[196:199], v196, s[12:15], s96 offen offset:256\n"
"v_and_b32 v200, 15, v253\n"
"v_lshrrev_b32 v201, 4, v253\n"
"v_lshlrev_b32 v200, 9, v200\n"
"v_lshl_add_u32 v200, v201, 4, v200\n"
"buffer_load_dwordx4 v[200:203], v200, s[12:15], s65 offen offset:320\n"
"v_and_b32 v204, 15, v253\n"
"v_lshrrev_b32 v205, 4, v253\n"
"v_lshlrev_b32 v204, 9, v204\n"
"v_lshl_add_u32 v204, v205, 4, v204\n"
"buffer_load_dwordx4 v[204:207], v204, s[12:15], s96 offen offset:320\n"
"s_waitcnt vmcnt(0)\n"
"v_mfma_f32_16x16x32_bf16 v[128:131], v[192:195], v[64:67], v[128:131]\n"
"v_mfma_f32_16x16x32_bf16 v[136:139], v[192:195], v[68:71], v[136:139]\n"
"v_mfma_f32_16x16x32_bf16 v[144:147], v[192:195], v[72:75], v[144:147]\n"
"v_mfma_f32_16x16x32_bf16 v[152:155], v[192:195], v[76:79], v[152:155]\n"
"v_mfma_f32_16x16x32_bf16 v[132:135], v[196:199], v[64:67], v[132:135]\n"
"v_mfma_f32_16x16x32_bf16 v[140:143], v[196:199], v[68:71], v[140:143]\n"
"v_mfma_f32_16x16x32_bf16 v[148:151], v[196:199], v[72:75], v[148:151]\n"
"v_mfma_f32_16x16x32_bf16 v[156:159], v[196:199], v[76:79], v[156:159]\n"
"v_mfma_f32_16x16x32_bf16 v[128:131], v[200:203], v[80:83], v[128:131]\n"
"v_mfma_f32_16x16x32_bf16 v[136:139], v[200:203], v[84:87], v[136:139]\n"
"v_mfma_f32_16x16x32_bf16 v[144:147], v[200:203], v[88:91], v[144:147]\n"
"v_mfma_f32_16x16x32_bf16 v[152:155], v[200:203], v[92:95], v[152:155]\n"
"v_mfma_f32_16x16x32_bf16 v[132:135], v[204:207], v[80:83], v[132:135]\n"
"v_mfma_f32_16x16x32_bf16 v[140:143], v[204:207], v[84:87], v[140:143]\n"
"v_mfma_f32_16x16x32_bf16 v[148:151], v[204:207], v[88:91], v[148:151]\n"
"v_mfma_f32_16x16x32_bf16 v[156:159], v[204:207], v[92:95], v[156:159]\n"
"s_nop 15\n"
"v_and_b32 v192, 15, v253\n"
"v_lshrrev_b32 v193, 4, v253\n"
"v_lshlrev_b32 v192, 9, v192\n"
"v_lshl_add_u32 v192, v193, 4, v192\n"
"buffer_load_dwordx4 v[192:195], v192, s[12:15], s65 offen offset:384\n"
"v_and_b32 v196, 15, v253\n"
"v_lshrrev_b32 v197, 4, v253\n"
"v_lshlrev_b32 v196, 9, v196\n"
"v_lshl_add_u32 v196, v197, 4, v196\n"
"buffer_load_dwordx4 v[196:199], v196, s[12:15], s96 offen offset:384\n"
"v_and_b32 v200, 15, v253\n"
"v_lshrrev_b32 v201, 4, v253\n"
"v_lshlrev_b32 v200, 9, v200\n"
"v_lshl_add_u32 v200, v201, 4, v200\n"
"buffer_load_dwordx4 v[200:203], v200, s[12:15], s65 offen offset:448\n"
"v_and_b32 v204, 15, v253\n"
"v_lshrrev_b32 v205, 4, v253\n"
"v_lshlrev_b32 v204, 9, v204\n"
"v_lshl_add_u32 v204, v205, 4, v204\n"
"buffer_load_dwordx4 v[204:207], v204, s[12:15], s96 offen offset:448\n"
"s_waitcnt vmcnt(0)\n"
"v_mfma_f32_16x16x32_bf16 v[128:131], v[192:195], v[96:99], v[128:131]\n"
"v_mfma_f32_16x16x32_bf16 v[136:139], v[192:195], v[100:103], v[136:139]\n"
"v_mfma_f32_16x16x32_bf16 v[144:147], v[192:195], v[104:107], v[144:147]\n"
"v_mfma_f32_16x16x32_bf16 v[152:155], v[192:195], v[108:111], v[152:155]\n"
"v_mfma_f32_16x16x32_bf16 v[132:135], v[196:199], v[96:99], v[132:135]\n"
"v_mfma_f32_16x16x32_bf16 v[140:143], v[196:199], v[100:103], v[140:143]\n"
"v_mfma_f32_16x16x32_bf16 v[148:151], v[196:199], v[104:107], v[148:151]\n"
"v_mfma_f32_16x16x32_bf16 v[156:159], v[196:199], v[108:111], v[156:159]\n"
"v_mfma_f32_16x16x32_bf16 v[128:131], v[200:203], v[112:115], v[128:131]\n"
"v_mfma_f32_16x16x32_bf16 v[136:139], v[200:203], v[116:119], v[136:139]\n"
"v_mfma_f32_16x16x32_bf16 v[144:147], v[200:203], v[120:123], v[144:147]\n"
"v_mfma_f32_16x16x32_bf16 v[152:155], v[200:203], v[124:127], v[152:155]\n"
"v_mfma_f32_16x16x32_bf16 v[132:135], v[204:207], v[112:115], v[132:135]\n"
"v_mfma_f32_16x16x32_bf16 v[140:143], v[204:207], v[116:119], v[140:143]\n"
"v_mfma_f32_16x16x32_bf16 v[148:151], v[204:207], v[120:123], v[148:151]\n"
"v_mfma_f32_16x16x32_bf16 v[156:159], v[204:207], v[124:127], v[156:159]\n"
"s_nop 15\n"
"v_max3_f32 v192, v128, v129, v130\n"
"v_max3_f32 v193, v136, v137, v138\n"
"v_max3_f32 v194, v144, v145, v146\n"
"v_max3_f32 v195, v152, v153, v154\n"
"v_max3_f32 v192, v192, v131, v132\n"
"v_max3_f32 v193, v193, v139, v140\n"
"v_max3_f32 v194, v194, v147, v148\n"
"v_max3_f32 v195, v195, v155, v156\n"
"v_max3_f32 v192, v192, v133, v134\n"
"v_max3_f32 v193, v193, v141, v142\n"
"v_max3_f32 v194, v194, v149, v150\n"
"v_max3_f32 v195, v195, v157, v158\n"
"v_max_f32 v192, v192, v135\n"
"v_max_f32 v193, v193, v143\n"
"v_max_f32 v194, v194, v151\n"
"v_max_f32 v195, v195, v159\n"
"v_mov_b32 v196, v192\n"
"v_mov_b32 v197, v193\n"
"v_mov_b32 v198, v194\n"
"v_mov_b32 v199, v195\n"
"v_permlane32_swap_b32 v192, v196\n"
"v_permlane32_swap_b32 v193, v197\n"
"v_permlane32_swap_b32 v194, v198\n"
"v_permlane32_swap_b32 v195, v199\n"
"v_max_f32 v192, v192, v196\n"
"v_max_f32 v193, v193, v197\n"
"v_max_f32 v194, v194, v198\n"
"v_max_f32 v195, v195, v199\n"
"v_mov_b32 v196, v192\n"
"v_mov_b32 v197, v193\n"
"v_mov_b32 v198, v194\n"
"v_mov_b32 v199, v195\n"
"v_permlane16_swap_b32 v192, v196\n"
"v_permlane16_swap_b32 v193, v197\n"
"v_permlane16_swap_b32 v194, v198\n"
"v_permlane16_swap_b32 v195, v199\n"
"v_max_f32 v192, v192, v196\n"
"v_max_f32 v193, v193, v197\n"
"v_max_f32 v194, v194, v198\n"
"v_max_f32 v195, v195, v199\n"
"v_sub_f32 v224, v224, v192\n"
"v_sub_f32 v225, v225, v192\n"
"v_sub_f32 v226, v226, v192\n"
"v_sub_f32 v227, v227, v192\n"
"v_sub_f32 v128, v128, v192\n"
"v_sub_f32 v129, v129, v192\n"
"v_sub_f32 v130, v130, v192\n"
"v_sub_f32 v131, v131, v192\n"
"v_sub_f32 v132, v132, v192\n"
"v_sub_f32 v133, v133, v192\n"
"v_sub_f32 v134, v134, v192\n"
"v_sub_f32 v135, v135, v192\n"
"v_sub_f32 v160, v160, v192\n"
"v_sub_f32 v161, v161, v192\n"
"v_sub_f32 v162, v162, v192\n"
"v_sub_f32 v163, v163, v192\n"
"v_sub_f32 v164, v164, v192\n"
"v_sub_f32 v165, v165, v192\n"
"v_sub_f32 v166, v166, v192\n"
"v_sub_f32 v167, v167, v192\n"
"v_sub_f32 v228, v228, v193\n"
"v_sub_f32 v229, v229, v193\n"
"v_sub_f32 v230, v230, v193\n"
"v_sub_f32 v231, v231, v193\n"
"v_sub_f32 v136, v136, v193\n"
"v_sub_f32 v137, v137, v193\n"
"v_sub_f32 v138, v138, v193\n"
"v_sub_f32 v139, v139, v193\n"
"v_sub_f32 v140, v140, v193\n"
"v_sub_f32 v141, v141, v193\n"
"v_sub_f32 v142, v142, v193\n"
"v_sub_f32 v143, v143, v193\n"
"v_sub_f32 v168, v168, v193\n"
"v_sub_f32 v169, v169, v193\n"
"v_sub_f32 v170, v170, v193\n"
"v_sub_f32 v171, v171, v193\n"
"v_sub_f32 v172, v172, v193\n"
"v_sub_f32 v173, v173, v193\n"
"v_sub_f32 v174, v174, v193\n"
"v_sub_f32 v175, v175, v193\n"
"v_sub_f32 v232, v232, v194\n"
"v_sub_f32 v233, v233, v194\n"
"v_sub_f32 v234, v234, v194\n"
"v_sub_f32 v235, v235, v194\n"
"v_sub_f32 v144, v144, v194\n"
"v_sub_f32 v145, v145, v194\n"
"v_sub_f32 v146, v146, v194\n"
"v_sub_f32 v147, v147, v194\n"
"v_sub_f32 v148, v148, v194\n"
"v_sub_f32 v149, v149, v194\n"
"v_sub_f32 v150, v150, v194\n"
"v_sub_f32 v151, v151, v194\n"
"v_sub_f32 v176, v176, v194\n"
"v_sub_f32 v177, v177, v194\n"
"v_sub_f32 v178, v178, v194\n"
"v_sub_f32 v179, v179, v194\n"
"v_sub_f32 v180, v180, v194\n"
"v_sub_f32 v181, v181, v194\n"
"v_sub_f32 v182, v182, v194\n"
"v_sub_f32 v183, v183, v194\n"
"v_sub_f32 v236, v236, v195\n"
"v_sub_f32 v237, v237, v195\n"
"v_sub_f32 v238, v238, v195\n"
"v_sub_f32 v239, v239, v195\n"
"v_sub_f32 v152, v152, v195\n"
"v_sub_f32 v153, v153, v195\n"
"v_sub_f32 v154, v154, v195\n"
"v_sub_f32 v155, v155, v195\n"
"v_sub_f32 v156, v156, v195\n"
"v_sub_f32 v157, v157, v195\n"
"v_sub_f32 v158, v158, v195\n"
"v_sub_f32 v159, v159, v195\n"
"v_sub_f32 v184, v184, v195\n"
"v_sub_f32 v185, v185, v195\n"
"v_sub_f32 v186, v186, v195\n"
"v_sub_f32 v187, v187, v195\n"
"v_sub_f32 v188, v188, v195\n"
"v_sub_f32 v189, v189, v195\n"
"v_sub_f32 v190, v190, v195\n"
"v_sub_f32 v191, v191, v195\n"
"v_exp_f32 v128, v128\n"
"v_exp_f32 v129, v129\n"
"v_exp_f32 v130, v130\n"
"v_exp_f32 v131, v131\n"
"v_exp_f32 v132, v132\n"
"v_exp_f32 v133, v133\n"
"v_exp_f32 v134, v134\n"
"v_exp_f32 v135, v135\n"
"v_exp_f32 v136, v136\n"
"v_cvt_pk_bf16_f32 v192, v128, v129\n"
"v_add_f32 v128, v128, v129\n"
"v_exp_f32 v137, v137\n"
"v_cvt_pk_bf16_f32 v193, v130, v131\n"
"v_exp_f32 v138, v138\n"
"v_add_f32 v130, v130, v131\n"
"v_cvt_pk_bf16_f32 v194, v132, v133\n"
"v_exp_f32 v139, v139\n"
"v_add_f32 v132, v132, v133\n"
"v_exp_f32 v140, v140\n"
"v_add_f32 v128, v128, v130\n"
"v_exp_f32 v141, v141\n"
"v_cvt_pk_bf16_f32 v195, v134, v135\n"
"v_add_f32 v134, v134, v135\n"
"v_exp_f32 v142, v142\n"
"v_add_f32 v128, v128, v132\n"
"v_exp_f32 v143, v143\n"
"v_add_f32 v128, v128, v134\n"
"v_exp_f32 v144, v144\n"
"v_cvt_pk_bf16_f32 v196, v136, v137\n"
"v_add_f32 v136, v136, v137\n"
"v_exp_f32 v145, v145\n"
"v_cvt_pk_bf16_f32 v197, v138, v139\n"
"v_exp_f32 v146, v146\n"
"v_add_f32 v138, v138, v139\n"
"v_cvt_pk_bf16_f32 v198, v140, v141\n"
"v_exp_f32 v147, v147\n"
"v_add_f32 v140, v140, v141\n"
"v_exp_f32 v148, v148\n"
"v_add_f32 v136, v136, v138\n"
"v_exp_f32 v149, v149\n"
"v_cvt_pk_bf16_f32 v199, v142, v143\n"
"v_add_f32 v142, v142, v143\n"
"v_exp_f32 v150, v150\n"
"v_add_f32 v136, v136, v140\n"
"v_exp_f32 v151, v151\n"
"v_add_f32 v136, v136, v142\n"
"v_exp_f32 v152, v152\n"
"v_cvt_pk_bf16_f32 v200, v144, v145\n"
"v_add_f32 v144, v144, v145\n"
"v_exp_f32 v153, v153\n"
"v_cvt_pk_bf16_f32 v201, v146, v147\n"
"v_exp_f32 v154, v154\n"
"v_add_f32 v146, v146, v147\n"
"v_cvt_pk_bf16_f32 v202, v148, v149\n"
"v_exp_f32 v155, v155\n"
"v_add_f32 v148, v148, v149\n"
"v_exp_f32 v156, v156\n"
"v_add_f32 v144, v144, v146\n"
"v_exp_f32 v157, v157\n"
"v_cvt_pk_bf16_f32 v203, v150, v151\n"
"v_add_f32 v150, v150, v151\n"
"v_exp_f32 v158, v158\n"
"v_add_f32 v144, v144, v148\n"
"v_exp_f32 v159, v159\n"
"v_add_f32 v144, v144, v150\n"
"v_cvt_pk_bf16_f32 v204, v152, v153\n"
"v_add_f32 v152, v152, v153\n"
"v_cvt_pk_bf16_f32 v205, v154, v155\n"
"v_add_f32 v154, v154, v155\n"
"v_cvt_pk_bf16_f32 v206, v156, v157\n"
"v_add_f32 v156, v156, v157\n"
"v_add_f32 v152, v152, v154\n"
"v_cvt_pk_bf16_f32 v207, v158, v159\n"
"v_add_f32 v158, v158, v159\n"
"v_add_f32 v152, v152, v156\n"
"v_add_f32 v152, v152, v158\n"
"s_add_u32 s72, s72, 4\n"
"s_addc_u32 s73, s73, 0\n"
"s_setpc_b64 s[72:73]\n"
"LSP_%=:\n"
"s_mov_b32 s14, 0x7fffc\n"
"LSPL_%=:\n"
"s_and_b32 s23, s14, 7\n"
"s_cbranch_scc1 LSPN_%=\n"
"global_load_dword v230, v245, s[12:13] sc1\n"
"s_waitcnt vmcnt(0)\n"
"v_or_b32 v231, s15, v230\n"
"s_mov_b64 exec, s[46:47]\n"
"global_store_dword v245, v231, s[74:75] sc1\n"
"s_mov_b64 exec, -1\n"
"s_nop 1\n"
"LSPN_%=:\n"
"s_add_u32 s14, s14, -1\n"
"global_load_dword v231, v244, s[74:75] sc1\n"
"s_waitcnt vmcnt(0)\n"
"v_subrev_u32 v229, s20, v231\n"
"v_cmp_lt_u32 vcc, s70, v229\n"
"s_cmp_eq_u32 s14, 0\n"
"s_cbranch_scc1 LSPX_%=\n"
"s_cbranch_vccnz LSPL_%=\n"
"LSPX_%=:\n"
"s_add_u32 s72, s72, 4\n"
"s_addc_u32 s73, s73, 0\n"
"s_setpc_b64 s[72:73]\n"
"LP2_%=:\n"
"v_accvgpr_read_b32 v200, a0\n"
"v_accvgpr_read_b32 v201, a1\n"
"v_mul_f32 v200, v200, v196\n"
"v_mul_f32 v201, v201, v196\n"
"v_accvgpr_write_b32 a0, v200\n"
"v_accvgpr_write_b32 a1, v201\n"
"v_accvgpr_read_b32 v200, a2\n"
"v_accvgpr_read_b32 v201, a3\n"
"v_mul_f32 v200, v200, v196\n"
"v_mul_f32 v201, v201, v196\n"
"v_accvgpr_write_b32 a2, v200\n"
"v_accvgpr_write_b32 a3, v201\n"
"v_accvgpr_read_b32 v200, a4\n"
"v_accvgpr_read_b32 v201, a5\n"
"v_mul_f32 v200, v200, v197\n"
"v_mul_f32 v201, v201, v197\n"
"v_accvgpr_write_b32 a4, v200\n"
"v_accvgpr_write_b32 a5, v201\n"
"v_accvgpr_read_b32 v200, a6\n"
"v_accvgpr_read_b32 v201, a7\n"
"v_mul_f32 v200, v200, v197\n"
"v_mul_f32 v201, v201, v197\n"
"v_accvgpr_write_b32 a6, v200\n"
"v_accvgpr_write_b32 a7, v201\n"
"v_accvgpr_read_b32 v200, a8\n"
"v_accvgpr_read_b32 v201, a9\n"
"v_mul_f32 v200, v200, v198\n"
"v_mul_f32 v201, v201, v198\n"
"v_accvgpr_write_b32 a8, v200\n"
"v_accvgpr_write_b32 a9, v201\n"
"v_accvgpr_read_b32 v200, a10\n"
"v_accvgpr_read_b32 v201, a11\n"
"v_mul_f32 v200, v200, v198\n"
"v_mul_f32 v201, v201, v198\n"
"v_accvgpr_write_b32 a10, v200\n"
"v_accvgpr_write_b32 a11, v201\n"
"v_accvgpr_read_b32 v200, a12\n"
"v_accvgpr_read_b32 v201, a13\n"
"v_mul_f32 v200, v200, v199\n"
"v_mul_f32 v201, v201, v199\n"
"v_accvgpr_write_b32 a12, v200\n"
"v_accvgpr_write_b32 a13, v201\n"
"v_accvgpr_read_b32 v200, a14\n"
"v_accvgpr_read_b32 v201, a15\n"
"v_mul_f32 v200, v200, v199\n"
"v_mul_f32 v201, v201, v199\n"
"v_accvgpr_write_b32 a14, v200\n"
"v_accvgpr_write_b32 a15, v201\n"
"v_accvgpr_read_b32 v200, a16\n"
"v_accvgpr_read_b32 v201, a17\n"
"v_mul_f32 v200, v200, v196\n"
"v_mul_f32 v201, v201, v196\n"
"v_accvgpr_write_b32 a16, v200\n"
"v_accvgpr_write_b32 a17, v201\n"
"v_accvgpr_read_b32 v200, a18\n"
"v_accvgpr_read_b32 v201, a19\n"
"v_mul_f32 v200, v200, v196\n"
"v_mul_f32 v201, v201, v196\n"
"v_accvgpr_write_b32 a18, v200\n"
"v_accvgpr_write_b32 a19, v201\n"
"v_accvgpr_read_b32 v200, a20\n"
"v_accvgpr_read_b32 v201, a21\n"
"v_mul_f32 v200, v200, v197\n"
"v_mul_f32 v201, v201, v197\n"
"v_accvgpr_write_b32 a20, v200\n"
"v_accvgpr_write_b32 a21, v201\n"
"v_accvgpr_read_b32 v200, a22\n"
"v_accvgpr_read_b32 v201, a23\n"
"v_mul_f32 v200, v200, v197\n"
"v_mul_f32 v201, v201, v197\n"
"v_accvgpr_write_b32 a22, v200\n"
"v_accvgpr_write_b32 a23, v201\n"
"v_accvgpr_read_b32 v200, a24\n"
"v_accvgpr_read_b32 v201, a25\n"
"v_mul_f32 v200, v200, v198\n"
"v_mul_f32 v201, v201, v198\n"
"v_accvgpr_write_b32 a24, v200\n"
"v_accvgpr_write_b32 a25, v201\n"
"v_accvgpr_read_b32 v200, a26\n"
"v_accvgpr_read_b32 v201, a27\n"
"v_mul_f32 v200, v200, v198\n"
"v_mul_f32 v201, v201, v198\n"
"v_accvgpr_write_b32 a26, v200\n"
"v_accvgpr_write_b32 a27, v201\n"
"v_accvgpr_read_b32 v200, a28\n"
"v_accvgpr_read_b32 v201, a29\n"
"v_mul_f32 v200, v200, v199\n"
"v_mul_f32 v201, v201, v199\n"
"v_accvgpr_write_b32 a28, v200\n"
"v_accvgpr_write_b32 a29, v201\n"
"v_accvgpr_read_b32 v200, a30\n"
"v_accvgpr_read_b32 v201, a31\n"
"v_mul_f32 v200, v200, v199\n"
"v_mul_f32 v201, v201, v199\n"
"v_accvgpr_write_b32 a30, v200\n"
"v_accvgpr_write_b32 a31, v201\n"
"v_accvgpr_read_b32 v200, a32\n"
"v_accvgpr_read_b32 v201, a33\n"
"v_mul_f32 v200, v200, v196\n"
"v_mul_f32 v201, v201, v196\n"
"v_accvgpr_write_b32 a32, v200\n"
"v_accvgpr_write_b32 a33, v201\n"
"v_accvgpr_read_b32 v200, a34\n"
"v_accvgpr_read_b32 v201, a35\n"
"v_mul_f32 v200, v200, v196\n"
"v_mul_f32 v201, v201, v196\n"
"v_accvgpr_write_b32 a34, v200\n"
"v_accvgpr_write_b32 a35, v201\n"
"v_accvgpr_read_b32 v200, a36\n"
"v_accvgpr_read_b32 v201, a37\n"
"v_mul_f32 v200, v200, v197\n"
"v_mul_f32 v201, v201, v197\n"
"v_accvgpr_write_b32 a36, v200\n"
"v_accvgpr_write_b32 a37, v201\n"
"v_accvgpr_read_b32 v200, a38\n"
"v_accvgpr_read_b32 v201, a39\n"
"v_mul_f32 v200, v200, v197\n"
"v_mul_f32 v201, v201, v197\n"
"v_accvgpr_write_b32 a38, v200\n"
"v_accvgpr_write_b32 a39, v201\n"
"v_accvgpr_read_b32 v200, a40\n"
"v_accvgpr_read_b32 v201, a41\n"
"v_mul_f32 v200, v200, v198\n"
"v_mul_f32 v201, v201, v198\n"
"v_accvgpr_write_b32 a40, v200\n"
"v_accvgpr_write_b32 a41, v201\n"
"v_accvgpr_read_b32 v200, a42\n"
"v_accvgpr_read_b32 v201, a43\n"
"v_mul_f32 v200, v200, v198\n"
"v_mul_f32 v201, v201, v198\n"
"v_accvgpr_write_b32 a42, v200\n"
"v_accvgpr_write_b32 a43, v201\n"
"v_accvgpr_read_b32 v200, a44\n"
"v_accvgpr_read_b32 v201, a45\n"
"v_mul_f32 v200, v200, v199\n"
"v_mul_f32 v201, v201, v199\n"
"v_accvgpr_write_b32 a44, v200\n"
"v_accvgpr_write_b32 a45, v201\n"
"v_accvgpr_read_b32 v200, a46\n"
"v_accvgpr_read_b32 v201, a47\n"
"v_mul_f32 v200, v200, v199\n"
"v_mul_f32 v201, v201, v199\n"
"v_accvgpr_write_b32 a46, v200\n"
"v_accvgpr_write_b32 a47, v201\n"
"v_accvgpr_read_b32 v200, a48\n"
"v_accvgpr_read_b32 v201, a49\n"
"v_mul_f32 v200, v200, v196\n"
"v_mul_f32 v201, v201, v196\n"
"v_accvgpr_write_b32 a48, v200\n"
"v_accvgpr_write_b32 a49, v201\n"
"v_accvgpr_read_b32 v200, a50\n"
"v_accvgpr_read_b32 v201, a51\n"
"v_mul_f32 v200, v200, v196\n"
"v_mul_f32 v201, v201, v196\n"
"v_accvgpr_write_b32 a50, v200\n"
"v_accvgpr_write_b32 a51, v201\n"
"v_accvgpr_read_b32 v200, a52\n"
"v_accvgpr_read_b32 v201, a53\n"
"v_mul_f32 v200, v200, v197\n"
"v_mul_f32 v201, v201, v197\n"
"v_accvgpr_write_b32 a52, v200\n"
"v_accvgpr_write_b32 a53, v201\n"
"v_accvgpr_read_b32 v200, a54\n"
"v_accvgpr_read_b32 v201, a55\n"
"v_mul_f32 v200, v200, v197\n"
"v_mul_f32 v201, v201, v197\n"
"v_accvgpr_write_b32 a54, v200\n"
"v_accvgpr_write_b32 a55, v201\n"
"v_accvgpr_read_b32 v200, a56\n"
"v_accvgpr_read_b32 v201, a57\n"
"v_mul_f32 v200, v200, v198\n"
"v_mul_f32 v201, v201, v198\n"
"v_accvgpr_write_b32 a56, v200\n"
"v_accvgpr_write_b32 a57, v201\n"
"v_accvgpr_read_b32 v200, a58\n"
"v_accvgpr_read_b32 v201, a59\n"
"v_mul_f32 v200, v200, v198\n"
"v_mul_f32 v201, v201, v198\n"
"v_accvgpr_write_b32 a58, v200\n"
"v_accvgpr_write_b32 a59, v201\n"
"v_accvgpr_read_b32 v200, a60\n"
"v_accvgpr_read_b32 v201, a61\n"
"v_mul_f32 v200, v200, v199\n"
"v_mul_f32 v201, v201, v199\n"
"v_accvgpr_write_b32 a60, v200\n"
"v_accvgpr_write_b32 a61, v201\n"
"v_accvgpr_read_b32 v200, a62\n"
"v_accvgpr_read_b32 v201, a63\n"
"v_mul_f32 v200, v200, v199\n"
"v_mul_f32 v201, v201, v199\n"
"v_accvgpr_write_b32 a62, v200\n"
"v_accvgpr_write_b32 a63, v201\n"
"v_accvgpr_read_b32 v200, a64\n"
"v_accvgpr_read_b32 v201, a65\n"
"v_mul_f32 v200, v200, v196\n"
"v_mul_f32 v201, v201, v196\n"
"v_accvgpr_write_b32 a64, v200\n"
"v_accvgpr_write_b32 a65, v201\n"
"v_accvgpr_read_b32 v200, a66\n"
"v_accvgpr_read_b32 v201, a67\n"
"v_mul_f32 v200, v200, v196\n"
"v_mul_f32 v201, v201, v196\n"
"v_accvgpr_write_b32 a66, v200\n"
"v_accvgpr_write_b32 a67, v201\n"
"v_accvgpr_read_b32 v200, a68\n"
"v_accvgpr_read_b32 v201, a69\n"
"v_mul_f32 v200, v200, v197\n"
"v_mul_f32 v201, v201, v197\n"
"v_accvgpr_write_b32 a68, v200\n"
"v_accvgpr_write_b32 a69, v201\n"
"v_accvgpr_read_b32 v200, a70\n"
"v_accvgpr_read_b32 v201, a71\n"
"v_mul_f32 v200, v200, v197\n"
"v_mul_f32 v201, v201, v197\n"
"v_accvgpr_write_b32 a70, v200\n"
"v_accvgpr_write_b32 a71, v201\n"
"v_accvgpr_read_b32 v200, a72\n"
"v_accvgpr_read_b32 v201, a73\n"
"v_mul_f32 v200, v200, v198\n"
"v_mul_f32 v201, v201, v198\n"
"v_accvgpr_write_b32 a72, v200\n"
"v_accvgpr_write_b32 a73, v201\n"
"v_accvgpr_read_b32 v200, a74\n"
"v_accvgpr_read_b32 v201, a75\n"
"v_mul_f32 v200, v200, v198\n"
"v_mul_f32 v201, v201, v198\n"
"v_accvgpr_write_b32 a74, v200\n"
"v_accvgpr_write_b32 a75, v201\n"
"v_accvgpr_read_b32 v200, a76\n"
"v_accvgpr_read_b32 v201, a77\n"
"v_mul_f32 v200, v200, v199\n"
"v_mul_f32 v201, v201, v199\n"
"v_accvgpr_write_b32 a76, v200\n"
"v_accvgpr_write_b32 a77, v201\n"
"v_accvgpr_read_b32 v200, a78\n"
"v_accvgpr_read_b32 v201, a79\n"
"v_mul_f32 v200, v200, v199\n"
"v_mul_f32 v201, v201, v199\n"
"v_accvgpr_write_b32 a78, v200\n"
"v_accvgpr_write_b32 a79, v201\n"
"v_accvgpr_read_b32 v200, a80\n"
"v_accvgpr_read_b32 v201, a81\n"
"v_mul_f32 v200, v200, v196\n"
"v_mul_f32 v201, v201, v196\n"
"v_accvgpr_write_b32 a80, v200\n"
"v_accvgpr_write_b32 a81, v201\n"
"v_accvgpr_read_b32 v200, a82\n"
"v_accvgpr_read_b32 v201, a83\n"
"v_mul_f32 v200, v200, v196\n"
"v_mul_f32 v201, v201, v196\n"
"v_accvgpr_write_b32 a82, v200\n"
"v_accvgpr_write_b32 a83, v201\n"
"v_accvgpr_read_b32 v200, a84\n"
"v_accvgpr_read_b32 v201, a85\n"
"v_mul_f32 v200, v200, v197\n"
"v_mul_f32 v201, v201, v197\n"
"v_accvgpr_write_b32 a84, v200\n"
"v_accvgpr_write_b32 a85, v201\n"
"v_accvgpr_read_b32 v200, a86\n"
"v_accvgpr_read_b32 v201, a87\n"
"v_mul_f32 v200, v200, v197\n"
"v_mul_f32 v201, v201, v197\n"
"v_accvgpr_write_b32 a86, v200\n"
"v_accvgpr_write_b32 a87, v201\n"
"v_accvgpr_read_b32 v200, a88\n"
"v_accvgpr_read_b32 v201, a89\n"
"v_mul_f32 v200, v200, v198\n"
"v_mul_f32 v201, v201, v198\n"
"v_accvgpr_write_b32 a88, v200\n"
"v_accvgpr_write_b32 a89, v201\n"
"v_accvgpr_read_b32 v200, a90\n"
"v_accvgpr_read_b32 v201, a91\n"
"v_mul_f32 v200, v200, v198\n"
"v_mul_f32 v201, v201, v198\n"
"v_accvgpr_write_b32 a90, v200\n"
"v_accvgpr_write_b32 a91, v201\n"
"v_accvgpr_read_b32 v200, a92\n"
"v_accvgpr_read_b32 v201, a93\n"
"v_mul_f32 v200, v200, v199\n"
"v_mul_f32 v201, v201, v199\n"
"v_accvgpr_write_b32 a92, v200\n"
"v_accvgpr_write_b32 a93, v201\n"
"v_accvgpr_read_b32 v200, a94\n"
"v_accvgpr_read_b32 v201, a95\n"
"v_mul_f32 v200, v200, v199\n"
"v_mul_f32 v201, v201, v199\n"
"v_accvgpr_write_b32 a94, v200\n"
"v_accvgpr_write_b32 a95, v201\n"
"v_accvgpr_read_b32 v200, a96\n"
"v_accvgpr_read_b32 v201, a97\n"
"v_mul_f32 v200, v200, v196\n"
"v_mul_f32 v201, v201, v196\n"
"v_accvgpr_write_b32 a96, v200\n"
"v_accvgpr_write_b32 a97, v201\n"
"v_accvgpr_read_b32 v200, a98\n"
"v_accvgpr_read_b32 v201, a99\n"
"v_mul_f32 v200, v200, v196\n"
"v_mul_f32 v201, v201, v196\n"
"v_accvgpr_write_b32 a98, v200\n"
"v_accvgpr_write_b32 a99, v201\n"
"v_accvgpr_read_b32 v200, a100\n"
"v_accvgpr_read_b32 v201, a101\n"
"v_mul_f32 v200, v200, v197\n"
"v_mul_f32 v201, v201, v197\n"
"v_accvgpr_write_b32 a100, v200\n"
"v_accvgpr_write_b32 a101, v201\n"
"v_accvgpr_read_b32 v200, a102\n"
"v_accvgpr_read_b32 v201, a103\n"
"v_mul_f32 v200, v200, v197\n"
"v_mul_f32 v201, v201, v197\n"
"v_accvgpr_write_b32 a102, v200\n"
"v_accvgpr_write_b32 a103, v201\n"
"v_accvgpr_read_b32 v200, a104\n"
"v_accvgpr_read_b32 v201, a105\n"
"v_mul_f32 v200, v200, v198\n"
"v_mul_f32 v201, v201, v198\n"
"v_accvgpr_write_b32 a104, v200\n"
"v_accvgpr_write_b32 a105, v201\n"
"v_accvgpr_read_b32 v200, a106\n"
"v_accvgpr_read_b32 v201, a107\n"
"v_mul_f32 v200, v200, v198\n"
"v_mul_f32 v201, v201, v198\n"
"v_accvgpr_write_b32 a106, v200\n"
"v_accvgpr_write_b32 a107, v201\n"
"v_accvgpr_read_b32 v200, a108\n"
"v_accvgpr_read_b32 v201, a109\n"
"v_mul_f32 v200, v200, v199\n"
"v_mul_f32 v201, v201, v199\n"
"v_accvgpr_write_b32 a108, v200\n"
"v_accvgpr_write_b32 a109, v201\n"
"v_accvgpr_read_b32 v200, a110\n"
"v_accvgpr_read_b32 v201, a111\n"
"v_mul_f32 v200, v200, v199\n"
"v_mul_f32 v201, v201, v199\n"
"v_accvgpr_write_b32 a110, v200\n"
"v_accvgpr_write_b32 a111, v201\n"
"v_accvgpr_read_b32 v200, a112\n"
"v_accvgpr_read_b32 v201, a113\n"
"v_mul_f32 v200, v200, v196\n"
"v_mul_f32 v201, v201, v196\n"
"v_accvgpr_write_b32 a112, v200\n"
"v_accvgpr_write_b32 a113, v201\n"
"v_accvgpr_read_b32 v200, a114\n"
"v_accvgpr_read_b32 v201, a115\n"
"v_mul_f32 v200, v200, v196\n"
"v_mul_f32 v201, v201, v196\n"
"v_accvgpr_write_b32 a114, v200\n"
"v_accvgpr_write_b32 a115, v201\n"
"v_accvgpr_read_b32 v200, a116\n"
"v_accvgpr_read_b32 v201, a117\n"
"v_mul_f32 v200, v200, v197\n"
"v_mul_f32 v201, v201, v197\n"
"v_accvgpr_write_b32 a116, v200\n"
"v_accvgpr_write_b32 a117, v201\n"
"v_accvgpr_read_b32 v200, a118\n"
"v_accvgpr_read_b32 v201, a119\n"
"v_mul_f32 v200, v200, v197\n"
"v_mul_f32 v201, v201, v197\n"
"v_accvgpr_write_b32 a118, v200\n"
"v_accvgpr_write_b32 a119, v201\n"
"v_accvgpr_read_b32 v200, a120\n"
"v_accvgpr_read_b32 v201, a121\n"
"v_mul_f32 v200, v200, v198\n"
"v_mul_f32 v201, v201, v198\n"
"v_accvgpr_write_b32 a120, v200\n"
"v_accvgpr_write_b32 a121, v201\n"
"v_accvgpr_read_b32 v200, a122\n"
"v_accvgpr_read_b32 v201, a123\n"
"v_mul_f32 v200, v200, v198\n"
"v_mul_f32 v201, v201, v198\n"
"v_accvgpr_write_b32 a122, v200\n"
"v_accvgpr_write_b32 a123, v201\n"
"v_accvgpr_read_b32 v200, a124\n"
"v_accvgpr_read_b32 v201, a125\n"
"v_mul_f32 v200, v200, v199\n"
"v_mul_f32 v201, v201, v199\n"
"v_accvgpr_write_b32 a124, v200\n"
"v_accvgpr_write_b32 a125, v201\n"
"v_accvgpr_read_b32 v200, a126\n"
"v_accvgpr_read_b32 v201, a127\n"
"v_mul_f32 v200, v200, v199\n"
"v_mul_f32 v201, v201, v199\n"
"v_accvgpr_write_b32 a126, v200\n"
"v_accvgpr_write_b32 a127, v201\n"
"v_accvgpr_read_b32 v200, a128\n"
"v_accvgpr_read_b32 v201, a129\n"
"v_mul_f32 v200, v200, v196\n"
"v_mul_f32 v201, v201, v196\n"
"v_accvgpr_write_b32 a128, v200\n"
"v_accvgpr_write_b32 a129, v201\n"
"v_accvgpr_read_b32 v200, a130\n"
"v_accvgpr_read_b32 v201, a131\n"
"v_mul_f32 v200, v200, v196\n"
"v_mul_f32 v201, v201, v196\n"
"v_accvgpr_write_b32 a130, v200\n"
"v_accvgpr_write_b32 a131, v201\n"
"v_accvgpr_read_b32 v200, a132\n"
"v_accvgpr_read_b32 v201, a133\n"
"v_mul_f32 v200, v200, v197\n"
"v_mul_f32 v201, v201, v197\n"
"v_accvgpr_write_b32 a132, v200\n"
"v_accvgpr_write_b32 a133, v201\n"
"v_accvgpr_read_b32 v200, a134\n"
"v_accvgpr_read_b32 v201, a135\n"
"v_mul_f32 v200, v200, v197\n"
"v_mul_f32 v201, v201, v197\n"
"v_accvgpr_write_b32 a134, v200\n"
"v_accvgpr_write_b32 a135, v201\n"
"v_accvgpr_read_b32 v200, a136\n"
"v_accvgpr_read_b32 v201, a137\n"
"v_mul_f32 v200, v200, v198\n"
"v_mul_f32 v201, v201, v198\n"
"v_accvgpr_write_b32 a136, v200\n"
"v_accvgpr_write_b32 a137, v201\n"
"v_accvgpr_read_b32 v200, a138\n"
"v_accvgpr_read_b32 v201, a139\n"
"v_mul_f32 v200, v200, v198\n"
"v_mul_f32 v201, v201, v198\n"
"v_accvgpr_write_b32 a138, v200\n"
"v_accvgpr_write_b32 a139, v201\n"
"v_accvgpr_read_b32 v200, a140\n"
"v_accvgpr_read_b32 v201, a141\n"
"v_mul_f32 v200, v200, v199\n"
"v_mul_f32 v201, v201, v199\n"
"v_accvgpr_write_b32 a140, v200\n"
"v_accvgpr_write_b32 a141, v201\n"
"v_accvgpr_read_b32 v200, a142\n"
"v_accvgpr_read_b32 v201, a143\n"
"v_mul_f32 v200, v200, v199\n"
"v_mul_f32 v201, v201, v199\n"
"v_accvgpr_write_b32 a142, v200\n"
"v_accvgpr_write_b32 a143, v201\n"
"v_accvgpr_read_b32 v200, a144\n"
"v_accvgpr_read_b32 v201, a145\n"
"v_mul_f32 v200, v200, v196\n"
"v_mul_f32 v201, v201, v196\n"
"v_accvgpr_write_b32 a144, v200\n"
"v_accvgpr_write_b32 a145, v201\n"
"v_accvgpr_read_b32 v200, a146\n"
"v_accvgpr_read_b32 v201, a147\n"
"v_mul_f32 v200, v200, v196\n"
"v_mul_f32 v201, v201, v196\n"
"v_accvgpr_write_b32 a146, v200\n"
"v_accvgpr_write_b32 a147, v201\n"
"v_accvgpr_read_b32 v200, a148\n"
"v_accvgpr_read_b32 v201, a149\n"
"v_mul_f32 v200, v200, v197\n"
"v_mul_f32 v201, v201, v197\n"
"v_accvgpr_write_b32 a148, v200\n"
"v_accvgpr_write_b32 a149, v201\n"
"v_accvgpr_read_b32 v200, a150\n"
"v_accvgpr_read_b32 v201, a151\n"
"v_mul_f32 v200, v200, v197\n"
"v_mul_f32 v201, v201, v197\n"
"v_accvgpr_write_b32 a150, v200\n"
"v_accvgpr_write_b32 a151, v201\n"
"v_accvgpr_read_b32 v200, a152\n"
"v_accvgpr_read_b32 v201, a153\n"
"v_mul_f32 v200, v200, v198\n"
"v_mul_f32 v201, v201, v198\n"
"v_accvgpr_write_b32 a152, v200\n"
"v_accvgpr_write_b32 a153, v201\n"
"v_accvgpr_read_b32 v200, a154\n"
"v_accvgpr_read_b32 v201, a155\n"
"v_mul_f32 v200, v200, v198\n"
"v_mul_f32 v201, v201, v198\n"
"v_accvgpr_write_b32 a154, v200\n"
"v_accvgpr_write_b32 a155, v201\n"
"v_accvgpr_read_b32 v200, a156\n"
"v_accvgpr_read_b32 v201, a157\n"
"v_mul_f32 v200, v200, v199\n"
"v_mul_f32 v201, v201, v199\n"
"v_accvgpr_write_b32 a156, v200\n"
"v_accvgpr_write_b32 a157, v201\n"
"v_accvgpr_read_b32 v200, a158\n"
"v_accvgpr_read_b32 v201, a159\n"
"v_mul_f32 v200, v200, v199\n"
"v_mul_f32 v201, v201, v199\n"
"v_accvgpr_write_b32 a158, v200\n"
"v_accvgpr_write_b32 a159, v201\n"
"v_accvgpr_read_b32 v200, a160\n"
"v_accvgpr_read_b32 v201, a161\n"
"v_mul_f32 v200, v200, v196\n"
"v_mul_f32 v201, v201, v196\n"
"v_accvgpr_write_b32 a160, v200\n"
"v_accvgpr_write_b32 a161, v201\n"
"v_accvgpr_read_b32 v200, a162\n"
"v_accvgpr_read_b32 v201, a163\n"
"v_mul_f32 v200, v200, v196\n"
"v_mul_f32 v201, v201, v196\n"
"v_accvgpr_write_b32 a162, v200\n"
"v_accvgpr_write_b32 a163, v201\n"
"v_accvgpr_read_b32 v200, a164\n"
"v_accvgpr_read_b32 v201, a165\n"
"v_mul_f32 v200, v200, v197\n"
"v_mul_f32 v201, v201, v197\n"
"v_accvgpr_write_b32 a164, v200\n"
"v_accvgpr_write_b32 a165, v201\n"
"v_accvgpr_read_b32 v200, a166\n"
"v_accvgpr_read_b32 v201, a167\n"
"v_mul_f32 v200, v200, v197\n"
"v_mul_f32 v201, v201, v197\n"
"v_accvgpr_write_b32 a166, v200\n"
"v_accvgpr_write_b32 a167, v201\n"
"v_accvgpr_read_b32 v200, a168\n"
"v_accvgpr_read_b32 v201, a169\n"
"v_mul_f32 v200, v200, v198\n"
"v_mul_f32 v201, v201, v198\n"
"v_accvgpr_write_b32 a168, v200\n"
"v_accvgpr_write_b32 a169, v201\n"
"v_accvgpr_read_b32 v200, a170\n"
"v_accvgpr_read_b32 v201, a171\n"
"v_mul_f32 v200, v200, v198\n"
"v_mul_f32 v201, v201, v198\n"
"v_accvgpr_write_b32 a170, v200\n"
"v_accvgpr_write_b32 a171, v201\n"
"v_accvgpr_read_b32 v200, a172\n"
"v_accvgpr_read_b32 v201, a173\n"
"v_mul_f32 v200, v200, v199\n"
"v_mul_f32 v201, v201, v199\n"
"v_accvgpr_write_b32 a172, v200\n"
"v_accvgpr_write_b32 a173, v201\n"
"v_accvgpr_read_b32 v200, a174\n"
"v_accvgpr_read_b32 v201, a175\n"
"v_mul_f32 v200, v200, v199\n"
"v_mul_f32 v201, v201, v199\n"
"v_accvgpr_write_b32 a174, v200\n"
"v_accvgpr_write_b32 a175, v201\n"
"v_accvgpr_read_b32 v200, a176\n"
"v_accvgpr_read_b32 v201, a177\n"
"v_mul_f32 v200, v200, v196\n"
"v_mul_f32 v201, v201, v196\n"
"v_accvgpr_write_b32 a176, v200\n"
"v_accvgpr_write_b32 a177, v201\n"
"v_accvgpr_read_b32 v200, a178\n"
"v_accvgpr_read_b32 v201, a179\n"
"v_mul_f32 v200, v200, v196\n"
"v_mul_f32 v201, v201, v196\n"
"v_accvgpr_write_b32 a178, v200\n"
"v_accvgpr_write_b32 a179, v201\n"
"v_accvgpr_read_b32 v200, a180\n"
"v_accvgpr_read_b32 v201, a181\n"
"v_mul_f32 v200, v200, v197\n"
"v_mul_f32 v201, v201, v197\n"
"v_accvgpr_write_b32 a180, v200\n"
"v_accvgpr_write_b32 a181, v201\n"
"v_accvgpr_read_b32 v200, a182\n"
"v_accvgpr_read_b32 v201, a183\n"
"v_mul_f32 v200, v200, v197\n"
"v_mul_f32 v201, v201, v197\n"
"v_accvgpr_write_b32 a182, v200\n"
"v_accvgpr_write_b32 a183, v201\n"
"v_accvgpr_read_b32 v200, a184\n"
"v_accvgpr_read_b32 v201, a185\n"
"v_mul_f32 v200, v200, v198\n"
"v_mul_f32 v201, v201, v198\n"
"v_accvgpr_write_b32 a184, v200\n"
"v_accvgpr_write_b32 a185, v201\n"
"v_accvgpr_read_b32 v200, a186\n"
"v_accvgpr_read_b32 v201, a187\n"
"v_mul_f32 v200, v200, v198\n"
"v_mul_f32 v201, v201, v198\n"
"v_accvgpr_write_b32 a186, v200\n"
"v_accvgpr_write_b32 a187, v201\n"
"v_accvgpr_read_b32 v200, a188\n"
"v_accvgpr_read_b32 v201, a189\n"
"v_mul_f32 v200, v200, v199\n"
"v_mul_f32 v201, v201, v199\n"
"v_accvgpr_write_b32 a188, v200\n"
"v_accvgpr_write_b32 a189, v201\n"
"v_accvgpr_read_b32 v200, a190\n"
"v_accvgpr_read_b32 v201, a191\n"
"v_mul_f32 v200, v200, v199\n"
"v_mul_f32 v201, v201, v199\n"
"v_accvgpr_write_b32 a190, v200\n"
"v_accvgpr_write_b32 a191, v201\n"
"v_accvgpr_read_b32 v200, a192\n"
"v_accvgpr_read_b32 v201, a193\n"
"v_mul_f32 v200, v200, v196\n"
"v_mul_f32 v201, v201, v196\n"
"v_accvgpr_write_b32 a192, v200\n"
"v_accvgpr_write_b32 a193, v201\n"
"v_accvgpr_read_b32 v200, a194\n"
"v_accvgpr_read_b32 v201, a195\n"
"v_mul_f32 v200, v200, v196\n"
"v_mul_f32 v201, v201, v196\n"
"v_accvgpr_write_b32 a194, v200\n"
"v_accvgpr_write_b32 a195, v201\n"
"v_accvgpr_read_b32 v200, a196\n"
"v_accvgpr_read_b32 v201, a197\n"
"v_mul_f32 v200, v200, v197\n"
"v_mul_f32 v201, v201, v197\n"
"v_accvgpr_write_b32 a196, v200\n"
"v_accvgpr_write_b32 a197, v201\n"
"v_accvgpr_read_b32 v200, a198\n"
"v_accvgpr_read_b32 v201, a199\n"
"v_mul_f32 v200, v200, v197\n"
"v_mul_f32 v201, v201, v197\n"
"v_accvgpr_write_b32 a198, v200\n"
"v_accvgpr_write_b32 a199, v201\n"
"v_accvgpr_read_b32 v200, a200\n"
"v_accvgpr_read_b32 v201, a201\n"
"v_mul_f32 v200, v200, v198\n"
"v_mul_f32 v201, v201, v198\n"
"v_accvgpr_write_b32 a200, v200\n"
"v_accvgpr_write_b32 a201, v201\n"
"v_accvgpr_read_b32 v200, a202\n"
"v_accvgpr_read_b32 v201, a203\n"
"v_mul_f32 v200, v200, v198\n"
"v_mul_f32 v201, v201, v198\n"
"v_accvgpr_write_b32 a202, v200\n"
"v_accvgpr_write_b32 a203, v201\n"
"v_accvgpr_read_b32 v200, a204\n"
"v_accvgpr_read_b32 v201, a205\n"
"v_mul_f32 v200, v200, v199\n"
"v_mul_f32 v201, v201, v199\n"
"v_accvgpr_write_b32 a204, v200\n"
"v_accvgpr_write_b32 a205, v201\n"
"v_accvgpr_read_b32 v200, a206\n"
"v_accvgpr_read_b32 v201, a207\n"
"v_mul_f32 v200, v200, v199\n"
"v_mul_f32 v201, v201, v199\n"
"v_accvgpr_write_b32 a206, v200\n"
"v_accvgpr_write_b32 a207, v201\n"
"v_accvgpr_read_b32 v200, a208\n"
"v_accvgpr_read_b32 v201, a209\n"
"v_mul_f32 v200, v200, v196\n"
"v_mul_f32 v201, v201, v196\n"
"v_accvgpr_write_b32 a208, v200\n"
"v_accvgpr_write_b32 a209, v201\n"
"v_accvgpr_read_b32 v200, a210\n"
"v_accvgpr_read_b32 v201, a211\n"
"v_mul_f32 v200, v200, v196\n"
"v_mul_f32 v201, v201, v196\n"
"v_accvgpr_write_b32 a210, v200\n"
"v_accvgpr_write_b32 a211, v201\n"
"v_accvgpr_read_b32 v200, a212\n"
"v_accvgpr_read_b32 v201, a213\n"
"v_mul_f32 v200, v200, v197\n"
"v_mul_f32 v201, v201, v197\n"
"v_accvgpr_write_b32 a212, v200\n"
"v_accvgpr_write_b32 a213, v201\n"
"v_accvgpr_read_b32 v200, a214\n"
"v_accvgpr_read_b32 v201, a215\n"
"v_mul_f32 v200, v200, v197\n"
"v_mul_f32 v201, v201, v197\n"
"v_accvgpr_write_b32 a214, v200\n"
"v_accvgpr_write_b32 a215, v201\n"
"v_accvgpr_read_b32 v200, a216\n"
"v_accvgpr_read_b32 v201, a217\n"
"v_mul_f32 v200, v200, v198\n"
"v_mul_f32 v201, v201, v198\n"
"v_accvgpr_write_b32 a216, v200\n"
"v_accvgpr_write_b32 a217, v201\n"
"v_accvgpr_read_b32 v200, a218\n"
"v_accvgpr_read_b32 v201, a219\n"
"v_mul_f32 v200, v200, v198\n"
"v_mul_f32 v201, v201, v198\n"
"v_accvgpr_write_b32 a218, v200\n"
"v_accvgpr_write_b32 a219, v201\n"
"v_accvgpr_read_b32 v200, a220\n"
"v_accvgpr_read_b32 v201, a221\n"
"v_mul_f32 v200, v200, v199\n"
"v_mul_f32 v201, v201, v199\n"
"v_accvgpr_write_b32 a220, v200\n"
"v_accvgpr_write_b32 a221, v201\n"
"v_accvgpr_read_b32 v200, a222\n"
"v_accvgpr_read_b32 v201, a223\n"
"v_mul_f32 v200, v200, v199\n"
"v_mul_f32 v201, v201, v199\n"
"v_accvgpr_write_b32 a222, v200\n"
"v_accvgpr_write_b32 a223, v201\n"
"v_accvgpr_read_b32 v200, a224\n"
"v_accvgpr_read_b32 v201, a225\n"
"v_mul_f32 v200, v200, v196\n"
"v_mul_f32 v201, v201, v196\n"
"v_accvgpr_write_b32 a224, v200\n"
"v_accvgpr_write_b32 a225, v201\n"
"v_accvgpr_read_b32 v200, a226\n"
"v_accvgpr_read_b32 v201, a227\n"
"v_mul_f32 v200, v200, v196\n"
"v_mul_f32 v201, v201, v196\n"
"v_accvgpr_write_b32 a226, v200\n"
"v_accvgpr_write_b32 a227, v201\n"
"v_accvgpr_read_b32 v200, a228\n"
"v_accvgpr_read_b32 v201, a229\n"
"v_mul_f32 v200, v200, v197\n"
"v_mul_f32 v201, v201, v197\n"
"v_accvgpr_write_b32 a228, v200\n"
"v_accvgpr_write_b32 a229, v201\n"
"v_accvgpr_read_b32 v200, a230\n"
"v_accvgpr_read_b32 v201, a231\n"
"v_mul_f32 v200, v200, v197\n"
"v_mul_f32 v201, v201, v197\n"
"v_accvgpr_write_b32 a230, v200\n"
"v_accvgpr_write_b32 a231, v201\n"
"v_accvgpr_read_b32 v200, a232\n"
"v_accvgpr_read_b32 v201, a233\n"
"v_mul_f32 v200, v200, v198\n"
"v_mul_f32 v201, v201, v198\n"
"v_accvgpr_write_b32 a232, v200\n"
"v_accvgpr_write_b32 a233, v201\n"
"v_accvgpr_read_b32 v200, a234\n"
"v_accvgpr_read_b32 v201, a235\n"
"v_mul_f32 v200, v200, v198\n"
"v_mul_f32 v201, v201, v198\n"
"v_accvgpr_write_b32 a234, v200\n"
"v_accvgpr_write_b32 a235, v201\n"
"v_accvgpr_read_b32 v200, a236\n"
"v_accvgpr_read_b32 v201, a237\n"
"v_mul_f32 v200, v200, v199\n"
"v_mul_f32 v201, v201, v199\n"
"v_accvgpr_write_b32 a236, v200\n"
"v_accvgpr_write_b32 a237, v201\n"
"v_accvgpr_read_b32 v200, a238\n"
"v_accvgpr_read_b32 v201, a239\n"
"v_mul_f32 v200, v200, v199\n"
"v_mul_f32 v201, v201, v199\n"
"v_accvgpr_write_b32 a238, v200\n"
"v_accvgpr_write_b32 a239, v201\n"
"v_accvgpr_read_b32 v200, a240\n"
"v_accvgpr_read_b32 v201, a241\n"
"v_mul_f32 v200, v200, v196\n"
"v_mul_f32 v201, v201, v196\n"
"v_accvgpr_write_b32 a240, v200\n"
"v_accvgpr_write_b32 a241, v201\n"
"v_accvgpr_read_b32 v200, a242\n"
"v_accvgpr_read_b32 v201, a243\n"
"v_mul_f32 v200, v200, v196\n"
"v_mul_f32 v201, v201, v196\n"
"v_accvgpr_write_b32 a242, v200\n"
"v_accvgpr_write_b32 a243, v201\n"
"v_accvgpr_read_b32 v200, a244\n"
"v_accvgpr_read_b32 v201, a245\n"
"v_mul_f32 v200, v200, v197\n"
"v_mul_f32 v201, v201, v197\n"
"v_accvgpr_write_b32 a244, v200\n"
"v_accvgpr_write_b32 a245, v201\n"
"v_accvgpr_read_b32 v200, a246\n"
"v_accvgpr_read_b32 v201, a247\n"
"v_mul_f32 v200, v200, v197\n"
"v_mul_f32 v201, v201, v197\n"
"v_accvgpr_write_b32 a246, v200\n"
"v_accvgpr_write_b32 a247, v201\n"
"v_accvgpr_read_b32 v200, a248\n"
"v_accvgpr_read_b32 v201, a249\n"
"v_mul_f32 v200, v200, v198\n"
"v_mul_f32 v201, v201, v198\n"
"v_accvgpr_write_b32 a248, v200\n"
"v_accvgpr_write_b32 a249, v201\n"
"v_accvgpr_read_b32 v200, a250\n"
"v_accvgpr_read_b32 v201, a251\n"
"v_mul_f32 v200, v200, v198\n"
"v_mul_f32 v201, v201, v198\n"
"v_accvgpr_write_b32 a250, v200\n"
"v_accvgpr_write_b32 a251, v201\n"
"v_accvgpr_read_b32 v200, a252\n"
"v_accvgpr_read_b32 v201, a253\n"
"v_mul_f32 v200, v200, v199\n"
"v_mul_f32 v201, v201, v199\n"
"v_accvgpr_write_b32 a252, v200\n"
"v_accvgpr_write_b32 a253, v201\n"
"v_accvgpr_read_b32 v200, a254\n"
"v_accvgpr_read_b32 v201, a255\n"
"v_mul_f32 v200, v200, v199\n"
"v_mul_f32 v201, v201, v199\n"
"v_accvgpr_write_b32 a254, v200\n"
"v_accvgpr_write_b32 a255, v201\n"
"s_add_u32 s90, s90, 4\n"
"s_addc_u32 s91, s91, 0\n"
"s_setpc_b64 s[90:91]\n"
"LEND_%=:\n"
      :
      : "s"(__builtin_amdgcn_kernarg_segment_ptr()), "s"(blockIdx.x), "v"(threadIdx.x)
      : "v1","v2","v3","v4","v5","v6","v7","v8","v9","v10","v11","v12","v13","v14","v15","v16","v17","v18","v19","v20","v21","v22","v23","v24","v25","v26","v27","v28","v29","v30","v31","v32","v33","v34","v35","v36","v37","v38","v39","v40","v41","v42","v43","v44","v45","v46","v47","v48","v49","v50","v51","v52","v53","v54","v55","v56","v57","v58","v59","v60","v61","v62","v63","v64","v65","v66","v67","v68","v69","v70","v71","v72","v73","v74","v75","v76","v77","v78","v79","v80","v81","v82","v83","v84","v85","v86","v87","v88","v89","v90","v91","v92","v93","v94","v95","v96","v97","v98","v99","v100","v101","v102","v103","v104","v105","v106","v107","v108","v109","v110","v111","v112","v113","v114","v115","v116","v117","v118","v119","v120","v121","v122","v123","v124","v125","v126","v127","v128","v129","v130","v131","v132","v133","v134","v135","v136","v137","v138","v139","v140","v141","v142","v143","v144","v145","v146","v147","v148","v149","v150","v151","v152","v153","v154","v155","v156","v157","v158","v159","v160","v161","v162","v163","v164","v165","v166","v167","v168","v169","v170","v171","v172","v173","v174","v175","v176","v177","v178","v179","v180","v181","v182","v183","v184","v185","v186","v187","v188","v189","v190","v191","v192","v193","v194","v195","v196","v197","v198","v199","v200","v201","v202","v203","v204","v205","v206","v207","v208","v209","v210","v211","v212","v213","v214","v215","v216","v217","v218","v219","v220","v221","v222","v223","v224","v225","v226","v227","v228","v229","v230","v231","v232","v233","v234","v235","v236","v237","v238","v239","v240","v241","v242","v243","v244","v245","v246","v247","v248","v249","v250","v251","v252","v253","v254","v255","a0","a1","a2","a3","a4","a5","a6","a7","a8","a9","a10","a11","a12","a13","a14","a15","a16","a17","a18","a19","a20","a21","a22","a23","a24","a25","a26","a27","a28","a29","a30","a31","a32","a33","a34","a35","a36","a37","a38","a39","a40","a41","a42","a43","a44","a45","a46","a47","a48","a49","a50","a51","a52","a53","a54","a55","a56","a57","a58","a59","a60","a61","a62","a63","a64","a65","a66","a67","a68","a69","a70","a71","a72","a73","a74","a75","a76","a77","a78","a79","a80","a81","a82","a83","a84","a85","a86","a87","a88","a89","a90","a91","a92","a93","a94","a95","a96","a97","a98","a99","a100","a101","a102","a103","a104","a105","a106","a107","a108","a109","a110","a111","a112","a113","a114","a115","a116","a117","a118","a119","a120","a121","a122","a123","a124","a125","a126","a127","a128","a129","a130","a131","a132","a133","a134","a135","a136","a137","a138","a139","a140","a141","a142","a143","a144","a145","a146","a147","a148","a149","a150","a151","a152","a153","a154","a155","a156","a157","a158","a159","a160","a161","a162","a163","a164","a165","a166","a167","a168","a169","a170","a171","a172","a173","a174","a175","a176","a177","a178","a179","a180","a181","a182","a183","a184","a185","a186","a187","a188","a189","a190","a191","a192","a193","a194","a195","a196","a197","a198","a199","a200","a201","a202","a203","a204","a205","a206","a207","a208","a209","a210","a211","a212","a213","a214","a215","a216","a217","a218","a219","a220","a221","a222","a223","a224","a225","a226","a227","a228","a229","a230","a231","a232","a233","a234","a235","a236","a237","a238","a239","a240","a241","a242","a243","a244","a245","a246","a247","a248","a249","a250","a251","a252","a253","a254","a255","s8","s9","s10","s11","s12","s13","s14","s15","s16","s17","s18","s19","s20","s21","s22","s23","s24","s25","s26","s27","s28","s29","s30","s31","s32","s33","s34","s35","s36","s37","s38","s39","s40","s41","s42","s43","s44","s45","s46","s47","s48","s49","s50","s51","s52","s53","s54","s55","s56","s57","s58","s59","s60","s61","s62","s63","s64","s65","s66","s67","s68","s69","s70","s71","s72","s73","s74","s75","s76","s77","s78","s79","s80","s81","s82","s83","s84","s85","s86","s87","s88","s89","s90","s91","s92","s93","s94","s95","s96","s97","s98","s99","vcc","scc","memory");
}

extern "C" void kernel_launch(void* const* d_in, const int* in_sizes, int n_in, void* d_out, int out_size, void* d_ws, size_t ws_size,
                              hipStream_t stream) {
  (void)in_sizes; (void)n_in; (void)out_size; (void)ws_size;
  (void)hipFuncSetAttribute((const void*)mha_block_fused, hipFuncAttributeMaxDynamicSharedMemorySize, 163840);
  mha_block_fused<<<256, 256, 163840, stream>>>(d_in[0], d_in[1], d_in[2], d_in[3], d_in[4], d_in[5], d_in[6], d_in[7], d_in[8], d_in[9],
                                                d_out, d_ws);
}
```
